```python
import torch

def _softmax(x, axis=-1):
    return torch.softmax(x, dim=axis)
def _gelu(x):
    return torch.nn.functional.gelu(x, approximate="tanh")
def _silu(x):
    return torch.nn.functional.silu(x)
def _sigmoid(x):
    return torch.sigmoid(x)
def _layernorm(x, axis=-1, eps=1e-5):
    m = x.mean(dim=axis, keepdim=True)
    v = x.var(dim=axis, keepdim=True, unbiased=False)
    return (x - m) / torch.sqrt(v + eps)
def _scatter_add(tgt, idx, src, axis=0):
    return tgt.clone().index_add_(axis, idx, src)
def _segment_sum(src, seg, n):
    out = torch.zeros((n,) + src.shape[1:], dtype=src.dtype)
    return out.index_add_(0, seg, src)
def _pad(x, pw):
    flat = tuple(v for pair in reversed(pw) for v in pair)
    return torch.nn.functional.pad(x, flat)

kernel_name = 'interleave_layers__sliding_local_dilated__sliding_local_dilated__prompt'

def setup_inputs():

    _w = (lambda a: (lambda t: t.to(torch.bfloat16) if t.is_floating_point() else t)(torch.as_tensor(a)))
    def _A_setup():
        B, S, D = 1, 2048, 512
        return {
            "x": _w((lambda *s: torch.randn(*s, dtype=torch.bfloat16) * 0.05)(B, S, D)),
            "wqkv": _w((lambda *s: torch.randn(*s, dtype=torch.bfloat16) * 0.05)(D, 3 * D)),
            "wo": _w((lambda *s: torch.randn(*s, dtype=torch.bfloat16) * 0.05)(D, D)),
        }
    _A_in = _A_setup()

    def _B_setup():
        B, S, D = 1, 2048, 512
        return {
            "x": _w((lambda *s: torch.randn(*s, dtype=torch.bfloat16) * 0.05)(B, S, D)),
            "wqkv": _w((lambda *s: torch.randn(*s, dtype=torch.bfloat16) * 0.05)(D, 3 * D)),
            "wo": _w((lambda *s: torch.randn(*s, dtype=torch.bfloat16) * 0.05)(D, D)),
        }
    _B_in = _B_setup()

    return {
        "A_x": _A_in["x"],
        "A_wqkv": _A_in["wqkv"],
        "A_wo": _A_in["wo"],
        "B_x": _B_in["x"],
        "B_wqkv": _B_in["wqkv"],
        "B_wo": _B_in["wo"],
    }

def reference(A_x, A_wqkv, A_wo, B_x, B_wqkv, B_wo):
    def _A_ref(x, wqkv, wo):
        B, S, D = x.shape
        H = 32; Dh = D // H
        W, dil = 8, 2
        qkv = (lambda x, *d: x.permute(*d))((x @ wqkv).reshape(B, S, 3, H, Dh), 2, 0, 3, 1, 4)
        q, k, v = qkv[0], qkv[1], qkv[2]
        scale = Dh ** -0.5
        pos = torch.arange(S)
        off = torch.arange(W) * dil
        idx = pos[:, None] - off[None, :]
        mask = idx >= 0
        idx_c = torch.where(mask, idx, 0).reshape(-1)
        kg = k[:, :, idx_c, :].reshape(B, H, S, W, Dh)
        vg = v[:, :, idx_c, :].reshape(B, H, S, W, Dh)
        logits = torch.einsum("bhsd,bhswd->bhsw", q, kg) * scale
        logits = torch.where(mask[None, None, :, :], logits, -1e9)
        attn = _softmax(logits, axis=-1)
        o = torch.einsum("bhsw,bhswd->bhsd", attn, vg)
        return (lambda x, *d: x.permute(*d))(o, 0, 2, 1, 3).reshape(B, S, D) @ wo

    def _B_ref(x, wqkv, wo):
        B, S, D = x.shape
        H = 32; Dh = D // H
        W, dil = 8, 2
        qkv = (lambda x, *d: x.permute(*d))((x @ wqkv).reshape(B, S, 3, H, Dh), 2, 0, 3, 1, 4)
        q, k, v = qkv[0], qkv[1], qkv[2]
        scale = Dh ** -0.5
        pos = torch.arange(S)
        off = torch.arange(W) * dil
        idx = pos[:, None] - off[None, :]
        mask = idx >= 0
        idx_c = torch.where(mask, idx, 0).reshape(-1)
        kg = k[:, :, idx_c, :].reshape(B, H, S, W, Dh)
        vg = v[:, :, idx_c, :].reshape(B, H, S, W, Dh)
        logits = torch.einsum("bhsd,bhswd->bhsw", q, kg) * scale
        logits = torch.where(mask[None, None, :, :], logits, -1e9)
        attn = _softmax(logits, axis=-1)
        o = torch.einsum("bhsw,bhswd->bhsd", attn, vg)
        return (lambda x, *d: x.permute(*d))(o, 0, 2, 1, 3).reshape(B, S, D) @ wo

    x = A_x
    x = x + _A_ref(_layernorm(x, axis=-1), A_wqkv, A_wo)
    x = x + _B_ref(_layernorm(x, axis=-1), B_wqkv, B_wo)
    x = x + _A_ref(_layernorm(x, axis=-1), A_wqkv, A_wo)
    x = x + _B_ref(_layernorm(x, axis=-1), B_wqkv, B_wo)
    return x
```

```cpp
#include <hip/hip_runtime.h>
#include <hip/hip_bf16.h>
typedef unsigned short u16;
__device__ __forceinline__ float bf2f(u16 b){ return __uint_as_float(((unsigned)b)<<16); }
__device__ __forceinline__ u16 f2bf(float f){ __hip_bfloat16 h = __float2bfloat16(f); return *(u16*)&h; }
constexpr int S=2048, D=512, H=32, DH=16;
__global__ void k_qkv(const u16* x, const u16* w, u16* qkv){
  __shared__ float xs[D]; __shared__ float red[8];
  int r=blockIdx.x, t=threadIdx.x;
  float a=bf2f(x[r*D+t]), b=bf2f(x[r*D+256+t]);
  float s=a+b; for(int m=32;m>=1;m>>=1) s+=__shfl_xor(s,m);
  if((t&63)==0) red[t>>6]=s; __syncthreads();
  float mean=(red[0]+red[1]+red[2]+red[3])/D; __syncthreads();
  float da=a-mean, db=b-mean; float q=da*da+db*db; for(int m=32;m>=1;m>>=1) q+=__shfl_xor(q,m);
  if((t&63)==0) red[t>>6]=q; __syncthreads();
  float var=(red[0]+red[1]+red[2]+red[3])/D; float rs=rsqrtf(var+1e-5f);
  xs[t]=da*rs; xs[256+t]=db*rs; __syncthreads();
  for(int n=t;n<3*D;n+=256){ float acc=0; for(int k=0;k<D;k++) acc+=xs[k]*bf2f(w[k*3*D+n]); qkv[r*3*D+n]=f2bf(acc);}
}
__global__ void k_attn(const u16* qkv, u16* o){
  int r=blockIdx.x, h=threadIdx.x;
  float q[DH]; for(int d=0;d<DH;d++) q[d]=bf2f(qkv[r*3*D+h*DH+d]);
  float l[8]; float mx=-1e30f;
  for(int w=0;w<8;w++){ int j=r-2*w; if(j>=0){ float s=0; for(int d=0;d<DH;d++) s+=q[d]*bf2f(qkv[j*3*D+D+h*DH+d]); l[w]=s*0.25f;} else l[w]=-1e9f; mx=fmaxf(mx,l[w]); }
  float sum=0; for(int w=0;w<8;w++){ l[w]=__expf(l[w]-mx); sum+=l[w]; }
  float acc[DH]; for(int d=0;d<DH;d++) acc[d]=0;
  for(int w=0;w<8;w++){ int j=r-2*w; if(j<0) j=0; float p=l[w]/sum; for(int d=0;d<DH;d++) acc[d]+=p*bf2f(qkv[j*3*D+2*D+h*DH+d]); }
  for(int d=0;d<DH;d++) o[r*D+h*DH+d]=f2bf(acc[d]);
}
__global__ void k_out(const u16* o, const u16* wo, const u16* xin, u16* xout){
  __shared__ float os[D]; int r=blockIdx.x,t=threadIdx.x;
  os[t]=bf2f(o[r*D+t]); os[256+t]=bf2f(o[r*D+256+t]); __syncthreads();
  for(int n=t;n<D;n+=256){ float acc=0; for(int k=0;k<D;k++) acc+=os[k]*bf2f(wo[k*D+n]); xout[r*D+n]=f2bf(bf2f(xin[r*D+n])+acc);}
}
extern "C" void kernel_launch(void* const* d_in, const int* in_sizes, int n_in, void* d_out, int out_size, void* d_ws, size_t ws_size, hipStream_t stream){
  const u16* x=(const u16*)d_in[0]; const u16* wq[2]={(const u16*)d_in[1],(const u16*)d_in[4]}; const u16* wo[2]={(const u16*)d_in[2],(const u16*)d_in[5]};
  u16* qkv=(u16*)d_ws; u16* o=qkv+S*3*D; u16* xa=o+S*D; u16* xb=xa+S*D;
  const u16* cur=x;
  for(int l=0;l<4;l++){
    u16* nxt = l==3 ? (u16*)d_out : (l&1? xb: xa);
    k_qkv<<<S,256,0,stream>>>(cur,wq[l&1],qkv);
    k_attn<<<S,32,0,stream>>>(qkv,o);
    k_out<<<S,256,0,stream>>>(o,wo[l&1],cur,nxt);
    cur=nxt;
  }
}
```

```cpp
#include <hip/hip_runtime.h>
constexpr unsigned WS_STAMP = 41943040u;

__global__ void __launch_bounds__(512, 2) __attribute__((aligned(4096)))
swa4_kernel(const void* x, const void* wA, const void* woA, const void* wB, const void* woB, void* out, void* ws) {
  __shared__ __attribute__((aligned(1024))) char smem[162048];
  asm volatile(
"s_load_dwordx8 s[80:87], %0, 0\n"
"s_load_dwordx4 s[88:91], %0, 32\n"
"s_load_dwordx2 s[92:93], %0, 48\n"
"v_readfirstlane_b32 s34, %2\n"
"v_and_b32 v1, 63, %2\n"
"s_mov_b32 s35, %1\n"
"s_lshr_b32 s34, s34, 6\n"
"s_and_b32 s28, s34, 3\n"
"s_lshr_b32 s29, s34, 2\n"
"s_and_b32 s68, s35, 7\n"
"s_lshl_b32 s68, s68, 2\n"
"s_lshr_b32 s69, s35, 6\n"
"s_add_u32 s30, s68, s69\n"
"s_bfe_u32 s31, s35, 0x30003\n"
"v_lshlrev_b32 v0, 4, v1\n"
"v_and_b32 v8, 7, v1\n"
"v_lshrrev_b32 v10, 3, v1\n"
"v_bfe_u32 v11, v1, 4, 1\n"
"v_lshrrev_b32 v12, 1, v8\n"
"v_xor_b32 v11, v11, v12\n"
"v_lshlrev_b32 v11, 5, v11\n"
"v_and_b32 v12, 1, v8\n"
"v_lshl_add_u32 v11, v12, 4, v11\n"
"v_mul_u32_u24 v10, 0xc00, v10\n"
"v_add_u32 v13, v10, v11\n"
"v_xor_b32 v11, 64, v11\n"
"v_add_u32 v14, v10, v11\n"
"s_lshl_b32 s50, s31, 7\n"
"s_mul_i32 s51, s28, 49152\n"
"s_add_u32 s51, s51, s50\n"
"s_lshl_b32 s52, s28, 11\n"
"v_lshlrev_b32 v9, 7, v1\n"
"s_and_b32 s53, s35, 7\n"
"s_lshl_b32 s53, s53, 8\n"
"s_and_b32 s54, s35, -8\n"
"s_add_u32 s53, s53, s54\n"
"s_add_u32 s53, s53, s34\n"
"s_mul_i32 s53, s53, 768\n"
"s_add_u32 s54, s54, s34\n"
"s_mul_i32 s54, s54, 6144\n"
"v_lshrrev_b32 v15, 3, v1\n"
"v_lshlrev_b32 v15, 10, v15\n"
"v_lshl_add_u32 v15, v8, 4, v15\n"
"s_waitcnt lgkmcnt(0)\n"
"s_mov_b32 s20, s82\n"
"s_and_b32 s21, s83, 0xffff\n"
"s_mov_b32 s22, 0x180000\n"
"s_mov_b32 s23, 0x20000\n"
"s_mov_b64 exec, 0x3f\n"
"buffer_load_dword v5, v9, s[20:23], s53 offen\n"
"s_sub_u32 s55, 384, s82\n"
"s_and_b32 s55, s55, 0x380\n"
"s_add_u32 s54, s54, s55\n"
"s_mov_b32 exec_lo, -1\n"
"s_mov_b32 exec_hi, 0xffff\n"
"buffer_load_dwordx4 v[24:27], v15, s[20:23], s54 offen\n"
"s_mov_b64 exec, -1\n"
"s_cmp_eq_u32 s29, 1\n"
"s_cbranch_scc1 L_SETY\n"
"s_nop 4\n"
"s_add_u32 s68, s51, 0\n"
"s_add_u32 m0, s52, 65536\n"
"s_nop 0\n"
"buffer_load_dwordx4 v13, s[20:23], s68 offen lds\n"
"s_add_u32 s68, s51, 1024\n"
"s_add_u32 m0, s52, 73728\n"
"s_nop 0\n"
"buffer_load_dwordx4 v13, s[20:23], s68 offen lds\n"
"s_add_u32 s68, s51, 2048\n"
"s_add_u32 m0, s52, 81920\n"
"s_nop 0\n"
"buffer_load_dwordx4 v13, s[20:23], s68 offen lds\n"
"s_add_u32 s68, s51, 24576\n"
"s_add_u32 m0, s52, 66560\n"
"s_nop 0\n"
"buffer_load_dwordx4 v14, s[20:23], s68 offen lds\n"
"s_add_u32 s68, s51, 25600\n"
"s_add_u32 m0, s52, 74752\n"
"s_nop 0\n"
"buffer_load_dwordx4 v14, s[20:23], s68 offen lds\n"
"s_add_u32 s68, s51, 26624\n"
"s_add_u32 m0, s52, 82944\n"
"s_nop 0\n"
"buffer_load_dwordx4 v14, s[20:23], s68 offen lds\n"
"s_lshr_b32 s53, s30, 1\n"
"s_lshl_b32 s54, s53, 2\n"
"s_add_u32 s54, s54, s28\n"
"s_lshl_b32 s55, s28, 10\n"
"s_add_u32 m0, s55, 139264\n"
"s_and_b32 s68, s28, 1\n"
"s_lshl_b32 s68, s68, 6\n"
"v_xor_b32 v10, s68, v13\n"
"s_lshl_b32 s69, s31, 2\n"
"s_add_u32 s69, s69, s28\n"
"s_mov_b32 s24, s86\n"
"s_and_b32 s25, s87, 0xffff\n"
"s_mov_b32 s26, 0x180000\n"
"s_mov_b32 s27, 0x20000\n"
"s_mul_i32 s70, s54, 24576\n"
"s_add_u32 s70, s70, s50\n"
"s_and_b32 s54, s30, 1\n"
"s_lshl_b32 s71, s54, 11\n"
"s_add_u32 s68, s70, s71\n"
"buffer_load_dwordx4 v10, s[24:27], s68 offen lds\n"
"s_add_u32 m0, m0, 3072\n"
"s_nop 0\n"
"buffer_load_dwordx4 v10, s[24:27], s70 offen offset:1024 lds\n"
"s_lshl_b32 s68, s28, 2\n"
"s_xor_b32 s68, s68, s53\n"
"s_mul_i32 s68, s68, 3072\n"
"s_mul_i32 s25, s69, 49152\n"
"s_add_u32 s25, s25, s68\n"
"s_add_u32 s25, s25, 0x840000\n"
"s_add_u32 s26, s25, 1024\n"
"s_add_u32 s25, s25, s71\n"
"s_lshr_b32 s68, s28, 1\n"
"s_cmp_eq_u32 s68, s54\n"
"s_cselect_b32 s26, s26, 0x80000000\n"
"s_mov_b32 s4, s88\n"
"s_and_b32 s5, s89, 0xffff\n"
"s_mov_b32 s6, 0x80000\n"
"s_mov_b32 s7, 0x20000\n"
"s_mov_b32 s12, s92\n"
"s_and_b32 s13, s93, 0xffff\n"
"s_mov_b32 s14, 0xf00000\n"
"s_mov_b32 s15, 0x20000\n"
"s_cmp_eq_u32 s28, 0\n"
"s_cselect_b32 s68, 0, 0x80000000\n"
"v_mov_b32 v8, s68\n"
"buffer_load_dwordx2 v[6:7], v8, s[12:15], 0 offen sc1\n"
"s_add_u32 s68, s51, 196608\n"
"s_add_u32 m0, s52, 90112\n"
"s_nop 0\n"
"buffer_load_dwordx4 v13, s[20:23], s68 offen lds\n"
"s_add_u32 s68, s51, 197632\n"
"s_add_u32 m0, s52, 98304\n"
"s_nop 0\n"
"buffer_load_dwordx4 v13, s[20:23], s68 offen lds\n"
"s_add_u32 s68, s51, 198656\n"
"s_add_u32 m0, s52, 106496\n"
"s_nop 0\n"
"buffer_load_dwordx4 v13, s[20:23], s68 offen lds\n"
"s_add_u32 s68, s51, 221184\n"
"s_add_u32 m0, s52, 91136\n"
"s_nop 0\n"
"buffer_load_dwordx4 v14, s[20:23], s68 offen lds\n"
"s_add_u32 s68, s51, 222208\n"
"s_add_u32 m0, s52, 99328\n"
"s_nop 0\n"
"buffer_load_dwordx4 v14, s[20:23], s68 offen lds\n"
"s_add_u32 s68, s51, 223232\n"
"s_add_u32 m0, s52, 107520\n"
"s_nop 0\n"
"buffer_load_dwordx4 v14, s[20:23], s68 offen lds\n"
"s_add_u32 s68, s51, 393216\n"
"s_add_u32 m0, s52, 114688\n"
"s_nop 0\n"
"buffer_load_dwordx4 v13, s[20:23], s68 offen lds\n"
"s_add_u32 s68, s51, 394240\n"
"s_add_u32 m0, s52, 122880\n"
"s_nop 0\n"
"buffer_load_dwordx4 v13, s[20:23], s68 offen lds\n"
"s_add_u32 s68, s51, 395264\n"
"s_add_u32 m0, s52, 131072\n"
"s_nop 0\n"
"buffer_load_dwordx4 v13, s[20:23], s68 offen lds\n"
"s_add_u32 s68, s51, 417792\n"
"s_add_u32 m0, s52, 115712\n"
"s_nop 0\n"
"buffer_load_dwordx4 v14, s[20:23], s68 offen lds\n"
"s_add_u32 s68, s51, 418816\n"
"s_add_u32 m0, s52, 123904\n"
"s_nop 0\n"
"buffer_load_dwordx4 v14, s[20:23], s68 offen lds\n"
"s_add_u32 s68, s51, 419840\n"
"s_add_u32 m0, s52, 132096\n"
"s_nop 0\n"
"buffer_load_dwordx4 v14, s[20:23], s68 offen lds\n"
"s_and_b32 s68, s30, 15\n"
"s_lshr_b32 s69, s30, 4\n"
"s_lshl_b32 s62, s68, 17\n"
"s_lshl_b32 s69, s69, 10\n"
"s_add_u32 s62, s62, s69\n"
"s_mov_b32 s16, s90\n"
"s_and_b32 s17, s91, 0xffff\n"
"s_mov_b32 s18, 0x200000\n"
"s_mov_b32 s19, 0x20000\n"
"s_and_b32 s70, s30, 15\n"
"s_cmp_eq_u32 s70, 0\n"
"s_cselect_b32 s60, 0x80000000, 0\n"
"s_or_b32 s61, s60, 0x7f800000\n"
"s_lshl_b32 s68, s30, 16\n"
"s_lshl_b32 s58, s28, 12\n"
"s_add_u32 s51, s68, s58\n"
"s_lshl_b32 s69, s31, 11\n"
"s_lshl_b32 s70, s28, 9\n"
"s_add_u32 s69, s69, s70\n"
"s_add_u32 s50, s68, s69\n"
"s_lshl_b32 s52, s34, 9\n"
"s_add_u32 s52, s52, 1024\n"
"s_and_b32 s68, s28, 1\n"
"s_lshl_b32 s68, s68, 3\n"
"s_lshr_b32 s70, s28, 1\n"
"s_lshl_b32 s70, s70, 8\n"
"s_add_u32 s53, s68, s70\n"
"s_lshl_b32 s70, s28, 5\n"
"s_lshl_b32 s69, s31, 7\n"
"s_add_u32 s55, s62, s69\n"
"s_add_u32 s55, s55, s70\n"
"s_lshl_b32 s70, s28, 14\n"
"s_add_u32 s57, s70, 65536\n"
"s_lshl_b32 s69, s31, 7\n"
"s_lshl_b32 s70, s28, 17\n"
"s_add_u32 s56, s69, s70\n"
"s_lshl_b32 s69, s31, 2\n"
"s_add_u32 s69, s69, s28\n"
"s_mul_i32 s59, s69, 49152\n"
"s_add_u32 s59, s59, 0x840000\n"
"s_lshl_b32 s68, s30, 5\n"
"s_add_u32 s68, s68, s69\n"
"s_lshl_b32 s68, s68, 11\n"
"s_add_u32 s54, s68, 0x40000\n"
"s_mov_b32 s72, 0xbb000000\n"
"s_mov_b32 s73, 0x3eb8aa3b\n"
"s_mov_b32 s63, 0\n"
"s_mov_b32 s27, 0\n"
"s_mov_b32 s44, 0xaaaaaaaa\n"
"s_mov_b32 s45, 0xaaaaaaaa\n"
"s_mov_b32 s46, 0xcccccccc\n"
"s_mov_b32 s47, 0xcccccccc\n"
"s_mov_b32 s48, 0x00f0000f\n"
"s_mov_b32 s49, 0xf0000f00\n"
"v_lshrrev_b32 v4, 4, v0\n"
"v_lshrrev_b32 v15, 4, v4\n"
"v_and_b32 v16, 15, v4\n"
"v_lshrrev_b32 v17, 5, v4\n"
"v_lshlrev_b32 v17, 8, v17\n"
"v_lshl_add_u32 v17, v16, 4, v17\n"
"v_and_b32 v18, 1, v15\n"
"v_lshl_add_u32 v1, v18, 3, v17\n"
"v_lshlrev_b32 v17, 4, v16\n"
"v_add_u32 v3, 154624, v17\n"
"v_add_u32 v60, s58, v0\n"
"s_lshl_b32 s68, s28, 10\n"
"s_add_u32 s68, s68, 131072\n"
"v_add_u32 v62, s68, v0\n"
"v_lshlrev_b32 v17, 2, v15\n"
"v_sub_u32 v17, v17, v16\n"
"v_cmp_lt_i32 s[36:37], 0, v17\n"
"v_cmp_lt_i32 s[38:39], -1, v17\n"
"v_cmp_lt_i32 s[40:41], -2, v17\n"
"v_cmp_lt_i32 s[42:43], -3, v17\n"
"v_mov_b32 v18, 0xff800000\n"
"v_add_u32 v19, -1, v17\n"
"v_and_b32 v19, 8, v19\n"
"v_cmp_eq_u32 vcc, 0, v19\n"
"s_nop 1\n"
"v_cndmask_b32 v40, 0, v18, vcc\n"
"v_add_u32 v19, 0, v17\n"
"v_and_b32 v19, 8, v19\n"
"v_cmp_eq_u32 vcc, 0, v19\n"
"s_nop 1\n"
"v_cndmask_b32 v41, 0, v18, vcc\n"
"v_add_u32 v19, 1, v17\n"
"v_and_b32 v19, 8, v19\n"
"v_cmp_eq_u32 vcc, 0, v19\n"
"s_nop 1\n"
"v_cndmask_b32 v42, 0, v18, vcc\n"
"v_add_u32 v19, 2, v17\n"
"v_and_b32 v19, 8, v19\n"
"v_cmp_eq_u32 vcc, 0, v19\n"
"s_nop 1\n"
"v_cndmask_b32 v43, 0, v18, vcc\n"
"v_mov_b32 v18, 0xffff\n"
"v_cndmask_b32_e64 v19, 0, v18, s[36:37]\n"
"v_mov_b32 v18, 0xffff0000\n"
"v_cndmask_b32_e64 v20, 0, v18, s[38:39]\n"
"v_or_b32 v44, v19, v20\n"
"v_mov_b32 v18, 0xffff\n"
"v_cndmask_b32_e64 v19, 0, v18, s[40:41]\n"
"v_mov_b32 v18, 0xffff0000\n"
"v_cndmask_b32_e64 v20, 0, v18, s[42:43]\n"
"v_or_b32 v45, v19, v20\n"
"v_mov_b32 v18, 0x3f80\n"
"v_cmp_eq_i32 vcc, 0, v17\n"
"s_nop 1\n"
"v_cndmask_b32 v19, 0, v18, vcc\n"
"v_mov_b32 v18, 0x3f800000\n"
"v_cmp_eq_i32 vcc, -1, v17\n"
"s_nop 1\n"
"v_cndmask_b32 v20, 0, v18, vcc\n"
"v_or_b32 v46, v19, v20\n"
"v_mov_b32 v18, 0x3f80\n"
"v_cmp_eq_i32 vcc, -2, v17\n"
"s_nop 1\n"
"v_cndmask_b32 v19, 0, v18, vcc\n"
"v_mov_b32 v18, 0x3f800000\n"
"v_cmp_eq_i32 vcc, -3, v17\n"
"s_nop 1\n"
"v_cndmask_b32 v20, 0, v18, vcc\n"
"v_or_b32 v47, v19, v20\n"
"s_lshl_b32 s68, s28, 9\n"
"s_add_u32 s68, s68, 159744\n"
"v_lshl_add_u32 v19, v4, 3, s68\n"
"v_mov_b32 v20, -1\n"
"v_mov_b32 v21, -1\n"
"ds_write_b64 v19, v[20:21]\n"
"v_mov_b32 v20, 0\n"
"v_mov_b32 v21, 0\n"
"ds_write_b64 v3, v[20:21]\n"
"v_add_u32 v19, 151552, v0\n"
"ds_write_b128 v19, v[40:43]\n"
"ds_write_b128 v19, v[44:47] offset:1024\n"
"ds_write_b32 v19, v1 offset:2048\n"
"v_bfe_u32 v17, v4, 2, 2\n"
"v_lshl_add_u32 v18, v15, 3, v17\n"
"v_lshlrev_b32 v18, 7, v18\n"
"v_lshrrev_b32 v20, 1, v17\n"
"v_and_b32 v22, 1, v15\n"
"v_lshl_or_b32 v20, v22, 1, v20\n"
"v_lshl_add_u32 v18, v20, 5, v18\n"
"v_and_b32 v20, 3, v4\n"
"v_lshl_add_u32 v18, v20, 3, v18\n"
"ds_write_b32 v19, v18 offset:2052\n"
"s_lshl_b32 s68, s28, 5\n"
"v_xor_b32 v19, s68, v18\n"
"v_add_u32 v19, 65536, v19\n"
"v_add_u32 v21, 73728, v19\n"
"v_add_u32 v22, 49152, v19\n"
"s_cmp_eq_u32 s28, 1\n"
"s_cbranch_scc1 Lv_2\n"
"s_cmp_eq_u32 s28, 2\n"
"s_cbranch_scc1 Lv_3\n"
"s_cmp_eq_u32 s28, 3\n"
"s_cbranch_scc1 Lv_4\n"
"s_waitcnt vmcnt(15)\n"
"s_waitcnt vmcnt(12)\n"
"v_mov_b32 v24, 0x3f803f80\n"
"v_mov_b32 v25, 0x3f803f80\n"
"v_mov_b32 v26, 0x3f803f80\n"
"v_mov_b32 v27, 0x3f803f80\n"
"v_mov_b32 v8, 153608\n"
"s_mov_b64 exec, 1\n"
"ds_write_b64 v8, v[6:7]\n"
"s_mov_b64 exec, -1\n"
"s_waitcnt lgkmcnt(0)\n"
"s_barrier\n"
"v_mov_b32 v8, 153608\n"
"ds_read_b64 v[6:7], v8\n"
"ds_read_b64_tr_b16 v[64:65], v19 offset:0\n"
"ds_read_b64_tr_b16 v[66:67], v19 offset:512\n"
"ds_read_b64_tr_b16 v[68:69], v19 offset:8192\n"
"ds_read_b64_tr_b16 v[70:71], v19 offset:8704\n"
"ds_read_b64_tr_b16 v[72:73], v19 offset:16384\n"
"ds_read_b64_tr_b16 v[74:75], v19 offset:16896\n"
"ds_read_b64_tr_b16 v[76:77], v19 offset:4096\n"
"ds_read_b64_tr_b16 v[78:79], v19 offset:4608\n"
"ds_read_b64_tr_b16 v[80:81], v19 offset:12288\n"
"ds_read_b64_tr_b16 v[82:83], v19 offset:12800\n"
"ds_read_b64_tr_b16 v[84:85], v19 offset:20480\n"
"ds_read_b64_tr_b16 v[86:87], v19 offset:20992\n"
"ds_read_b64_tr_b16 v[40:41], v21 offset:0\n"
"ds_read_b64_tr_b16 v[42:43], v21 offset:512\n"
"ds_read_b64_tr_b16 v[44:45], v21 offset:4096\n"
"ds_read_b64_tr_b16 v[46:47], v21 offset:4608\n"
"s_waitcnt lgkmcnt(0)\n"
"buffer_store_dwordx4 v[40:43], v0, s[12:15], s25 offen sc1\n"
"buffer_store_dwordx4 v[44:47], v0, s[12:15], s26 offen sc1\n"
"s_waitcnt vmcnt(8)\n"
"s_barrier\n"
"ds_read_b64_tr_b16 v[88:89], v19 offset:24576\n"
"ds_read_b64_tr_b16 v[90:91], v19 offset:25088\n"
"ds_read_b64_tr_b16 v[92:93], v19 offset:32768\n"
"ds_read_b64_tr_b16 v[94:95], v19 offset:33280\n"
"ds_read_b64_tr_b16 v[96:97], v19 offset:40960\n"
"ds_read_b64_tr_b16 v[98:99], v19 offset:41472\n"
"ds_read_b64_tr_b16 v[100:101], v19 offset:28672\n"
"ds_read_b64_tr_b16 v[102:103], v19 offset:29184\n"
"ds_read_b64_tr_b16 v[104:105], v19 offset:36864\n"
"ds_read_b64_tr_b16 v[106:107], v19 offset:37376\n"
"ds_read_b64_tr_b16 v[108:109], v19 offset:45056\n"
"ds_read_b64_tr_b16 v[110:111], v19 offset:45568\n"
"v_readfirstlane_b32 s64, v6\n"
"v_readfirstlane_b32 s65, v7\n"
"s_nop 0\n"
"s_add_u32 s64, s64, 1\n"
"s_addc_u32 s65, s65, 0\n"
"v_mfma_f32_16x16x32_bf16 v[28:31], v[24:27], v[64:67], 0\n"
"v_mfma_f32_16x16x32_bf16 v[32:35], v[24:27], v[68:71], 0\n"
"v_mfma_f32_16x16x32_bf16 v[36:39], v[24:27], v[72:75], 0\n"
"v_mfma_f32_16x16x32_bf16 v[28:31], v[24:27], v[76:79], v[28:31]\n"
"v_mfma_f32_16x16x32_bf16 v[32:35], v[24:27], v[80:83], v[32:35]\n"
"v_mfma_f32_16x16x32_bf16 v[36:39], v[24:27], v[84:87], v[36:39]\n"
"s_waitcnt vmcnt(2)\n"
"s_waitcnt lgkmcnt(0)\n"
"s_barrier\n"
"ds_read_b64_tr_b16 v[112:113], v22 offset:0\n"
"ds_read_b64_tr_b16 v[114:115], v22 offset:512\n"
"ds_read_b64_tr_b16 v[116:117], v22 offset:8192\n"
"ds_read_b64_tr_b16 v[118:119], v22 offset:8704\n"
"ds_read_b64_tr_b16 v[120:121], v22 offset:16384\n"
"ds_read_b64_tr_b16 v[122:123], v22 offset:16896\n"
"ds_read_b64_tr_b16 v[124:125], v22 offset:4096\n"
"ds_read_b64_tr_b16 v[126:127], v22 offset:4608\n"
"ds_read_b64_tr_b16 v[128:129], v22 offset:12288\n"
"ds_read_b64_tr_b16 v[130:131], v22 offset:12800\n"
"ds_read_b64_tr_b16 v[132:133], v22 offset:20480\n"
"ds_read_b64_tr_b16 v[134:135], v22 offset:20992\n"
"v_mfma_f32_16x16x32_bf16 v[28:31], v[24:27], v[88:91], v[28:31]\n"
"v_mfma_f32_16x16x32_bf16 v[32:35], v[24:27], v[92:95], v[32:35]\n"
"v_mfma_f32_16x16x32_bf16 v[36:39], v[24:27], v[96:99], v[36:39]\n"
"v_mfma_f32_16x16x32_bf16 v[28:31], v[24:27], v[100:103], v[28:31]\n"
"v_mfma_f32_16x16x32_bf16 v[32:35], v[24:27], v[104:107], v[32:35]\n"
"v_mfma_f32_16x16x32_bf16 v[36:39], v[24:27], v[108:111], v[36:39]\n"
"s_waitcnt vmcnt(0)\n"
"s_waitcnt lgkmcnt(0)\n"
"s_barrier\n"
"ds_read_b64_tr_b16 v[136:137], v19 offset:0\n"
"ds_read_b64_tr_b16 v[138:139], v19 offset:512\n"
"ds_read_b64_tr_b16 v[140:141], v19 offset:8192\n"
"ds_read_b64_tr_b16 v[142:143], v19 offset:8704\n"
"ds_read_b64_tr_b16 v[144:145], v19 offset:16384\n"
"ds_read_b64_tr_b16 v[146:147], v19 offset:16896\n"
"ds_read_b64_tr_b16 v[148:149], v19 offset:4096\n"
"ds_read_b64_tr_b16 v[150:151], v19 offset:4608\n"
"ds_read_b64_tr_b16 v[152:153], v19 offset:12288\n"
"ds_read_b64_tr_b16 v[154:155], v19 offset:12800\n"
"ds_read_b64_tr_b16 v[156:157], v19 offset:20480\n"
"ds_read_b64_tr_b16 v[158:159], v19 offset:20992\n"
"s_lshl_b32 s68, s31, 9\n"
"s_lshl_b32 s69, s30, 4\n"
"s_add_u32 s68, s68, s69\n"
"s_add_u32 s68, s68, 4096\n"
"s_cmp_eq_u32 s28, 0\n"
"s_cselect_b32 s69, 0, 0x80000000\n"
"v_mov_b32 v40, s64\n"
"v_mov_b32 v41, s65\n"
"v_mov_b32 v42, s69\n"
"s_mov_b64 exec, 1\n"
"buffer_store_dwordx2 v[40:41], v42, s[12:15], s68 offen sc1\n"
"s_mov_b64 exec, -1\n"
"v_mfma_f32_16x16x32_bf16 v[28:31], v[24:27], v[112:115], v[28:31]\n"
"v_mfma_f32_16x16x32_bf16 v[32:35], v[24:27], v[116:119], v[32:35]\n"
"v_mfma_f32_16x16x32_bf16 v[36:39], v[24:27], v[120:123], v[36:39]\n"
"v_mfma_f32_16x16x32_bf16 v[28:31], v[24:27], v[124:127], v[28:31]\n"
"v_mfma_f32_16x16x32_bf16 v[32:35], v[24:27], v[128:131], v[32:35]\n"
"v_mfma_f32_16x16x32_bf16 v[36:39], v[24:27], v[132:135], v[36:39]\n"
"s_waitcnt lgkmcnt(0)\n"
"s_barrier\n"
"ds_read_b64_tr_b16 v[160:161], v19 offset:24576\n"
"ds_read_b64_tr_b16 v[162:163], v19 offset:25088\n"
"ds_read_b64_tr_b16 v[164:165], v19 offset:32768\n"
"ds_read_b64_tr_b16 v[166:167], v19 offset:33280\n"
"ds_read_b64_tr_b16 v[168:169], v19 offset:40960\n"
"ds_read_b64_tr_b16 v[170:171], v19 offset:41472\n"
"ds_read_b64_tr_b16 v[172:173], v19 offset:28672\n"
"ds_read_b64_tr_b16 v[174:175], v19 offset:29184\n"
"ds_read_b64_tr_b16 v[176:177], v19 offset:36864\n"
"ds_read_b64_tr_b16 v[178:179], v19 offset:37376\n"
"ds_read_b64_tr_b16 v[180:181], v19 offset:45056\n"
"ds_read_b64_tr_b16 v[182:183], v19 offset:45568\n"
"v_mfma_f32_16x16x32_bf16 v[28:31], v[24:27], v[136:139], v[28:31]\n"
"v_mfma_f32_16x16x32_bf16 v[32:35], v[24:27], v[140:143], v[32:35]\n"
"v_mfma_f32_16x16x32_bf16 v[36:39], v[24:27], v[144:147], v[36:39]\n"
"v_mfma_f32_16x16x32_bf16 v[28:31], v[24:27], v[148:151], v[28:31]\n"
"v_mfma_f32_16x16x32_bf16 v[32:35], v[24:27], v[152:155], v[32:35]\n"
"v_mfma_f32_16x16x32_bf16 v[36:39], v[24:27], v[156:159], v[36:39]\n"
"s_waitcnt lgkmcnt(0)\n"
"s_barrier\n"
"ds_read_b64_tr_b16 v[184:185], v22 offset:0\n"
"ds_read_b64_tr_b16 v[186:187], v22 offset:512\n"
"ds_read_b64_tr_b16 v[188:189], v22 offset:8192\n"
"ds_read_b64_tr_b16 v[190:191], v22 offset:8704\n"
"ds_read_b64_tr_b16 v[192:193], v22 offset:16384\n"
"ds_read_b64_tr_b16 v[194:195], v22 offset:16896\n"
"ds_read_b64_tr_b16 v[196:197], v22 offset:4096\n"
"ds_read_b64_tr_b16 v[198:199], v22 offset:4608\n"
"ds_read_b64_tr_b16 v[200:201], v22 offset:12288\n"
"ds_read_b64_tr_b16 v[202:203], v22 offset:12800\n"
"ds_read_b64_tr_b16 v[204:205], v22 offset:20480\n"
"ds_read_b64_tr_b16 v[206:207], v22 offset:20992\n"
"v_mfma_f32_16x16x32_bf16 v[28:31], v[24:27], v[160:163], v[28:31]\n"
"v_mfma_f32_16x16x32_bf16 v[32:35], v[24:27], v[164:167], v[32:35]\n"
"v_mfma_f32_16x16x32_bf16 v[36:39], v[24:27], v[168:171], v[36:39]\n"
"v_mfma_f32_16x16x32_bf16 v[28:31], v[24:27], v[172:175], v[28:31]\n"
"v_mfma_f32_16x16x32_bf16 v[32:35], v[24:27], v[176:179], v[32:35]\n"
"v_mfma_f32_16x16x32_bf16 v[36:39], v[24:27], v[180:183], v[36:39]\n"
"s_waitcnt lgkmcnt(0)\n"
"s_barrier\n"
"ds_read_b64_tr_b16 v[208:209], v19 offset:0\n"
"ds_read_b64_tr_b16 v[210:211], v19 offset:512\n"
"ds_read_b64_tr_b16 v[212:213], v19 offset:8192\n"
"ds_read_b64_tr_b16 v[214:215], v19 offset:8704\n"
"ds_read_b64_tr_b16 v[216:217], v19 offset:16384\n"
"ds_read_b64_tr_b16 v[218:219], v19 offset:16896\n"
"ds_read_b64_tr_b16 v[220:221], v19 offset:4096\n"
"ds_read_b64_tr_b16 v[222:223], v19 offset:4608\n"
"ds_read_b64_tr_b16 v[224:225], v19 offset:12288\n"
"ds_read_b64_tr_b16 v[226:227], v19 offset:12800\n"
"ds_read_b64_tr_b16 v[228:229], v19 offset:20480\n"
"ds_read_b64_tr_b16 v[230:231], v19 offset:20992\n"
"v_mfma_f32_16x16x32_bf16 v[28:31], v[24:27], v[184:187], v[28:31]\n"
"v_mfma_f32_16x16x32_bf16 v[32:35], v[24:27], v[188:191], v[32:35]\n"
"v_mfma_f32_16x16x32_bf16 v[36:39], v[24:27], v[192:195], v[36:39]\n"
"v_mfma_f32_16x16x32_bf16 v[28:31], v[24:27], v[196:199], v[28:31]\n"
"v_mfma_f32_16x16x32_bf16 v[32:35], v[24:27], v[200:203], v[32:35]\n"
"v_mfma_f32_16x16x32_bf16 v[36:39], v[24:27], v[204:207], v[36:39]\n"
"s_waitcnt lgkmcnt(0)\n"
"s_barrier\n"
"ds_read_b64_tr_b16 v[232:233], v19 offset:24576\n"
"ds_read_b64_tr_b16 v[234:235], v19 offset:25088\n"
"ds_read_b64_tr_b16 v[236:237], v19 offset:32768\n"
"ds_read_b64_tr_b16 v[238:239], v19 offset:33280\n"
"ds_read_b64_tr_b16 v[240:241], v19 offset:40960\n"
"ds_read_b64_tr_b16 v[242:243], v19 offset:41472\n"
"ds_read_b64_tr_b16 v[244:245], v19 offset:28672\n"
"ds_read_b64_tr_b16 v[246:247], v19 offset:29184\n"
"ds_read_b64_tr_b16 v[248:249], v19 offset:36864\n"
"ds_read_b64_tr_b16 v[250:251], v19 offset:37376\n"
"ds_read_b64_tr_b16 v[252:253], v19 offset:45056\n"
"ds_read_b64_tr_b16 v[254:255], v19 offset:45568\n"
"v_mfma_f32_16x16x32_bf16 v[28:31], v[24:27], v[208:211], v[28:31]\n"
"v_mfma_f32_16x16x32_bf16 v[32:35], v[24:27], v[212:215], v[32:35]\n"
"v_mfma_f32_16x16x32_bf16 v[36:39], v[24:27], v[216:219], v[36:39]\n"
"v_mfma_f32_16x16x32_bf16 v[28:31], v[24:27], v[220:223], v[28:31]\n"
"v_mfma_f32_16x16x32_bf16 v[32:35], v[24:27], v[224:227], v[32:35]\n"
"v_mfma_f32_16x16x32_bf16 v[36:39], v[24:27], v[228:231], v[36:39]\n"
"s_waitcnt lgkmcnt(0)\n"
"v_mfma_f32_16x16x32_bf16 v[28:31], v[24:27], v[232:235], v[28:31]\n"
"v_mfma_f32_16x16x32_bf16 v[32:35], v[24:27], v[236:239], v[32:35]\n"
"v_mfma_f32_16x16x32_bf16 v[36:39], v[24:27], v[240:243], v[36:39]\n"
"v_mfma_f32_16x16x32_bf16 v[28:31], v[24:27], v[244:247], v[28:31]\n"
"v_mfma_f32_16x16x32_bf16 v[32:35], v[24:27], v[248:251], v[32:35]\n"
"v_mfma_f32_16x16x32_bf16 v[36:39], v[24:27], v[252:255], v[36:39]\n"
"Lj_5:\n"
"s_nop 7\n"
"v_cvt_pk_bf16_f32 v40, v28, v28\n"
"v_and_b32 v41, 0xffff0000, v40\n"
"v_sub_f32 v41, v28, v41\n"
"v_cvt_pk_bf16_f32 v41, v41, v41\n"
"v_cvt_pk_bf16_f32 v42, v32, v32\n"
"v_and_b32 v43, 0xffff0000, v42\n"
"v_sub_f32 v43, v32, v43\n"
"v_cvt_pk_bf16_f32 v43, v43, v43\n"
"v_cvt_pk_bf16_f32 v44, v36, v36\n"
"v_and_b32 v45, 0xffff0000, v44\n"
"v_sub_f32 v45, v36, v45\n"
"v_cvt_pk_bf16_f32 v45, v45, v45\n"
"v_add_u32 v46, s52, v3\n"
"s_mov_b64 exec, 0xffff\n"
"ds_write_b128 v46, v[40:43]\n"
"ds_write_b64 v46, v[44:45] offset:256\n"
"s_mov_b64 exec, -1\n"
"s_lshr_b32 s68, s35, 3\n"
"s_lshl_b32 s68, s68, 2\n"
"s_add_u32 s68, s68, s28\n"
"s_lshl_b32 s68, s68, 12\n"
"v_lshlrev_b32 v46, 3, v0\n"
"s_mov_b32 m0, 161792\n"
"s_mov_b32 exec_hi, 0\n"
"buffer_load_dword v46, s[4:7], s68 offen lds\n"
"s_mov_b32 exec_hi, -1\n"
"v_mov_b32 v56, 0x3f803f80\n"
"v_mov_b32 v57, 0x3f803f80\n"
"v_mov_b32 v58, 0x3f803f80\n"
"v_mov_b32 v59, 0x3f803f80\n"
"v_lshrrev_b32 v61, 8, v0\n"
"v_lshlrev_b32 v61, 6, v61\n"
"v_add_u32 v61, 154624, v61\n"
"v_lshlrev_b32 v17, 7, v16\n"
"v_bfe_u32 v18, v16, 1, 2\n"
"v_xor_b32 v18, v18, v15\n"
"v_lshl_add_u32 v17, v18, 4, v17\n"
"v_add_u32 v60, s58, v17\n"
"s_setprio 1\n"
"ds_read_b128 v[32:35], v60 offset:49152\n"
"ds_read_b128 v[36:39], v60 offset:49216\n"
"ds_read_b128 v[40:43], v60 offset:51200\n"
"ds_read_b128 v[44:47], v60 offset:51264\n"
"s_waitcnt lgkmcnt(3)\n"
"v_mfma_f32_16x16x32_bf16 v[48:51], v[56:59], v[32:35], 0\n"
"v_mfma_f32_16x16x32_bf16 v[52:55], v[32:35], v[32:35], 0\n"
"s_waitcnt lgkmcnt(2)\n"
"v_mfma_f32_16x16x32_bf16 v[48:51], v[56:59], v[36:39], v[48:51]\n"
"v_mfma_f32_16x16x32_bf16 v[52:55], v[36:39], v[36:39], v[52:55]\n"
"s_waitcnt lgkmcnt(1)\n"
"v_mfma_f32_16x16x32_bf16 v[48:51], v[56:59], v[40:43], v[48:51]\n"
"v_mfma_f32_16x16x32_bf16 v[52:55], v[40:43], v[40:43], v[52:55]\n"
"s_waitcnt lgkmcnt(0)\n"
"v_mfma_f32_16x16x32_bf16 v[48:51], v[56:59], v[44:47], v[48:51]\n"
"v_mfma_f32_16x16x32_bf16 v[52:55], v[44:47], v[44:47], v[52:55]\n"
"v_mfma_f32_16x16x32_bf16 v[4:7], v[68:71], v[32:35], 0\n"
"v_mfma_f32_16x16x32_bf16 v[8:11], v[32:35], v[72:75], 0\n"
"v_mfma_f32_16x16x32_bf16 v[12:15], v[64:67], v[32:35], 0\n"
"v_mfma_f32_16x16x32_bf16 v[4:7], v[80:83], v[36:39], v[4:7]\n"
"s_nop 3\n"
"v_cndmask_b32_e64 v52, v52, v53, s[44:45]\n"
"v_cndmask_b32_e64 v54, v54, v55, s[44:45]\n"
"v_mfma_f32_16x16x32_bf16 v[8:11], v[36:39], v[84:87], v[8:11]\n"
"v_cndmask_b32_e64 v49, v52, v54, s[46:47]\n"
"v_and_b32 v48, -2, v48\n"
"v_xor_b32 v2, 0x1000, v60\n"
"v_mfma_f32_16x16x32_bf16 v[12:15], v[76:79], v[36:39], v[12:15]\n"
"s_barrier\n"
"ds_read_b128 v[32:35], v2 offset:49152\n"
"ds_read_b128 v[36:39], v2 offset:49216\n"
"v_add_u32 v63, s53, v3\n"
"v_mfma_f32_16x16x32_bf16 v[4:7], v[92:95], v[40:43], v[4:7]\n"
"s_mov_b64 exec, s[48:49]\n"
"ds_write_b64 v63, v[48:49] offset:6656\n"
"s_mov_b64 exec, -1\n"
"v_mfma_f32_16x16x32_bf16 v[8:11], v[40:43], v[96:99], v[8:11]\n"
"v_mfma_f32_16x16x32_bf16 v[12:15], v[88:91], v[40:43], v[12:15]\n"
"ds_read_b128 v[40:43], v2 offset:51200\n"
"v_mfma_f32_16x16x32_bf16 v[4:7], v[104:107], v[44:47], v[4:7]\n"
"v_mfma_f32_16x16x32_bf16 v[8:11], v[44:47], v[108:111], v[8:11]\n"
"v_mfma_f32_16x16x32_bf16 v[12:15], v[100:103], v[44:47], v[12:15]\n"
"ds_read_b128 v[44:47], v2 offset:51264\n"
"s_waitcnt lgkmcnt(4)\n"
"v_mfma_f32_16x16x32_bf16 v[4:7], v[116:119], v[32:35], v[4:7]\n"
"ds_read_b128 v[48:51], v3 offset:6656\n"
"ds_read_b128 v[52:55], v3 offset:6912\n"
"v_mfma_f32_16x16x32_bf16 v[8:11], v[32:35], v[120:123], v[8:11]\n"
"v_xor_b32 v2, 0x2000, v60\n"
"v_mfma_f32_16x16x32_bf16 v[12:15], v[112:115], v[32:35], v[12:15]\n"
"ds_read_b128 v[32:35], v2 offset:49152\n"
"s_waitcnt lgkmcnt(6)\n"
"v_mfma_f32_16x16x32_bf16 v[4:7], v[128:131], v[36:39], v[4:7]\n"
"v_mfma_f32_16x16x32_bf16 v[8:11], v[36:39], v[132:135], v[8:11]\n"
"s_waitcnt lgkmcnt(1)\n"
"v_mfma_f32_16x16x32_bf16 v[12:15], v[124:127], v[36:39], v[12:15]\n"
"ds_read_b128 v[36:39], v2 offset:49216\n"
"v_bitop3_b32 v63, 1, v48, v50 bitop3:0x10\n"
"v_bitop3_b32 v63, v63, v52, v54 bitop3:0x10\n"
"v_mfma_f32_16x16x32_bf16 v[4:7], v[140:143], v[40:43], v[4:7]\n"
"v_cmp_eq_u32 vcc, 0, v63\n"
"s_nop 1\n"
"s_cbranch_vccnz LQ_6\n"
"LP_7:\n"
"v_mfma_f32_16x16x32_bf16 v[8:11], v[40:43], v[144:147], v[8:11]\n"
"v_add_f32 v48, v48, v50\n"
"v_add_f32 v52, v52, v54\n"
"v_mfma_f32_16x16x32_bf16 v[12:15], v[136:139], v[40:43], v[12:15]\n"
"ds_read_b128 v[40:43], v2 offset:51200\n"
"v_mfma_f32_16x16x32_bf16 v[4:7], v[152:155], v[44:47], v[4:7]\n"
"v_add_f32 v49, v49, v51\n"
"v_add_f32 v53, v53, v55\n"
"v_mfma_f32_16x16x32_bf16 v[8:11], v[44:47], v[156:159], v[8:11]\n"
"v_add_f32 v52, v52, v48\n"
"v_add_f32 v53, v53, v49\n"
"v_mfma_f32_16x16x32_bf16 v[12:15], v[148:151], v[44:47], v[12:15]\n"
"ds_read_b128 v[44:47], v2 offset:51264\n"
"s_waitcnt lgkmcnt(3)\n"
"v_mfma_f32_16x16x32_bf16 v[4:7], v[164:167], v[32:35], v[4:7]\n"
"v_mul_f32 v48, s72, v52\n"
"v_mul_f32 v52, 0xba000000, v52\n"
"v_mfma_f32_16x16x32_bf16 v[8:11], v[32:35], v[168:171], v[8:11]\n"
"v_xor_b32 v2, 0x3000, v60\n"
"v_mul_f32 v49, v48, v48\n"
"v_mfma_f32_16x16x32_bf16 v[12:15], v[160:163], v[32:35], v[12:15]\n"
"ds_read_b128 v[32:35], v2 offset:49152\n"
"s_waitcnt lgkmcnt(3)\n"
"v_mfma_f32_16x16x32_bf16 v[4:7], v[176:179], v[36:39], v[4:7]\n"
"v_fma_f32 v54, -v53, s72, -v49\n"
"v_cvt_pk_bf16_f32 v53, v52, v52\n"
"v_mfma_f32_16x16x32_bf16 v[8:11], v[36:39], v[180:183], v[8:11]\n"
"v_max_f32 v54, 0, v54\n"
"v_and_b32 v53, 0xffff0000, v53\n"
"v_mfma_f32_16x16x32_bf16 v[12:15], v[172:175], v[36:39], v[12:15]\n"
"ds_read_b128 v[36:39], v2 offset:49216\n"
"s_waitcnt lgkmcnt(3)\n"
"v_mfma_f32_16x16x32_bf16 v[4:7], v[188:191], v[40:43], v[4:7]\n"
"v_add_f32 v54, 0x3727c5ac, v54\n"
"v_sub_f32 v55, v52, v53\n"
"v_mfma_f32_16x16x32_bf16 v[8:11], v[40:43], v[192:195], v[8:11]\n"
"v_rsq_f32 v1, v54\n"
"v_cvt_pk_bf16_f32 v53, v52, v55\n"
"v_mfma_f32_16x16x32_bf16 v[12:15], v[184:187], v[40:43], v[12:15]\n"
"ds_read_b128 v[40:43], v2 offset:51200\n"
"s_waitcnt lgkmcnt(3)\n"
"v_mfma_f32_16x16x32_bf16 v[4:7], v[200:203], v[44:47], v[4:7]\n"
"v_cvt_pk_bf16_f32 v52, v52, v55\n"
"v_mul_f32 v55, s73, v1\n"
"v_mfma_f32_16x16x32_bf16 v[8:11], v[44:47], v[204:207], v[8:11]\n"
"s_mov_b64 exec, 0xffff\n"
"ds_write2_b32 v3, v1, v55 offset0:194 offset1:195\n"
"s_mov_b64 exec, -1\n"
"v_add_u32 v63, s52, v3\n"
"v_mfma_f32_16x16x32_bf16 v[12:15], v[196:199], v[44:47], v[12:15]\n"
"ds_read_b128 v[44:47], v2 offset:51264\n"
"ds_read_b128 v[48:51], v63\n"
"ds_read_b64 v[54:55], v63 offset:256\n"
"s_waitcnt lgkmcnt(6)\n"
"v_mfma_f32_16x16x32_bf16 v[4:7], v[212:215], v[32:35], v[4:7]\n"
"v_mfma_f32_16x16x32_bf16 v[8:11], v[32:35], v[216:219], v[8:11]\n"
"v_mfma_f32_16x16x32_bf16 v[12:15], v[208:211], v[32:35], v[12:15]\n"
"ds_read_b128 v[32:35], v60 offset:32768\n"
"s_waitcnt lgkmcnt(6)\n"
"v_mfma_f32_16x16x32_bf16 v[4:7], v[224:227], v[36:39], v[4:7]\n"
"ds_read2_b32 v[16:17], v61 offset0:194 offset1:198\n"
"ds_read2_b32 v[18:19], v61 offset0:202 offset1:206\n"
"v_mfma_f32_16x16x32_bf16 v[8:11], v[36:39], v[228:231], v[8:11]\n"
"v_mfma_f32_16x16x32_bf16 v[12:15], v[220:223], v[36:39], v[12:15]\n"
"s_barrier\n"
"ds_read_b128 v[36:39], v60 offset:32832\n"
"s_waitcnt lgkmcnt(8)\n"
"v_mfma_f32_16x16x32_bf16 v[4:7], v[236:239], v[40:43], v[4:7]\n"
"v_mfma_f32_16x16x32_bf16 v[8:11], v[40:43], v[240:243], v[8:11]\n"
"v_mfma_f32_16x16x32_bf16 v[12:15], v[232:235], v[40:43], v[12:15]\n"
"ds_read_b128 v[40:43], v60 offset:34816\n"
"s_waitcnt lgkmcnt(7)\n"
"s_waitcnt lgkmcnt(5)\n"
"v_mfma_f32_16x16x16_bf16 v[4:7], v[50:51], v[52:53], v[4:7]\n"
"v_mfma_f32_16x16x16_bf16 v[8:11], v[52:53], v[54:55], v[8:11]\n"
"v_mfma_f32_16x16x16_bf16 v[12:15], v[48:49], v[52:53], v[12:15]\n"
"v_mfma_f32_16x16x32_bf16 v[4:7], v[248:251], v[44:47], v[4:7]\n"
"v_mfma_f32_16x16x32_bf16 v[8:11], v[44:47], v[252:255], v[8:11]\n"
"v_mfma_f32_16x16x32_bf16 v[12:15], v[244:247], v[44:47], v[12:15]\n"
"ds_read_b128 v[44:47], v60 offset:34880\n"
"s_waitcnt lgkmcnt(3)\n"
"v_mfma_f32_16x16x32_bf16 v[48:51], v[56:59], v[32:35], 0\n"
"v_mfma_f32_16x16x32_bf16 v[52:55], v[32:35], v[32:35], 0\n"
"s_nop 1\n"
"v_mul_f32 v4, v4, v1\n"
"v_mul_f32 v5, v5, v1\n"
"s_waitcnt lgkmcnt(2)\n"
"v_mfma_f32_16x16x32_bf16 v[48:51], v[56:59], v[36:39], v[48:51]\n"
"v_mul_f32 v6, v6, v1\n"
"v_mul_f32 v7, v7, v1\n"
"v_mfma_f32_16x16x32_bf16 v[52:55], v[36:39], v[36:39], v[52:55]\n"
"v_cvt_pk_bf16_f32 v4, v4, v5\n"
"v_cvt_pk_bf16_f32 v5, v6, v7\n"
"s_waitcnt lgkmcnt(1)\n"
"v_mfma_f32_16x16x32_bf16 v[48:51], v[56:59], v[40:43], v[48:51]\n"
"v_mul_f32 v8, v8, v16\n"
"v_mul_f32 v9, v9, v17\n"
"v_mfma_f32_16x16x32_bf16 v[52:55], v[40:43], v[40:43], v[52:55]\n"
"v_mul_f32 v10, v10, v18\n"
"v_mul_f32 v11, v11, v19\n"
"s_waitcnt lgkmcnt(0)\n"
"v_mfma_f32_16x16x32_bf16 v[48:51], v[56:59], v[44:47], v[48:51]\n"
"v_cvt_pk_bf16_f32 v6, v8, v9\n"
"v_cvt_pk_bf16_f32 v7, v10, v11\n"
"v_mfma_f32_16x16x32_bf16 v[52:55], v[44:47], v[44:47], v[52:55]\n"
"ds_write_b128 v62, v[4:7] offset:0\n"
"v_cvt_pk_bf16_f32 v12, v12, v13\n"
"v_cvt_pk_bf16_f32 v13, v14, v15\n"
"v_mfma_f32_16x16x32_bf16 v[24:27], v[68:71], v[32:35], 0\n"
"ds_write_b64 v62, v[12:13] offset:8192\n"
"v_mfma_f32_16x16x32_bf16 v[20:23], v[32:35], v[72:75], 0\n"
"v_mfma_f32_16x16x32_bf16 v[28:31], v[64:67], v[32:35], 0\n"
"v_mfma_f32_16x16x32_bf16 v[24:27], v[80:83], v[36:39], v[24:27]\n"
"v_cndmask_b32_e64 v52, v52, v53, s[44:45]\n"
"v_cndmask_b32_e64 v54, v54, v55, s[44:45]\n"
"v_mfma_f32_16x16x32_bf16 v[20:23], v[36:39], v[84:87], v[20:23]\n"
"v_cndmask_b32_e64 v49, v52, v54, s[46:47]\n"
"v_and_b32 v48, -2, v48\n"
"v_xor_b32 v2, 0x1000, v60\n"
"v_mfma_f32_16x16x32_bf16 v[28:31], v[76:79], v[36:39], v[28:31]\n"
"s_waitcnt lgkmcnt(0)\n"
"s_barrier\n"
"ds_read_b128 v[32:35], v2 offset:32768\n"
"ds_read_b128 v[36:39], v2 offset:32832\n"
"v_add_u32 v63, s53, v3\n"
"v_mfma_f32_16x16x32_bf16 v[24:27], v[92:95], v[40:43], v[24:27]\n"
"s_mov_b64 exec, s[48:49]\n"
"ds_write_b64 v63, v[48:49] offset:6144\n"
"s_mov_b64 exec, -1\n"
"v_mfma_f32_16x16x32_bf16 v[20:23], v[40:43], v[96:99], v[20:23]\n"
"v_mfma_f32_16x16x32_bf16 v[28:31], v[88:91], v[40:43], v[28:31]\n"
"ds_read_b128 v[40:43], v2 offset:34816\n"
"v_mfma_f32_16x16x32_bf16 v[24:27], v[104:107], v[44:47], v[24:27]\n"
"v_mfma_f32_16x16x32_bf16 v[20:23], v[44:47], v[108:111], v[20:23]\n"
"v_mfma_f32_16x16x32_bf16 v[28:31], v[100:103], v[44:47], v[28:31]\n"
"ds_read_b128 v[44:47], v2 offset:34880\n"
"s_waitcnt lgkmcnt(4)\n"
"v_mfma_f32_16x16x32_bf16 v[24:27], v[116:119], v[32:35], v[24:27]\n"
"ds_read_b128 v[48:51], v3 offset:6144\n"
"ds_read_b128 v[52:55], v3 offset:6400\n"
"v_mfma_f32_16x16x32_bf16 v[20:23], v[32:35], v[120:123], v[20:23]\n"
"v_xor_b32 v2, 0x2000, v60\n"
"v_mfma_f32_16x16x32_bf16 v[28:31], v[112:115], v[32:35], v[28:31]\n"
"ds_read_b128 v[32:35], v2 offset:32768\n"
"s_waitcnt lgkmcnt(6)\n"
"v_mfma_f32_16x16x32_bf16 v[24:27], v[128:131], v[36:39], v[24:27]\n"
"v_mfma_f32_16x16x32_bf16 v[20:23], v[36:39], v[132:135], v[20:23]\n"
"s_waitcnt lgkmcnt(1)\n"
"v_mfma_f32_16x16x32_bf16 v[28:31], v[124:127], v[36:39], v[28:31]\n"
"ds_read_b128 v[36:39], v2 offset:32832\n"
"v_bitop3_b32 v63, 1, v48, v50 bitop3:0x10\n"
"v_bitop3_b32 v63, v63, v52, v54 bitop3:0x10\n"
"v_mfma_f32_16x16x32_bf16 v[24:27], v[140:143], v[40:43], v[24:27]\n"
"v_cmp_eq_u32 vcc, 0, v63\n"
"s_nop 1\n"
"s_cbranch_vccnz LQ_8\n"
"LP_9:\n"
"v_mfma_f32_16x16x32_bf16 v[20:23], v[40:43], v[144:147], v[20:23]\n"
"v_add_f32 v48, v48, v50\n"
"v_add_f32 v52, v52, v54\n"
"v_mfma_f32_16x16x32_bf16 v[28:31], v[136:139], v[40:43], v[28:31]\n"
"ds_read_b128 v[40:43], v2 offset:34816\n"
"v_mfma_f32_16x16x32_bf16 v[24:27], v[152:155], v[44:47], v[24:27]\n"
"v_add_f32 v49, v49, v51\n"
"v_add_f32 v53, v53, v55\n"
"v_mfma_f32_16x16x32_bf16 v[20:23], v[44:47], v[156:159], v[20:23]\n"
"v_add_f32 v52, v52, v48\n"
"v_add_f32 v53, v53, v49\n"
"v_mfma_f32_16x16x32_bf16 v[28:31], v[148:151], v[44:47], v[28:31]\n"
"ds_read_b128 v[44:47], v2 offset:34880\n"
"s_waitcnt lgkmcnt(3)\n"
"v_mfma_f32_16x16x32_bf16 v[24:27], v[164:167], v[32:35], v[24:27]\n"
"v_mul_f32 v48, s72, v52\n"
"v_mul_f32 v52, 0xba000000, v52\n"
"v_mfma_f32_16x16x32_bf16 v[20:23], v[32:35], v[168:171], v[20:23]\n"
"v_xor_b32 v2, 0x3000, v60\n"
"v_mul_f32 v49, v48, v48\n"
"v_mfma_f32_16x16x32_bf16 v[28:31], v[160:163], v[32:35], v[28:31]\n"
"ds_read_b128 v[32:35], v2 offset:32768\n"
"s_waitcnt lgkmcnt(3)\n"
"v_mfma_f32_16x16x32_bf16 v[24:27], v[176:179], v[36:39], v[24:27]\n"
"v_fma_f32 v54, -v53, s72, -v49\n"
"v_cvt_pk_bf16_f32 v53, v52, v52\n"
"v_mfma_f32_16x16x32_bf16 v[20:23], v[36:39], v[180:183], v[20:23]\n"
"v_max_f32 v54, 0, v54\n"
"v_and_b32 v53, 0xffff0000, v53\n"
"v_mfma_f32_16x16x32_bf16 v[28:31], v[172:175], v[36:39], v[28:31]\n"
"ds_read_b128 v[36:39], v2 offset:32832\n"
"s_waitcnt lgkmcnt(3)\n"
"v_mfma_f32_16x16x32_bf16 v[24:27], v[188:191], v[40:43], v[24:27]\n"
"v_add_f32 v54, 0x3727c5ac, v54\n"
"v_sub_f32 v55, v52, v53\n"
"v_mfma_f32_16x16x32_bf16 v[20:23], v[40:43], v[192:195], v[20:23]\n"
"v_rsq_f32 v1, v54\n"
"v_cvt_pk_bf16_f32 v53, v52, v55\n"
"v_mfma_f32_16x16x32_bf16 v[28:31], v[184:187], v[40:43], v[28:31]\n"
"ds_read_b128 v[40:43], v2 offset:34816\n"
"s_waitcnt lgkmcnt(3)\n"
"v_mfma_f32_16x16x32_bf16 v[24:27], v[200:203], v[44:47], v[24:27]\n"
"v_cvt_pk_bf16_f32 v52, v52, v55\n"
"v_mul_f32 v55, s73, v1\n"
"v_mfma_f32_16x16x32_bf16 v[20:23], v[44:47], v[204:207], v[20:23]\n"
"s_mov_b64 exec, 0xffff\n"
"ds_write2_b32 v3, v1, v55 offset0:130 offset1:131\n"
"s_mov_b64 exec, -1\n"
"v_add_u32 v63, s52, v3\n"
"v_mfma_f32_16x16x32_bf16 v[28:31], v[196:199], v[44:47], v[28:31]\n"
"ds_read_b128 v[44:47], v2 offset:34880\n"
"ds_read_b128 v[48:51], v63\n"
"ds_read_b64 v[54:55], v63 offset:256\n"
"s_waitcnt lgkmcnt(6)\n"
"v_mfma_f32_16x16x32_bf16 v[24:27], v[212:215], v[32:35], v[24:27]\n"
"v_mfma_f32_16x16x32_bf16 v[20:23], v[32:35], v[216:219], v[20:23]\n"
"v_mfma_f32_16x16x32_bf16 v[28:31], v[208:211], v[32:35], v[28:31]\n"
"ds_read_b128 v[32:35], v60 offset:16384\n"
"s_waitcnt lgkmcnt(6)\n"
"v_mfma_f32_16x16x32_bf16 v[24:27], v[224:227], v[36:39], v[24:27]\n"
"ds_read2_b32 v[16:17], v61 offset0:130 offset1:134\n"
"ds_read2_b32 v[18:19], v61 offset0:138 offset1:142\n"
"v_mfma_f32_16x16x32_bf16 v[20:23], v[36:39], v[228:231], v[20:23]\n"
"v_mfma_f32_16x16x32_bf16 v[28:31], v[220:223], v[36:39], v[28:31]\n"
"ds_read_b128 v[36:39], v60 offset:16448\n"
"s_waitcnt lgkmcnt(8)\n"
"v_mfma_f32_16x16x32_bf16 v[24:27], v[236:239], v[40:43], v[24:27]\n"
"v_mfma_f32_16x16x32_bf16 v[20:23], v[40:43], v[240:243], v[20:23]\n"
"v_mfma_f32_16x16x32_bf16 v[28:31], v[232:235], v[40:43], v[28:31]\n"
"ds_read_b128 v[40:43], v60 offset:18432\n"
"s_waitcnt lgkmcnt(7)\n"
"s_waitcnt lgkmcnt(5)\n"
"v_mfma_f32_16x16x16_bf16 v[24:27], v[50:51], v[52:53], v[24:27]\n"
"v_mfma_f32_16x16x16_bf16 v[20:23], v[52:53], v[54:55], v[20:23]\n"
"v_mfma_f32_16x16x16_bf16 v[28:31], v[48:49], v[52:53], v[28:31]\n"
"v_mfma_f32_16x16x32_bf16 v[24:27], v[248:251], v[44:47], v[24:27]\n"
"v_mfma_f32_16x16x32_bf16 v[20:23], v[44:47], v[252:255], v[20:23]\n"
"v_mfma_f32_16x16x32_bf16 v[28:31], v[244:247], v[44:47], v[28:31]\n"
"ds_read_b128 v[44:47], v60 offset:18496\n"
"s_waitcnt lgkmcnt(3)\n"
"v_mfma_f32_16x16x32_bf16 v[48:51], v[56:59], v[32:35], 0\n"
"v_mfma_f32_16x16x32_bf16 v[52:55], v[32:35], v[32:35], 0\n"
"s_nop 1\n"
"v_mul_f32 v24, v24, v1\n"
"v_mul_f32 v25, v25, v1\n"
"s_waitcnt lgkmcnt(2)\n"
"v_mfma_f32_16x16x32_bf16 v[48:51], v[56:59], v[36:39], v[48:51]\n"
"v_mul_f32 v26, v26, v1\n"
"v_mul_f32 v27, v27, v1\n"
"v_mfma_f32_16x16x32_bf16 v[52:55], v[36:39], v[36:39], v[52:55]\n"
"v_mul_f32 v20, v20, v16\n"
"v_mul_f32 v21, v21, v17\n"
"s_waitcnt lgkmcnt(1)\n"
"v_mfma_f32_16x16x32_bf16 v[48:51], v[56:59], v[40:43], v[48:51]\n"
"v_mul_f32 v22, v22, v18\n"
"v_mul_f32 v23, v23, v19\n"
"v_mfma_f32_16x16x32_bf16 v[52:55], v[40:43], v[40:43], v[52:55]\n"
"v_cvt_pk_bf16_f32 v20, v20, v21\n"
"v_cvt_pk_bf16_f32 v21, v22, v23\n"
"s_waitcnt lgkmcnt(0)\n"
"v_mfma_f32_16x16x32_bf16 v[48:51], v[56:59], v[44:47], v[48:51]\n"
"v_cvt_pk_bf16_f32 v22, v24, v25\n"
"v_cvt_pk_bf16_f32 v23, v26, v27\n"
"v_mfma_f32_16x16x32_bf16 v[52:55], v[44:47], v[44:47], v[52:55]\n"
"ds_write_b128 v62, v[20:23] offset:4096\n"
"v_cvt_pk_bf16_f32 v28, v28, v29\n"
"v_cvt_pk_bf16_f32 v29, v30, v31\n"
"v_mfma_f32_16x16x32_bf16 v[4:7], v[68:71], v[32:35], 0\n"
"ds_write_b64 v62, v[28:29] offset:8200\n"
"v_mfma_f32_16x16x32_bf16 v[8:11], v[32:35], v[72:75], 0\n"
"v_mfma_f32_16x16x32_bf16 v[12:15], v[64:67], v[32:35], 0\n"
"v_mfma_f32_16x16x32_bf16 v[4:7], v[80:83], v[36:39], v[4:7]\n"
"v_cndmask_b32_e64 v52, v52, v53, s[44:45]\n"
"v_cndmask_b32_e64 v54, v54, v55, s[44:45]\n"
"v_mfma_f32_16x16x32_bf16 v[8:11], v[36:39], v[84:87], v[8:11]\n"
"v_cndmask_b32_e64 v49, v52, v54, s[46:47]\n"
"v_and_b32 v48, -2, v48\n"
"v_xor_b32 v2, 0x1000, v60\n"
"v_mfma_f32_16x16x32_bf16 v[12:15], v[76:79], v[36:39], v[12:15]\n"
"s_waitcnt lgkmcnt(0)\n"
"s_barrier\n"
"ds_read_b128 v[32:35], v2 offset:16384\n"
"ds_read_b128 v[36:39], v2 offset:16448\n"
"v_add_u32 v63, s53, v3\n"
"v_mfma_f32_16x16x32_bf16 v[4:7], v[92:95], v[40:43], v[4:7]\n"
"s_mov_b64 exec, s[48:49]\n"
"ds_write_b64 v63, v[48:49] offset:5632\n"
"s_mov_b64 exec, -1\n"
"v_mfma_f32_16x16x32_bf16 v[8:11], v[40:43], v[96:99], v[8:11]\n"
"v_mfma_f32_16x16x32_bf16 v[12:15], v[88:91], v[40:43], v[12:15]\n"
"ds_read_b128 v[40:43], v2 offset:18432\n"
"v_mfma_f32_16x16x32_bf16 v[4:7], v[104:107], v[44:47], v[4:7]\n"
"v_mfma_f32_16x16x32_bf16 v[8:11], v[44:47], v[108:111], v[8:11]\n"
"v_mfma_f32_16x16x32_bf16 v[12:15], v[100:103], v[44:47], v[12:15]\n"
"ds_read_b128 v[44:47], v2 offset:18496\n"
"s_waitcnt lgkmcnt(4)\n"
"v_mfma_f32_16x16x32_bf16 v[4:7], v[116:119], v[32:35], v[4:7]\n"
"ds_read_b128 v[48:51], v3 offset:5632\n"
"ds_read_b128 v[52:55], v3 offset:5888\n"
"v_mfma_f32_16x16x32_bf16 v[8:11], v[32:35], v[120:123], v[8:11]\n"
"v_xor_b32 v2, 0x2000, v60\n"
"v_mfma_f32_16x16x32_bf16 v[12:15], v[112:115], v[32:35], v[12:15]\n"
"ds_read_b128 v[32:35], v2 offset:16384\n"
"s_waitcnt lgkmcnt(6)\n"
"v_mfma_f32_16x16x32_bf16 v[4:7], v[128:131], v[36:39], v[4:7]\n"
"v_mfma_f32_16x16x32_bf16 v[8:11], v[36:39], v[132:135], v[8:11]\n"
"s_waitcnt lgkmcnt(1)\n"
"v_mfma_f32_16x16x32_bf16 v[12:15], v[124:127], v[36:39], v[12:15]\n"
"ds_read_b128 v[36:39], v2 offset:16448\n"
"v_bitop3_b32 v63, 1, v48, v50 bitop3:0x10\n"
"v_bitop3_b32 v63, v63, v52, v54 bitop3:0x10\n"
"v_mfma_f32_16x16x32_bf16 v[4:7], v[140:143], v[40:43], v[4:7]\n"
"v_cmp_eq_u32 vcc, 0, v63\n"
"s_nop 1\n"
"s_cbranch_vccnz LQ_10\n"
"LP_11:\n"
"v_mfma_f32_16x16x32_bf16 v[8:11], v[40:43], v[144:147], v[8:11]\n"
"v_add_f32 v48, v48, v50\n"
"v_add_f32 v52, v52, v54\n"
"v_mfma_f32_16x16x32_bf16 v[12:15], v[136:139], v[40:43], v[12:15]\n"
"ds_read_b128 v[40:43], v2 offset:18432\n"
"v_mfma_f32_16x16x32_bf16 v[4:7], v[152:155], v[44:47], v[4:7]\n"
"v_add_f32 v49, v49, v51\n"
"v_add_f32 v53, v53, v55\n"
"v_mfma_f32_16x16x32_bf16 v[8:11], v[44:47], v[156:159], v[8:11]\n"
"v_add_f32 v52, v52, v48\n"
"v_add_f32 v53, v53, v49\n"
"v_mfma_f32_16x16x32_bf16 v[12:15], v[148:151], v[44:47], v[12:15]\n"
"ds_read_b128 v[44:47], v2 offset:18496\n"
"s_waitcnt lgkmcnt(3)\n"
"v_mfma_f32_16x16x32_bf16 v[4:7], v[164:167], v[32:35], v[4:7]\n"
"v_mul_f32 v48, s72, v52\n"
"v_mul_f32 v52, 0xba000000, v52\n"
"v_mfma_f32_16x16x32_bf16 v[8:11], v[32:35], v[168:171], v[8:11]\n"
"v_xor_b32 v2, 0x3000, v60\n"
"v_mul_f32 v49, v48, v48\n"
"v_mfma_f32_16x16x32_bf16 v[12:15], v[160:163], v[32:35], v[12:15]\n"
"ds_read_b128 v[32:35], v2 offset:16384\n"
"s_waitcnt lgkmcnt(3)\n"
"v_mfma_f32_16x16x32_bf16 v[4:7], v[176:179], v[36:39], v[4:7]\n"
"v_fma_f32 v54, -v53, s72, -v49\n"
"v_cvt_pk_bf16_f32 v53, v52, v52\n"
"v_mfma_f32_16x16x32_bf16 v[8:11], v[36:39], v[180:183], v[8:11]\n"
"v_max_f32 v54, 0, v54\n"
"v_and_b32 v53, 0xffff0000, v53\n"
"v_mfma_f32_16x16x32_bf16 v[12:15], v[172:175], v[36:39], v[12:15]\n"
"ds_read_b128 v[36:39], v2 offset:16448\n"
"s_waitcnt lgkmcnt(3)\n"
"v_mfma_f32_16x16x32_bf16 v[4:7], v[188:191], v[40:43], v[4:7]\n"
"v_add_f32 v54, 0x3727c5ac, v54\n"
"v_sub_f32 v55, v52, v53\n"
"v_mfma_f32_16x16x32_bf16 v[8:11], v[40:43], v[192:195], v[8:11]\n"
"v_rsq_f32 v1, v54\n"
"v_cvt_pk_bf16_f32 v53, v52, v55\n"
"v_mfma_f32_16x16x32_bf16 v[12:15], v[184:187], v[40:43], v[12:15]\n"
"ds_read_b128 v[40:43], v2 offset:18432\n"
"s_waitcnt lgkmcnt(3)\n"
"v_mfma_f32_16x16x32_bf16 v[4:7], v[200:203], v[44:47], v[4:7]\n"
"v_cvt_pk_bf16_f32 v52, v52, v55\n"
"v_mul_f32 v55, s73, v1\n"
"v_mfma_f32_16x16x32_bf16 v[8:11], v[44:47], v[204:207], v[8:11]\n"
"s_mov_b64 exec, 0xffff\n"
"ds_write2_b32 v3, v1, v55 offset0:66 offset1:67\n"
"s_mov_b64 exec, -1\n"
"v_add_u32 v63, s52, v3\n"
"v_mfma_f32_16x16x32_bf16 v[12:15], v[196:199], v[44:47], v[12:15]\n"
"ds_read_b128 v[44:47], v2 offset:18496\n"
"ds_read_b128 v[48:51], v63\n"
"ds_read_b64 v[54:55], v63 offset:256\n"
"s_waitcnt lgkmcnt(6)\n"
"v_mfma_f32_16x16x32_bf16 v[4:7], v[212:215], v[32:35], v[4:7]\n"
"v_mfma_f32_16x16x32_bf16 v[8:11], v[32:35], v[216:219], v[8:11]\n"
"v_mfma_f32_16x16x32_bf16 v[12:15], v[208:211], v[32:35], v[12:15]\n"
"ds_read_b128 v[32:35], v60 offset:0\n"
"s_waitcnt lgkmcnt(6)\n"
"v_mfma_f32_16x16x32_bf16 v[4:7], v[224:227], v[36:39], v[4:7]\n"
"ds_read2_b32 v[16:17], v61 offset0:66 offset1:70\n"
"ds_read2_b32 v[18:19], v61 offset0:74 offset1:78\n"
"v_mfma_f32_16x16x32_bf16 v[8:11], v[36:39], v[228:231], v[8:11]\n"
"v_mfma_f32_16x16x32_bf16 v[12:15], v[220:223], v[36:39], v[12:15]\n"
"ds_read_b128 v[36:39], v60 offset:64\n"
"s_waitcnt lgkmcnt(8)\n"
"v_mfma_f32_16x16x32_bf16 v[4:7], v[236:239], v[40:43], v[4:7]\n"
"v_mfma_f32_16x16x32_bf16 v[8:11], v[40:43], v[240:243], v[8:11]\n"
"v_mfma_f32_16x16x32_bf16 v[12:15], v[232:235], v[40:43], v[12:15]\n"
"ds_read_b128 v[40:43], v60 offset:2048\n"
"s_waitcnt lgkmcnt(7)\n"
"s_waitcnt lgkmcnt(5)\n"
"v_mfma_f32_16x16x16_bf16 v[4:7], v[50:51], v[52:53], v[4:7]\n"
"v_mfma_f32_16x16x16_bf16 v[8:11], v[52:53], v[54:55], v[8:11]\n"
"v_mfma_f32_16x16x16_bf16 v[12:15], v[48:49], v[52:53], v[12:15]\n"
"v_mfma_f32_16x16x32_bf16 v[4:7], v[248:251], v[44:47], v[4:7]\n"
"v_mfma_f32_16x16x32_bf16 v[8:11], v[44:47], v[252:255], v[8:11]\n"
"v_mfma_f32_16x16x32_bf16 v[12:15], v[244:247], v[44:47], v[12:15]\n"
"ds_read_b128 v[44:47], v60 offset:2112\n"
"s_waitcnt lgkmcnt(3)\n"
"v_mfma_f32_16x16x32_bf16 v[48:51], v[56:59], v[32:35], 0\n"
"v_mfma_f32_16x16x32_bf16 v[52:55], v[32:35], v[32:35], 0\n"
"s_nop 1\n"
"v_mul_f32 v4, v4, v1\n"
"v_mul_f32 v5, v5, v1\n"
"s_waitcnt lgkmcnt(2)\n"
"v_mfma_f32_16x16x32_bf16 v[48:51], v[56:59], v[36:39], v[48:51]\n"
"v_mul_f32 v6, v6, v1\n"
"v_mul_f32 v7, v7, v1\n"
"v_mfma_f32_16x16x32_bf16 v[52:55], v[36:39], v[36:39], v[52:55]\n"
"v_cvt_pk_bf16_f32 v4, v4, v5\n"
"v_cvt_pk_bf16_f32 v5, v6, v7\n"
"s_waitcnt lgkmcnt(1)\n"
"v_mfma_f32_16x16x32_bf16 v[48:51], v[56:59], v[40:43], v[48:51]\n"
"v_mul_f32 v8, v8, v16\n"
"v_mul_f32 v9, v9, v17\n"
"v_mfma_f32_16x16x32_bf16 v[52:55], v[40:43], v[40:43], v[52:55]\n"
"v_mul_f32 v10, v10, v18\n"
"v_mul_f32 v11, v11, v19\n"
"s_waitcnt lgkmcnt(0)\n"
"v_mfma_f32_16x16x32_bf16 v[48:51], v[56:59], v[44:47], v[48:51]\n"
"v_cvt_pk_bf16_f32 v6, v8, v9\n"
"v_cvt_pk_bf16_f32 v7, v10, v11\n"
"v_mfma_f32_16x16x32_bf16 v[52:55], v[44:47], v[44:47], v[52:55]\n"
"ds_write_b128 v62, v[4:7] offset:0\n"
"v_cvt_pk_bf16_f32 v12, v12, v13\n"
"v_cvt_pk_bf16_f32 v13, v14, v15\n"
"v_mfma_f32_16x16x32_bf16 v[24:27], v[68:71], v[32:35], 0\n"
"ds_write_b64 v62, v[12:13] offset:8192\n"
"v_mfma_f32_16x16x32_bf16 v[20:23], v[32:35], v[72:75], 0\n"
"v_mfma_f32_16x16x32_bf16 v[28:31], v[64:67], v[32:35], 0\n"
"v_mfma_f32_16x16x32_bf16 v[24:27], v[80:83], v[36:39], v[24:27]\n"
"v_cndmask_b32_e64 v52, v52, v53, s[44:45]\n"
"v_cndmask_b32_e64 v54, v54, v55, s[44:45]\n"
"v_mfma_f32_16x16x32_bf16 v[20:23], v[36:39], v[84:87], v[20:23]\n"
"v_cndmask_b32_e64 v49, v52, v54, s[46:47]\n"
"v_and_b32 v48, -2, v48\n"
"v_xor_b32 v2, 0x1000, v60\n"
"v_mfma_f32_16x16x32_bf16 v[28:31], v[76:79], v[36:39], v[28:31]\n"
"s_waitcnt lgkmcnt(0)\n"
"s_barrier\n"
"ds_read_b128 v[32:35], v2 offset:0\n"
"ds_read_b128 v[36:39], v2 offset:64\n"
"s_add_u32 s69, s51, 0xacc000\n"
"buffer_load_dwordx4 v[4:7], v0, s[12:15], s69 offen offset:0 sc1\n"
"buffer_load_dwordx4 v[8:11], v0, s[12:15], s69 offen offset:1024 sc1\n"
"buffer_load_dwordx4 v[12:15], v0, s[12:15], s69 offen offset:2048 sc1\n"
"buffer_load_dwordx4 v[16:19], v0, s[12:15], s69 offen offset:3072 sc1\n"
"v_add_u32 v63, s53, v3\n"
"v_mfma_f32_16x16x32_bf16 v[24:27], v[92:95], v[40:43], v[24:27]\n"
"s_mov_b64 exec, s[48:49]\n"
"ds_write_b64 v63, v[48:49] offset:5120\n"
"s_mov_b64 exec, -1\n"
"v_mfma_f32_16x16x32_bf16 v[20:23], v[40:43], v[96:99], v[20:23]\n"
"v_mfma_f32_16x16x32_bf16 v[28:31], v[88:91], v[40:43], v[28:31]\n"
"ds_read_b128 v[40:43], v2 offset:2048\n"
"v_mfma_f32_16x16x32_bf16 v[24:27], v[104:107], v[44:47], v[24:27]\n"
"v_mfma_f32_16x16x32_bf16 v[20:23], v[44:47], v[108:111], v[20:23]\n"
"v_mfma_f32_16x16x32_bf16 v[28:31], v[100:103], v[44:47], v[28:31]\n"
"ds_read_b128 v[44:47], v2 offset:2112\n"
"s_waitcnt lgkmcnt(4)\n"
"v_mfma_f32_16x16x32_bf16 v[24:27], v[116:119], v[32:35], v[24:27]\n"
"ds_read_b128 v[48:51], v3 offset:5120\n"
"ds_read_b128 v[52:55], v3 offset:5376\n"
"v_mfma_f32_16x16x32_bf16 v[20:23], v[32:35], v[120:123], v[20:23]\n"
"v_xor_b32 v2, 0x2000, v60\n"
"v_mfma_f32_16x16x32_bf16 v[28:31], v[112:115], v[32:35], v[28:31]\n"
"ds_read_b128 v[32:35], v2 offset:0\n"
"s_waitcnt lgkmcnt(6)\n"
"v_mfma_f32_16x16x32_bf16 v[24:27], v[128:131], v[36:39], v[24:27]\n"
"v_mfma_f32_16x16x32_bf16 v[20:23], v[36:39], v[132:135], v[20:23]\n"
"s_waitcnt lgkmcnt(1)\n"
"v_mfma_f32_16x16x32_bf16 v[28:31], v[124:127], v[36:39], v[28:31]\n"
"ds_read_b128 v[36:39], v2 offset:64\n"
"v_bitop3_b32 v63, 1, v48, v50 bitop3:0x10\n"
"v_bitop3_b32 v63, v63, v52, v54 bitop3:0x10\n"
"v_mfma_f32_16x16x32_bf16 v[24:27], v[140:143], v[40:43], v[24:27]\n"
"v_cmp_eq_u32 vcc, 0, v63\n"
"s_nop 1\n"
"s_cbranch_vccnz LQ_12\n"
"LP_13:\n"
"v_mfma_f32_16x16x32_bf16 v[20:23], v[40:43], v[144:147], v[20:23]\n"
"v_add_f32 v48, v48, v50\n"
"v_add_f32 v52, v52, v54\n"
"v_mfma_f32_16x16x32_bf16 v[28:31], v[136:139], v[40:43], v[28:31]\n"
"ds_read_b128 v[40:43], v2 offset:2048\n"
"v_mfma_f32_16x16x32_bf16 v[24:27], v[152:155], v[44:47], v[24:27]\n"
"v_add_f32 v49, v49, v51\n"
"v_add_f32 v53, v53, v55\n"
"v_mfma_f32_16x16x32_bf16 v[20:23], v[44:47], v[156:159], v[20:23]\n"
"v_add_f32 v52, v52, v48\n"
"v_add_f32 v53, v53, v49\n"
"v_mfma_f32_16x16x32_bf16 v[28:31], v[148:151], v[44:47], v[28:31]\n"
"ds_read_b128 v[44:47], v2 offset:2112\n"
"s_waitcnt lgkmcnt(3)\n"
"v_mfma_f32_16x16x32_bf16 v[24:27], v[164:167], v[32:35], v[24:27]\n"
"v_mul_f32 v48, s72, v52\n"
"v_mul_f32 v52, 0xba000000, v52\n"
"v_mfma_f32_16x16x32_bf16 v[20:23], v[32:35], v[168:171], v[20:23]\n"
"v_xor_b32 v2, 0x3000, v60\n"
"v_mul_f32 v49, v48, v48\n"
"v_mfma_f32_16x16x32_bf16 v[28:31], v[160:163], v[32:35], v[28:31]\n"
"ds_read_b128 v[32:35], v2 offset:0\n"
"s_waitcnt lgkmcnt(3)\n"
"v_mfma_f32_16x16x32_bf16 v[24:27], v[176:179], v[36:39], v[24:27]\n"
"v_fma_f32 v54, -v53, s72, -v49\n"
"v_cvt_pk_bf16_f32 v53, v52, v52\n"
"v_mfma_f32_16x16x32_bf16 v[20:23], v[36:39], v[180:183], v[20:23]\n"
"v_max_f32 v54, 0, v54\n"
"v_and_b32 v53, 0xffff0000, v53\n"
"v_mfma_f32_16x16x32_bf16 v[28:31], v[172:175], v[36:39], v[28:31]\n"
"ds_read_b128 v[36:39], v2 offset:64\n"
"s_waitcnt lgkmcnt(3)\n"
"v_mfma_f32_16x16x32_bf16 v[24:27], v[188:191], v[40:43], v[24:27]\n"
"v_add_f32 v54, 0x3727c5ac, v54\n"
"v_sub_f32 v55, v52, v53\n"
"v_mfma_f32_16x16x32_bf16 v[20:23], v[40:43], v[192:195], v[20:23]\n"
"v_rsq_f32 v1, v54\n"
"v_cvt_pk_bf16_f32 v53, v52, v55\n"
"v_mfma_f32_16x16x32_bf16 v[28:31], v[184:187], v[40:43], v[28:31]\n"
"ds_read_b128 v[40:43], v2 offset:2048\n"
"s_waitcnt lgkmcnt(3)\n"
"v_mfma_f32_16x16x32_bf16 v[24:27], v[200:203], v[44:47], v[24:27]\n"
"v_cvt_pk_bf16_f32 v52, v52, v55\n"
"v_mul_f32 v55, s73, v1\n"
"v_mfma_f32_16x16x32_bf16 v[20:23], v[44:47], v[204:207], v[20:23]\n"
"s_mov_b64 exec, 0xffff\n"
"ds_write2_b32 v3, v1, v55 offset0:2 offset1:3\n"
"s_mov_b64 exec, -1\n"
"v_add_u32 v63, s52, v3\n"
"v_mfma_f32_16x16x32_bf16 v[28:31], v[196:199], v[44:47], v[28:31]\n"
"ds_read_b128 v[44:47], v2 offset:2112\n"
"ds_read_b128 v[48:51], v63\n"
"ds_read_b64 v[54:55], v63 offset:256\n"
"s_waitcnt lgkmcnt(6)\n"
"v_mfma_f32_16x16x32_bf16 v[24:27], v[212:215], v[32:35], v[24:27]\n"
"v_mfma_f32_16x16x32_bf16 v[20:23], v[32:35], v[216:219], v[20:23]\n"
"v_mfma_f32_16x16x32_bf16 v[28:31], v[208:211], v[32:35], v[28:31]\n"
"s_waitcnt lgkmcnt(5)\n"
"v_mfma_f32_16x16x32_bf16 v[24:27], v[224:227], v[36:39], v[24:27]\n"
"ds_read2_b32 v[32:33], v61 offset0:2 offset1:6\n"
"ds_read2_b32 v[34:35], v61 offset0:10 offset1:14\n"
"v_mfma_f32_16x16x32_bf16 v[20:23], v[36:39], v[228:231], v[20:23]\n"
"v_add_u32 v63, 153600, v0\n"
"ds_read_b32 v61, v63 offset:4\n"
"v_mfma_f32_16x16x32_bf16 v[28:31], v[220:223], v[36:39], v[28:31]\n"
"s_waitcnt lgkmcnt(7)\n"
"v_mfma_f32_16x16x32_bf16 v[24:27], v[236:239], v[40:43], v[24:27]\n"
"s_xor_b32 s68, s28, 1\n"
"s_lshl_b32 s68, s68, 5\n"
"v_mfma_f32_16x16x32_bf16 v[20:23], v[40:43], v[240:243], v[20:23]\n"
"s_waitcnt lgkmcnt(0)\n"
"v_xor_b32 v61, s68, v61\n"
"v_add_u32 v61, s57, v61\n"
"v_mfma_f32_16x16x32_bf16 v[28:31], v[232:235], v[40:43], v[28:31]\n"
"v_mfma_f32_16x16x16_bf16 v[24:27], v[50:51], v[52:53], v[24:27]\n"
"v_mfma_f32_16x16x16_bf16 v[20:23], v[52:53], v[54:55], v[20:23]\n"
"v_mfma_f32_16x16x16_bf16 v[28:31], v[48:49], v[52:53], v[28:31]\n"
"v_mfma_f32_16x16x32_bf16 v[24:27], v[248:251], v[44:47], v[24:27]\n"
"ds_read_b64_tr_b16 v[36:37], v61 offset:4096\n"
"ds_read_b64_tr_b16 v[38:39], v61 offset:4608\n"
"ds_read_b64_tr_b16 v[40:41], v61 offset:8192\n"
"ds_read_b64_tr_b16 v[42:43], v61 offset:8704\n"
"v_mfma_f32_16x16x32_bf16 v[20:23], v[44:47], v[252:255], v[20:23]\n"
"v_mfma_f32_16x16x32_bf16 v[28:31], v[244:247], v[44:47], v[28:31]\n"
"v_add_u32 v63, 152576, v0\n"
"ds_read_b64 v[50:51], v62 offset:16392\n"
"ds_read_b64 v[48:49], v63 offset:8\n"
"ds_read_b64_tr_b16 v[44:45], v61 offset:12288\n"
"ds_read_b64_tr_b16 v[46:47], v61 offset:12800\n"
"v_add_u32 v63, 153600, v0\n"
"v_mul_f32 v24, v24, v1\n"
"v_mul_f32 v25, v25, v1\n"
"v_mul_f32 v26, v26, v1\n"
"v_mul_f32 v27, v27, v1\n"
"v_mul_f32 v20, v20, v32\n"
"v_mul_f32 v21, v21, v33\n"
"v_mul_f32 v22, v22, v34\n"
"v_mul_f32 v23, v23, v35\n"
"v_cvt_pk_bf16_f32 v20, v20, v21\n"
"v_cvt_pk_bf16_f32 v21, v22, v23\n"
"v_cvt_pk_bf16_f32 v22, v24, v25\n"
"v_cvt_pk_bf16_f32 v23, v26, v27\n"
"ds_write_b128 v62, v[20:23] offset:4096\n"
"v_cvt_pk_bf16_f32 v28, v28, v29\n"
"v_cvt_pk_bf16_f32 v29, v30, v31\n"
"ds_write_b64 v62, v[28:29] offset:8200\n"
"ds_read_b64_tr_b16 v[32:33], v61 offset:0\n"
"ds_read_b64_tr_b16 v[34:35], v61 offset:512\n"
"ds_read_b32 v1, v63\n"
"v_add_u32 v60, s58, v0\n"
"s_waitcnt lgkmcnt(3)\n"
"s_barrier\n"
"s_setprio 3\n"
"v_mfma_f32_16x16x16_bf16 v[28:31], v[48:49], v[50:51], 0\n"
"v_xor_b32 v61, 96, v61\n"
"ds_read_b64_tr_b16 v[48:49], v61 offset:0\n"
"ds_read_b64_tr_b16 v[50:51], v61 offset:512\n"
"s_waitcnt vmcnt(0)\n"
"LB_14:\n"
"s_waitcnt lgkmcnt(3)\n"
"v_mfma_f32_16x16x32_bf16 v[20:23], v[32:35], v[4:7], 0\n"
"ds_read_b64_tr_b16 v[32:33], v61 offset:4096\n"
"ds_read_b64_tr_b16 v[34:35], v61 offset:4608\n"
"v_bitop3_b32 v63, 1, v4, v6 bitop3:0x10\n"
"v_bitop3_b32 v63, v63, v8, v10 bitop3:0x10\n"
"v_mfma_f32_16x16x32_bf16 v[20:23], v[36:39], v[8:11], v[20:23]\n"
"ds_read_b64_tr_b16 v[36:37], v61 offset:8192\n"
"ds_read_b64_tr_b16 v[38:39], v61 offset:8704\n"
"v_bitop3_b32 v63, v63, v12, v14 bitop3:0x10\n"
"v_bitop3_b32 v63, v63, v16, v18 bitop3:0x10\n"
"v_mfma_f32_16x16x32_bf16 v[20:23], v[40:43], v[12:15], v[20:23]\n"
"ds_read_b64_tr_b16 v[40:41], v61 offset:12288\n"
"ds_read_b64_tr_b16 v[42:43], v61 offset:12800\n"
"v_cmp_eq_u32 vcc, 0, v63\n"
"v_mfma_f32_16x16x32_bf16 v[20:23], v[44:47], v[16:19], v[20:23]\n"
"v_xor_b32 v61, 32, v61\n"
"ds_read_b64_tr_b16 v[44:45], v61 offset:0\n"
"ds_read_b64_tr_b16 v[46:47], v61 offset:512\n"
"s_cbranch_vccnz LC_15\n"
"s_waitcnt lgkmcnt(8)\n"
"v_mfma_f32_16x16x32_bf16 v[24:27], v[48:51], v[4:7], 0\n"
"ds_read_b64_tr_b16 v[48:49], v61 offset:4096\n"
"ds_read_b64_tr_b16 v[50:51], v61 offset:4608\n"
"s_waitcnt lgkmcnt(8)\n"
"v_mfma_f32_16x16x32_bf16 v[24:27], v[32:35], v[8:11], v[24:27]\n"
"ds_read_b64_tr_b16 v[32:33], v61 offset:8192\n"
"ds_read_b64_tr_b16 v[34:35], v61 offset:8704\n"
"s_waitcnt lgkmcnt(8)\n"
"v_mfma_f32_16x16x32_bf16 v[24:27], v[36:39], v[12:15], v[24:27]\n"
"ds_read_b64_tr_b16 v[36:37], v61 offset:12288\n"
"ds_read_b64_tr_b16 v[38:39], v61 offset:12800\n"
"ds_write_b128 v60, v[20:23] offset:32768\n"
"s_waitcnt lgkmcnt(9)\n"
"v_mfma_f32_16x16x32_bf16 v[24:27], v[40:43], v[16:19], v[24:27]\n"
"v_xor_b32 v61, 96, v61\n"
"ds_read_b64_tr_b16 v[40:41], v61 offset:0\n"
"ds_read_b64_tr_b16 v[42:43], v61 offset:512\n"
"s_waitcnt lgkmcnt(9)\n"
"v_mfma_f32_16x16x32_bf16 v[20:23], v[44:47], v[4:7], 0\n"
"ds_read_b64_tr_b16 v[44:45], v61 offset:4096\n"
"ds_read_b64_tr_b16 v[46:47], v61 offset:4608\n"
"s_waitcnt lgkmcnt(9)\n"
"v_mfma_f32_16x16x32_bf16 v[20:23], v[48:51], v[8:11], v[20:23]\n"
"ds_read_b64_tr_b16 v[48:49], v61 offset:8192\n"
"ds_read_b64_tr_b16 v[50:51], v61 offset:8704\n"
"s_waitcnt lgkmcnt(9)\n"
"v_mfma_f32_16x16x32_bf16 v[20:23], v[32:35], v[12:15], v[20:23]\n"
"ds_read_b64_tr_b16 v[32:33], v61 offset:12288\n"
"ds_read_b64_tr_b16 v[34:35], v61 offset:12800\n"
"v_xor_b32 v61, 32, v61\n"
"ds_write_b128 v60, v[24:27] offset:33792\n"
"s_waitcnt lgkmcnt(10)\n"
"v_mfma_f32_16x16x32_bf16 v[20:23], v[36:39], v[16:19], v[20:23]\n"
"s_waitcnt lgkmcnt(7)\n"
"v_mfma_f32_16x16x32_bf16 v[28:31], v[40:43], v[4:7], v[28:31]\n"
"s_add_u32 m0, s58, 16384\n"
"s_add_u32 s69, s51, 0xac8000\n"
"buffer_load_dwordx4 v0, s[12:15], s69 offen offset:0 sc1 lds\n"
"buffer_load_dwordx4 v0, s[12:15], s69 offen offset:1024 sc1 lds\n"
"buffer_load_dwordx4 v0, s[12:15], s69 offen offset:2048 sc1 lds\n"
"buffer_load_dwordx4 v0, s[12:15], s69 offen offset:3072 sc1 lds\n"
"s_waitcnt lgkmcnt(5)\n"
"v_mfma_f32_16x16x32_bf16 v[28:31], v[44:47], v[8:11], v[28:31]\n"
"s_waitcnt lgkmcnt(3)\n"
"v_mfma_f32_16x16x32_bf16 v[28:31], v[48:51], v[12:15], v[28:31]\n"
"ds_write_b128 v60, v[20:23] offset:34816\n"
"s_waitcnt lgkmcnt(2)\n"
"v_mfma_f32_16x16x32_bf16 v[28:31], v[32:35], v[16:19], v[28:31]\n"
"s_waitcnt lgkmcnt(0)\n"
"s_barrier\n"
"v_xor_b32 v47, 0x3000, v60\n"
"v_xor_b32 v45, 0x1000, v60\n"
"v_xor_b32 v46, 0x2000, v60\n"
"ds_read_b128 v[40:43], v47 offset:34816\n"
"ds_read_b128 v[32:35], v45 offset:32768\n"
"ds_read_b128 v[36:39], v46 offset:33792\n"
"s_add_u32 s69, s50, 0xccc000\n"
"s_waitcnt lgkmcnt(2)\n"
"v_add_f32 v28, v28, v40\n"
"v_add_f32 v29, v29, v41\n"
"v_add_f32 v30, v30, v42\n"
"v_add_f32 v31, v31, v43\n"
"s_waitcnt lgkmcnt(0)\n"
"v_add_f32 v32, v32, v36\n"
"v_add_f32 v33, v33, v37\n"
"v_add_f32 v34, v34, v38\n"
"v_add_f32 v35, v35, v39\n"
"v_add_f32 v28, v28, v32\n"
"v_add_f32 v29, v29, v33\n"
"v_cvt_pk_bf16_f32 v46, v28, v29\n"
"v_add_f32 v30, v30, v34\n"
"v_add_f32 v31, v31, v35\n"
"v_and_b32 v44, -2, v46\n"
"v_cvt_pk_bf16_f32 v45, v30, v31\n"
"buffer_store_dwordx2 v[44:45], v1, s[12:15], s69 offen sc1\n"
"v_mov_b32 v47, v45\n"
"ds_write_b64 v62, v[46:47] offset:16392\n"
"v_add_u32 v63, 152576, v0\n"
"ds_read_b64 v[50:51], v62 offset:16384\n"
"ds_read_b64 v[48:49], v63 offset:8\n"
"ds_read_b64_tr_b16 v[32:33], v61 offset:0\n"
"ds_read_b64_tr_b16 v[34:35], v61 offset:512\n"
"ds_read_b64_tr_b16 v[36:37], v61 offset:4096\n"
"ds_read_b64_tr_b16 v[38:39], v61 offset:4608\n"
"ds_read_b64_tr_b16 v[40:41], v61 offset:8192\n"
"ds_read_b64_tr_b16 v[42:43], v61 offset:8704\n"
"ds_read_b64_tr_b16 v[44:45], v61 offset:12288\n"
"ds_read_b64_tr_b16 v[46:47], v61 offset:12800\n"
"s_waitcnt lgkmcnt(8)\n"
"v_mfma_f32_16x16x16_bf16 v[28:31], v[48:49], v[50:51], 0\n"
"v_xor_b32 v61, 96, v61\n"
"ds_read_b64_tr_b16 v[48:49], v61 offset:0\n"
"ds_read_b64_tr_b16 v[50:51], v61 offset:512\n"
"s_waitcnt vmcnt(1)\n"
"ds_read_b128 v[4:7], v60 offset:16384\n"
"ds_read_b128 v[8:11], v60 offset:17408\n"
"ds_read_b128 v[12:15], v60 offset:18432\n"
"ds_read_b128 v[16:19], v60 offset:19456\n"
"LB_20:\n"
"s_waitcnt lgkmcnt(3)\n"
"v_mfma_f32_16x16x32_bf16 v[20:23], v[32:35], v[4:7], 0\n"
"ds_read_b64_tr_b16 v[32:33], v61 offset:4096\n"
"ds_read_b64_tr_b16 v[34:35], v61 offset:4608\n"
"v_bitop3_b32 v63, 1, v4, v6 bitop3:0x10\n"
"s_waitcnt lgkmcnt(4)\n"
"v_mfma_f32_16x16x32_bf16 v[20:23], v[36:39], v[8:11], v[20:23]\n"
"ds_read_b64_tr_b16 v[36:37], v61 offset:8192\n"
"ds_read_b64_tr_b16 v[38:39], v61 offset:8704\n"
"v_bitop3_b32 v63, v63, v8, v10 bitop3:0x10\n"
"s_waitcnt lgkmcnt(5)\n"
"v_mfma_f32_16x16x32_bf16 v[20:23], v[40:43], v[12:15], v[20:23]\n"
"ds_read_b64_tr_b16 v[40:41], v61 offset:12288\n"
"ds_read_b64_tr_b16 v[42:43], v61 offset:12800\n"
"v_bitop3_b32 v63, v63, v12, v14 bitop3:0x10\n"
"s_waitcnt lgkmcnt(6)\n"
"v_mfma_f32_16x16x32_bf16 v[20:23], v[44:47], v[16:19], v[20:23]\n"
"v_xor_b32 v61, 32, v61\n"
"ds_read_b64_tr_b16 v[44:45], v61 offset:0\n"
"ds_read_b64_tr_b16 v[46:47], v61 offset:512\n"
"v_bitop3_b32 v63, v63, v16, v18 bitop3:0x10\n"
"v_cmp_eq_u32 vcc, 0, v63\n"
"v_mfma_f32_16x16x32_bf16 v[24:27], v[48:51], v[4:7], 0\n"
"ds_read_b64_tr_b16 v[48:49], v61 offset:4096\n"
"ds_read_b64_tr_b16 v[50:51], v61 offset:4608\n"
"s_cbranch_vccnz LC_21\n"
"s_waitcnt lgkmcnt(8)\n"
"v_mfma_f32_16x16x32_bf16 v[24:27], v[32:35], v[8:11], v[24:27]\n"
"ds_read_b64_tr_b16 v[32:33], v61 offset:8192\n"
"ds_read_b64_tr_b16 v[34:35], v61 offset:8704\n"
"s_waitcnt lgkmcnt(8)\n"
"v_mfma_f32_16x16x32_bf16 v[24:27], v[36:39], v[12:15], v[24:27]\n"
"ds_read_b64_tr_b16 v[36:37], v61 offset:12288\n"
"ds_read_b64_tr_b16 v[38:39], v61 offset:12800\n"
"ds_write_b128 v60, v[20:23] offset:16384\n"
"s_waitcnt lgkmcnt(9)\n"
"v_mfma_f32_16x16x32_bf16 v[24:27], v[40:43], v[16:19], v[24:27]\n"
"v_xor_b32 v61, 96, v61\n"
"ds_read_b64_tr_b16 v[40:41], v61 offset:0\n"
"ds_read_b64_tr_b16 v[42:43], v61 offset:512\n"
"s_waitcnt lgkmcnt(9)\n"
"v_mfma_f32_16x16x32_bf16 v[20:23], v[44:47], v[4:7], 0\n"
"ds_read_b64_tr_b16 v[44:45], v61 offset:4096\n"
"ds_read_b64_tr_b16 v[46:47], v61 offset:4608\n"
"s_waitcnt lgkmcnt(9)\n"
"v_mfma_f32_16x16x32_bf16 v[20:23], v[48:51], v[8:11], v[20:23]\n"
"ds_read_b64_tr_b16 v[48:49], v61 offset:8192\n"
"ds_read_b64_tr_b16 v[50:51], v61 offset:8704\n"
"s_waitcnt lgkmcnt(9)\n"
"v_mfma_f32_16x16x32_bf16 v[20:23], v[32:35], v[12:15], v[20:23]\n"
"ds_read_b64_tr_b16 v[32:33], v61 offset:12288\n"
"ds_read_b64_tr_b16 v[34:35], v61 offset:12800\n"
"v_xor_b32 v61, 32, v61\n"
"ds_write_b128 v60, v[24:27] offset:17408\n"
"s_waitcnt lgkmcnt(10)\n"
"v_mfma_f32_16x16x32_bf16 v[20:23], v[36:39], v[16:19], v[20:23]\n"
"s_waitcnt lgkmcnt(7)\n"
"v_mfma_f32_16x16x32_bf16 v[28:31], v[40:43], v[4:7], v[28:31]\n"
"s_waitcnt lgkmcnt(5)\n"
"v_mfma_f32_16x16x32_bf16 v[28:31], v[44:47], v[8:11], v[28:31]\n"
"s_waitcnt lgkmcnt(3)\n"
"v_mfma_f32_16x16x32_bf16 v[28:31], v[48:51], v[12:15], v[28:31]\n"
"s_nop 1\n"
"ds_write_b128 v60, v[20:23] offset:18432\n"
"s_waitcnt lgkmcnt(2)\n"
"v_mfma_f32_16x16x32_bf16 v[28:31], v[32:35], v[16:19], v[28:31]\n"
"s_add_u32 m0, s58, 0\n"
"s_add_u32 s69, s51, 0xac4000\n"
"buffer_load_dwordx4 v0, s[12:15], s69 offen offset:0 sc1 lds\n"
"buffer_load_dwordx4 v0, s[12:15], s69 offen offset:1024 sc1 lds\n"
"buffer_load_dwordx4 v0, s[12:15], s69 offen offset:2048 sc1 lds\n"
"buffer_load_dwordx4 v0, s[12:15], s69 offen offset:3072 sc1 lds\n"
"s_waitcnt lgkmcnt(0)\n"
"s_barrier\n"
"v_xor_b32 v47, 0x3000, v60\n"
"v_xor_b32 v45, 0x1000, v60\n"
"v_xor_b32 v46, 0x2000, v60\n"
"ds_read_b128 v[40:43], v47 offset:18432\n"
"ds_read_b128 v[32:35], v45 offset:16384\n"
"ds_read_b128 v[36:39], v46 offset:17408\n"
"s_add_u32 s69, s50, 0xcc8000\n"
"s_waitcnt lgkmcnt(2)\n"
"v_add_f32 v28, v28, v40\n"
"v_add_f32 v29, v29, v41\n"
"v_add_f32 v30, v30, v42\n"
"v_add_f32 v31, v31, v43\n"
"s_waitcnt lgkmcnt(0)\n"
"v_add_f32 v32, v32, v36\n"
"v_add_f32 v33, v33, v37\n"
"v_add_f32 v34, v34, v38\n"
"v_add_f32 v35, v35, v39\n"
"v_add_f32 v28, v28, v32\n"
"v_add_f32 v29, v29, v33\n"
"v_cvt_pk_bf16_f32 v46, v28, v29\n"
"v_add_f32 v30, v30, v34\n"
"v_add_f32 v31, v31, v35\n"
"v_and_b32 v44, -2, v46\n"
"v_cvt_pk_bf16_f32 v45, v30, v31\n"
"buffer_store_dwordx2 v[44:45], v1, s[12:15], s69 offen sc1\n"
"v_mov_b32 v47, v45\n"
"ds_write_b64 v62, v[46:47] offset:16384\n"
"v_add_u32 v63, 152576, v0\n"
"ds_read_b64 v[50:51], v62 offset:12296\n"
"ds_read_b64 v[48:49], v63 offset:8\n"
"ds_read_b64_tr_b16 v[32:33], v61 offset:0\n"
"ds_read_b64_tr_b16 v[34:35], v61 offset:512\n"
"ds_read_b64_tr_b16 v[36:37], v61 offset:4096\n"
"ds_read_b64_tr_b16 v[38:39], v61 offset:4608\n"
"ds_read_b64_tr_b16 v[40:41], v61 offset:8192\n"
"ds_read_b64_tr_b16 v[42:43], v61 offset:8704\n"
"ds_read_b64_tr_b16 v[44:45], v61 offset:12288\n"
"ds_read_b64_tr_b16 v[46:47], v61 offset:12800\n"
"s_waitcnt lgkmcnt(8)\n"
"v_mfma_f32_16x16x16_bf16 v[28:31], v[48:49], v[50:51], 0\n"
"v_xor_b32 v61, 96, v61\n"
"ds_read_b64_tr_b16 v[48:49], v61 offset:0\n"
"ds_read_b64_tr_b16 v[50:51], v61 offset:512\n"
"s_waitcnt vmcnt(1)\n"
"ds_read_b128 v[4:7], v60 offset:0\n"
"ds_read_b128 v[8:11], v60 offset:1024\n"
"ds_read_b128 v[12:15], v60 offset:2048\n"
"ds_read_b128 v[16:19], v60 offset:3072\n"
"LB_22:\n"
"s_waitcnt lgkmcnt(3)\n"
"v_mfma_f32_16x16x32_bf16 v[20:23], v[32:35], v[4:7], 0\n"
"ds_read_b64_tr_b16 v[32:33], v61 offset:4096\n"
"ds_read_b64_tr_b16 v[34:35], v61 offset:4608\n"
"v_bitop3_b32 v63, 1, v4, v6 bitop3:0x10\n"
"s_waitcnt lgkmcnt(4)\n"
"v_mfma_f32_16x16x32_bf16 v[20:23], v[36:39], v[8:11], v[20:23]\n"
"ds_read_b64_tr_b16 v[36:37], v61 offset:8192\n"
"ds_read_b64_tr_b16 v[38:39], v61 offset:8704\n"
"v_bitop3_b32 v63, v63, v8, v10 bitop3:0x10\n"
"s_waitcnt lgkmcnt(5)\n"
"v_mfma_f32_16x16x32_bf16 v[20:23], v[40:43], v[12:15], v[20:23]\n"
"ds_read_b64_tr_b16 v[40:41], v61 offset:12288\n"
"ds_read_b64_tr_b16 v[42:43], v61 offset:12800\n"
"v_bitop3_b32 v63, v63, v12, v14 bitop3:0x10\n"
"s_waitcnt lgkmcnt(6)\n"
"v_mfma_f32_16x16x32_bf16 v[20:23], v[44:47], v[16:19], v[20:23]\n"
"v_xor_b32 v61, 32, v61\n"
"ds_read_b64_tr_b16 v[44:45], v61 offset:0\n"
"ds_read_b64_tr_b16 v[46:47], v61 offset:512\n"
"v_bitop3_b32 v63, v63, v16, v18 bitop3:0x10\n"
"v_cmp_eq_u32 vcc, 0, v63\n"
"v_mfma_f32_16x16x32_bf16 v[24:27], v[48:51], v[4:7], 0\n"
"ds_read_b64_tr_b16 v[48:49], v61 offset:4096\n"
"ds_read_b64_tr_b16 v[50:51], v61 offset:4608\n"
"s_cbranch_vccnz LC_23\n"
"v_xor_b32 v63, 0x400, v62\n"
"s_waitcnt lgkmcnt(8)\n"
"v_mfma_f32_16x16x32_bf16 v[24:27], v[32:35], v[8:11], v[24:27]\n"
"ds_read_b64_tr_b16 v[32:33], v61 offset:8192\n"
"ds_read_b64_tr_b16 v[34:35], v61 offset:8704\n"
"s_waitcnt lgkmcnt(8)\n"
"v_mfma_f32_16x16x32_bf16 v[24:27], v[36:39], v[12:15], v[24:27]\n"
"ds_read_b64_tr_b16 v[36:37], v61 offset:12288\n"
"ds_read_b64_tr_b16 v[38:39], v61 offset:12800\n"
"ds_write_b128 v63, v[20:23] offset:0\n"
"s_waitcnt lgkmcnt(9)\n"
"v_mfma_f32_16x16x32_bf16 v[24:27], v[40:43], v[16:19], v[24:27]\n"
"v_xor_b32 v61, 96, v61\n"
"ds_read_b64_tr_b16 v[40:41], v61 offset:0\n"
"ds_read_b64_tr_b16 v[42:43], v61 offset:512\n"
"v_xor_b32 v63, 0x800, v62\n"
"s_waitcnt lgkmcnt(9)\n"
"v_mfma_f32_16x16x32_bf16 v[20:23], v[44:47], v[4:7], 0\n"
"ds_read_b64_tr_b16 v[44:45], v61 offset:4096\n"
"ds_read_b64_tr_b16 v[46:47], v61 offset:4608\n"
"s_add_u32 m0, s58, 0\n"
"s_add_u32 s69, s51, 0xac0000\n"
"buffer_load_dwordx4 v0, s[12:15], s69 offen offset:0 sc1 lds\n"
"buffer_load_dwordx4 v0, s[12:15], s69 offen offset:1024 sc1 lds\n"
"buffer_load_dwordx4 v0, s[12:15], s69 offen offset:2048 sc1 lds\n"
"buffer_load_dwordx4 v0, s[12:15], s69 offen offset:3072 sc1 lds\n"
"s_waitcnt lgkmcnt(9)\n"
"v_mfma_f32_16x16x32_bf16 v[20:23], v[48:51], v[8:11], v[20:23]\n"
"ds_read_b64_tr_b16 v[48:49], v61 offset:8192\n"
"ds_read_b64_tr_b16 v[50:51], v61 offset:8704\n"
"s_waitcnt lgkmcnt(9)\n"
"v_mfma_f32_16x16x32_bf16 v[20:23], v[32:35], v[12:15], v[20:23]\n"
"ds_read_b64_tr_b16 v[32:33], v61 offset:12288\n"
"ds_read_b64_tr_b16 v[34:35], v61 offset:12800\n"
"v_xor_b32 v61, 32, v61\n"
"ds_write_b128 v63, v[24:27] offset:4096\n"
"s_waitcnt lgkmcnt(10)\n"
"v_mfma_f32_16x16x32_bf16 v[20:23], v[36:39], v[16:19], v[20:23]\n"
"v_xor_b32 v63, 0xc00, v62\n"
"s_waitcnt lgkmcnt(7)\n"
"v_mfma_f32_16x16x32_bf16 v[28:31], v[40:43], v[4:7], v[28:31]\n"
"s_waitcnt lgkmcnt(5)\n"
"v_mfma_f32_16x16x32_bf16 v[28:31], v[44:47], v[8:11], v[28:31]\n"
"s_waitcnt lgkmcnt(3)\n"
"v_mfma_f32_16x16x32_bf16 v[28:31], v[48:51], v[12:15], v[28:31]\n"
"s_nop 0\n"
"ds_write_b128 v63, v[20:23] offset:8192\n"
"s_waitcnt lgkmcnt(2)\n"
"v_mfma_f32_16x16x32_bf16 v[28:31], v[32:35], v[16:19], v[28:31]\n"
"v_mov_b32 v63, 3\n"
"s_lshl_b64 exec, 1, s28\n"
"ds_write_b32 v3, v63 offset:4\n"
"s_mov_b64 exec, -1\n"
"ds_read_b32 v63, v3 offset:4\n"
"s_waitcnt lgkmcnt(0)\n"
"v_cmp_gt_u32 vcc, 3, v63\n"
"s_nop 1\n"
"s_and_b32 s68, vcc_lo, 15\n"
"s_cbranch_scc1 Lf_24\n"
"Lg_25:\n"
"ds_read_b128 v[40:43], v62 offset:8192\n"
"ds_read_b128 v[32:35], v62 offset:0\n"
"ds_read_b128 v[36:39], v62 offset:4096\n"
"s_add_u32 s69, s50, 0xcc4000\n"
"s_waitcnt lgkmcnt(2)\n"
"v_add_f32 v28, v28, v40\n"
"v_add_f32 v29, v29, v41\n"
"v_add_f32 v30, v30, v42\n"
"v_add_f32 v31, v31, v43\n"
"s_waitcnt lgkmcnt(0)\n"
"v_mov_b32 v63, 4\n"
"s_lshl_b64 exec, 1, s28\n"
"ds_write_b32 v3, v63 offset:4\n"
"s_mov_b64 exec, -1\n"
"v_add_f32 v32, v32, v36\n"
"v_add_f32 v33, v33, v37\n"
"v_add_f32 v34, v34, v38\n"
"v_add_f32 v35, v35, v39\n"
"v_add_f32 v28, v28, v32\n"
"v_add_f32 v29, v29, v33\n"
"v_cvt_pk_bf16_f32 v46, v28, v29\n"
"v_add_f32 v30, v30, v34\n"
"v_add_f32 v31, v31, v35\n"
"v_and_b32 v44, -2, v46\n"
"v_cvt_pk_bf16_f32 v45, v30, v31\n"
"buffer_store_dwordx2 v[44:45], v1, s[12:15], s69 offen sc1\n"
"v_mov_b32 v47, v45\n"
"ds_write_b64 v62, v[46:47] offset:12296\n"
"s_setprio 3\n"
"v_add_u32 v63, 152576, v0\n"
"ds_read_b64 v[50:51], v62 offset:12288\n"
"ds_read_b64 v[48:49], v63 offset:8\n"
"ds_read_b64_tr_b16 v[32:33], v61 offset:0\n"
"ds_read_b64_tr_b16 v[34:35], v61 offset:512\n"
"ds_read_b64_tr_b16 v[36:37], v61 offset:4096\n"
"ds_read_b64_tr_b16 v[38:39], v61 offset:4608\n"
"ds_read_b64_tr_b16 v[40:41], v61 offset:8192\n"
"ds_read_b64_tr_b16 v[42:43], v61 offset:8704\n"
"ds_read_b64_tr_b16 v[44:45], v61 offset:12288\n"
"ds_read_b64_tr_b16 v[46:47], v61 offset:12800\n"
"s_waitcnt lgkmcnt(8)\n"
"v_mfma_f32_16x16x16_bf16 v[28:31], v[48:49], v[50:51], 0\n"
"v_xor_b32 v61, 96, v61\n"
"ds_read_b64_tr_b16 v[48:49], v61 offset:0\n"
"ds_read_b64_tr_b16 v[50:51], v61 offset:512\n"
"s_waitcnt vmcnt(1)\n"
"ds_read_b128 v[4:7], v60 offset:0\n"
"ds_read_b128 v[8:11], v60 offset:1024\n"
"ds_read_b128 v[12:15], v60 offset:2048\n"
"ds_read_b128 v[16:19], v60 offset:3072\n"
"LB_26:\n"
"s_waitcnt lgkmcnt(3)\n"
"v_mfma_f32_16x16x32_bf16 v[20:23], v[32:35], v[4:7], 0\n"
"ds_read_b64_tr_b16 v[32:33], v61 offset:4096\n"
"ds_read_b64_tr_b16 v[34:35], v61 offset:4608\n"
"v_bitop3_b32 v63, 1, v4, v6 bitop3:0x10\n"
"s_waitcnt lgkmcnt(4)\n"
"v_mfma_f32_16x16x32_bf16 v[20:23], v[36:39], v[8:11], v[20:23]\n"
"ds_read_b64_tr_b16 v[36:37], v61 offset:8192\n"
"ds_read_b64_tr_b16 v[38:39], v61 offset:8704\n"
"v_bitop3_b32 v63, v63, v8, v10 bitop3:0x10\n"
"s_waitcnt lgkmcnt(5)\n"
"v_mfma_f32_16x16x32_bf16 v[20:23], v[40:43], v[12:15], v[20:23]\n"
"ds_read_b64_tr_b16 v[40:41], v61 offset:12288\n"
"ds_read_b64_tr_b16 v[42:43], v61 offset:12800\n"
"v_bitop3_b32 v63, v63, v12, v14 bitop3:0x10\n"
"s_waitcnt lgkmcnt(6)\n"
"v_mfma_f32_16x16x32_bf16 v[20:23], v[44:47], v[16:19], v[20:23]\n"
"v_xor_b32 v61, 32, v61\n"
"ds_read_b64_tr_b16 v[44:45], v61 offset:0\n"
"ds_read_b64_tr_b16 v[46:47], v61 offset:512\n"
"v_bitop3_b32 v63, v63, v16, v18 bitop3:0x10\n"
"v_cmp_eq_u32 vcc, 0, v63\n"
"v_mfma_f32_16x16x32_bf16 v[24:27], v[48:51], v[4:7], 0\n"
"ds_read_b64_tr_b16 v[48:49], v61 offset:4096\n"
"ds_read_b64_tr_b16 v[50:51], v61 offset:4608\n"
"s_cbranch_vccnz LC_27\n"
"s_waitcnt lgkmcnt(8)\n"
"v_mfma_f32_16x16x32_bf16 v[24:27], v[32:35], v[8:11], v[24:27]\n"
"ds_read_b64_tr_b16 v[32:33], v61 offset:8192\n"
"ds_read_b64_tr_b16 v[34:35], v61 offset:8704\n"
"s_waitcnt lgkmcnt(8)\n"
"v_mfma_f32_16x16x32_bf16 v[24:27], v[36:39], v[12:15], v[24:27]\n"
"ds_read_b64_tr_b16 v[36:37], v61 offset:12288\n"
"ds_read_b64_tr_b16 v[38:39], v61 offset:12800\n"
"ds_write_b128 v60, v[20:23] offset:0\n"
"s_waitcnt lgkmcnt(9)\n"
"v_mfma_f32_16x16x32_bf16 v[24:27], v[40:43], v[16:19], v[24:27]\n"
"v_xor_b32 v61, 96, v61\n"
"ds_read_b64_tr_b16 v[40:41], v61 offset:0\n"
"ds_read_b64_tr_b16 v[42:43], v61 offset:512\n"
"s_waitcnt lgkmcnt(9)\n"
"v_mfma_f32_16x16x32_bf16 v[20:23], v[44:47], v[4:7], 0\n"
"ds_read_b64_tr_b16 v[44:45], v61 offset:4096\n"
"ds_read_b64_tr_b16 v[46:47], v61 offset:4608\n"
"s_waitcnt lgkmcnt(9)\n"
"v_mfma_f32_16x16x32_bf16 v[20:23], v[48:51], v[8:11], v[20:23]\n"
"ds_read_b64_tr_b16 v[48:49], v61 offset:8192\n"
"ds_read_b64_tr_b16 v[50:51], v61 offset:8704\n"
"s_waitcnt lgkmcnt(9)\n"
"v_mfma_f32_16x16x32_bf16 v[20:23], v[32:35], v[12:15], v[20:23]\n"
"ds_read_b64_tr_b16 v[32:33], v61 offset:12288\n"
"ds_read_b64_tr_b16 v[34:35], v61 offset:12800\n"
"v_xor_b32 v61, 32, v61\n"
"ds_write_b128 v60, v[24:27] offset:1024\n"
"s_waitcnt lgkmcnt(10)\n"
"v_mfma_f32_16x16x32_bf16 v[20:23], v[36:39], v[16:19], v[20:23]\n"
"s_waitcnt lgkmcnt(7)\n"
"v_mfma_f32_16x16x32_bf16 v[28:31], v[40:43], v[4:7], v[28:31]\n"
"s_waitcnt lgkmcnt(5)\n"
"v_mfma_f32_16x16x32_bf16 v[28:31], v[44:47], v[8:11], v[28:31]\n"
"s_waitcnt lgkmcnt(3)\n"
"v_mfma_f32_16x16x32_bf16 v[28:31], v[48:51], v[12:15], v[28:31]\n"
"s_nop 1\n"
"ds_write_b128 v60, v[20:23] offset:2048\n"
"s_waitcnt lgkmcnt(2)\n"
"v_mfma_f32_16x16x32_bf16 v[28:31], v[32:35], v[16:19], v[28:31]\n"
"v_mov_b32 v63, 5\n"
"s_lshl_b64 exec, 1, s28\n"
"ds_write_b32 v3, v63 offset:4\n"
"s_mov_b64 exec, -1\n"
"ds_read_b32 v63, v3 offset:4\n"
"s_waitcnt lgkmcnt(0)\n"
"v_cmp_gt_u32 vcc, 5, v63\n"
"s_nop 1\n"
"s_and_b32 s68, vcc_lo, 15\n"
"s_cbranch_scc1 Lf_28\n"
"Lg_29:\n"
"v_xor_b32 v47, 0x3000, v60\n"
"v_xor_b32 v45, 0x1000, v60\n"
"v_xor_b32 v46, 0x2000, v60\n"
"ds_read_b128 v[40:43], v47 offset:2048\n"
"ds_read_b128 v[32:35], v45 offset:0\n"
"ds_read_b128 v[36:39], v46 offset:1024\n"
"s_add_u32 s69, s50, 0xcc0000\n"
"s_waitcnt lgkmcnt(2)\n"
"v_add_f32 v28, v28, v40\n"
"v_add_f32 v29, v29, v41\n"
"v_add_f32 v30, v30, v42\n"
"v_add_f32 v31, v31, v43\n"
"s_waitcnt lgkmcnt(0)\n"
"v_mov_b32 v63, 6\n"
"s_lshl_b64 exec, 1, s28\n"
"ds_write_b32 v3, v63 offset:4\n"
"s_mov_b64 exec, -1\n"
"v_add_f32 v32, v32, v36\n"
"v_add_f32 v33, v33, v37\n"
"v_add_f32 v34, v34, v38\n"
"v_add_f32 v35, v35, v39\n"
"v_add_f32 v28, v28, v32\n"
"v_add_f32 v29, v29, v33\n"
"v_cvt_pk_bf16_f32 v46, v28, v29\n"
"v_add_f32 v30, v30, v34\n"
"v_add_f32 v31, v31, v35\n"
"v_and_b32 v44, -2, v46\n"
"v_cvt_pk_bf16_f32 v45, v30, v31\n"
"buffer_store_dwordx2 v[44:45], v1, s[12:15], s69 offen sc1\n"
"v_mov_b32 v47, v45\n"
"ds_write_b64 v62, v[46:47] offset:12288\n"
"v_bfe_u32 v56, v0, 5, 2\n"
"v_bfe_u32 v57, v0, 8, 1\n"
"v_xor_b32 v56, v56, v57\n"
"v_and_b32 v57, 16, v0\n"
"v_lshl_add_u32 v56, v56, 5, v57\n"
"v_lshrrev_b32 v57, 7, v0\n"
"v_lshl_add_u32 v56, v57, 10, v56\n"
"v_xor_b32 v57, 64, v56\n"
"s_add_u32 m0, s57, 0\n"
"s_nop 0\n"
"buffer_load_dwordx4 v56, s[4:7], s56 offen lds\n"
"s_add_u32 m0, s57, 1024\n"
"s_add_u32 s71, s56, 0x2000\n"
"buffer_load_dwordx4 v57, s[4:7], s71 offen lds\n"
"v_add_u32 v63, 151552, v0\n"
"v_mov_b32 v40, 0x3f803f80\n"
"v_mov_b32 v41, 0x3f803f80\n"
"ds_read_b128 v[34:37], v63\n"
"ds_read_b64 v[38:39], v63 offset:1024\n"
"s_setprio 2\n"
"ds_read_b32 v63, v3 offset:0\n"
"s_waitcnt lgkmcnt(0)\n"
"v_cmp_gt_u32 vcc, 4, v63\n"
"s_nop 1\n"
"s_and_b32 s68, vcc_lo, 15\n"
"s_cbranch_scc1 Lf_30\n"
"Lg_31:\n"
"s_memrealtime s[70:71]\n"
"ds_read_b128 v[20:23], v62 offset:0\n"
"ds_read_b64 v[28:29], v62 offset:8192\n"
"ds_read_b32 v32, v3 offset:780\n"
"ds_read_b96 v[46:48], v62\n"
"ds_read_b32 v50, v62 offset:12\n"
"v_mov_b32 v49, s64\n"
"v_mov_b32 v51, s64\n"
"v_lshrrev_b32 v63, 1, v0\n"
"s_add_u32 s69, s54, 0x200000\n"
"s_waitcnt lgkmcnt(0)\n"
"buffer_store_dwordx4 v[46:49], v0, s[12:15], s69 offen sc1\n"
"buffer_store_dwordx2 v[50:51], v63, s[12:15], s69 offen offset:1024 sc1\n"
"s_lshl_b32 s68, s30, 10\n"
"s_add_u32 s68, s68, 0xed0000\n"
"s_lshl_b32 s69, s31, 7\n"
"s_add_u32 s68, s68, s69\n"
"s_cmp_eq_u32 s28, 0\n"
"s_cselect_b32 s69, 0, 0x80000000\n"
"v_mov_b32 v52, s64\n"
"v_mov_b32 v53, s65\n"
"v_mov_b32 v54, s70\n"
"v_mov_b32 v55, s69\n"
"s_mov_b64 exec, 1\n"
"buffer_store_dwordx3 v[52:54], v55, s[12:15], s68 offen sc1\n"
"s_mov_b64 exec, -1\n"
"v_mfma_f32_16x16x16_bf16 v[42:45], v[20:21], v[28:29], v[34:37]\n"
"s_add_u32 m0, s57, 2048\n"
"s_add_u32 s71, s56, 0x4000\n"
"buffer_load_dwordx4 v56, s[4:7], s71 offen lds\n"
"s_add_u32 m0, s57, 3072\n"
"s_add_u32 s71, s56, 0x6000\n"
"buffer_load_dwordx4 v57, s[4:7], s71 offen lds\n"
"s_add_u32 m0, s57, 4096\n"
"s_add_u32 s71, s56, 0x8000\n"
"buffer_load_dwordx4 v56, s[4:7], s71 offen lds\n"
"s_add_u32 m0, s57, 5120\n"
"s_add_u32 s71, s56, 0xa000\n"
"buffer_load_dwordx4 v57, s[4:7], s71 offen lds\n"
"s_add_u32 m0, s57, 6144\n"
"s_add_u32 s71, s56, 0xc000\n"
"buffer_load_dwordx4 v56, s[4:7], s71 offen lds\n"
"s_add_u32 m0, s57, 7168\n"
"s_add_u32 s71, s56, 0xe000\n"
"buffer_load_dwordx4 v57, s[4:7], s71 offen lds\n"
"s_add_u32 m0, s57, 8192\n"
"s_add_u32 s71, s56, 0x10000\n"
"buffer_load_dwordx4 v56, s[4:7], s71 offen lds\n"
"s_add_u32 m0, s57, 9216\n"
"s_add_u32 s71, s56, 0x12000\n"
"buffer_load_dwordx4 v57, s[4:7], s71 offen lds\n"
"s_barrier\n"
"ds_read_b128 v[24:27], v62 offset:4096\n"
"ds_read_b64 v[30:31], v62 offset:8200\n"
"ds_read_b32 v33, v3 offset:524\n"
"s_waitcnt lgkmcnt(2)\n"
"v_mfma_f32_16x16x16_bf16 v[4:7], v[26:27], v[28:29], v[34:37]\n"
"s_nop 7\n"
"v_cndmask_b32_e64 v42, v42, v4, s[36:37]\n"
"v_cndmask_b32_e64 v43, v43, v5, s[38:39]\n"
"v_cndmask_b32_e64 v44, v44, v6, s[40:41]\n"
"v_cndmask_b32_e64 v45, v45, v7, s[42:43]\n"
"v_max3_f32 v8, v42, v43, v44\n"
"v_max_f32 v8, v8, v45\n"
"v_mov_b32 v9, v8\n"
"s_nop 1\n"
"v_permlane32_swap_b32 v8, v9\n"
"v_max_f32 v8, v8, v9\n"
"v_mov_b32 v9, v8\n"
"s_nop 1\n"
"v_permlane16_swap_b32 v8, v9\n"
"v_max_f32 v8, v8, v9\n"
"v_mul_f32 v8, v8, v32\n"
"v_fma_f32 v42, v42, v32, -v8\n"
"v_fma_f32 v43, v43, v32, -v8\n"
"v_fma_f32 v44, v44, v32, -v8\n"
"v_fma_f32 v45, v45, v32, -v8\n"
"v_exp_f32 v42, v42\n"
"v_exp_f32 v43, v43\n"
"v_exp_f32 v44, v44\n"
"v_exp_f32 v45, v45\n"
"v_cvt_pk_bf16_f32 v10, v42, v43\n"
"v_cvt_pk_bf16_f32 v11, v44, v45\n"
"v_and_b32 v14, v10, v38\n"
"v_and_b32 v15, v11, v39\n"
"v_mfma_f32_16x16x16_bf16 v[4:7], v[40:41], v[10:11], 0\n"
"v_xor_b32 v12, v10, v14\n"
"v_xor_b32 v13, v11, v15\n"
"s_nop 1\n"
"v_mfma_f32_16x16x32_bf16 v[42:45], v[22:25], v[12:15], 0\n"
"s_lshl_b32 s68, s30, 10\n"
"s_add_u32 s68, s68, 0xed0000\n"
"v_bfe_u32 v52, v0, 4, 3\n"
"v_lshlrev_b32 v52, 7, v52\n"
"buffer_load_dwordx3 v[52:54], v52, s[12:15], s68 offen sc1\n"
"s_add_u32 s69, s50, 0xacc000\n"
"v_rcp_f32 v8, v4\n"
"s_nop 0\n"
"v_mul_f32 v42, v42, v8\n"
"v_mul_f32 v43, v43, v8\n"
"v_mul_f32 v44, v44, v8\n"
"v_mul_f32 v45, v45, v8\n"
"v_cvt_pk_bf16_f32 v10, v42, v43\n"
"v_cvt_pk_bf16_f32 v11, v44, v45\n"
"v_or_b32 v10, 1, v10\n"
"buffer_store_dwordx2 v[10:11], v1, s[12:15], s69 offen sc1\n"
"s_waitcnt lgkmcnt(0)\n"
"v_mfma_f32_16x16x16_bf16 v[42:45], v[26:27], v[30:31], v[34:37]\n"
"s_add_u32 m0, s57, 10240\n"
"s_add_u32 s71, s56, 0x14000\n"
"buffer_load_dwordx4 v56, s[4:7], s71 offen lds\n"
"s_add_u32 m0, s57, 11264\n"
"s_add_u32 s71, s56, 0x16000\n"
"buffer_load_dwordx4 v57, s[4:7], s71 offen lds\n"
"s_add_u32 m0, s57, 12288\n"
"s_add_u32 s71, s56, 0x18000\n"
"buffer_load_dwordx4 v56, s[4:7], s71 offen lds\n"
"s_add_u32 m0, s57, 13312\n"
"s_add_u32 s71, s56, 0x1a000\n"
"buffer_load_dwordx4 v57, s[4:7], s71 offen lds\n"
"s_add_u32 m0, s57, 14336\n"
"s_add_u32 s71, s56, 0x1c000\n"
"buffer_load_dwordx4 v56, s[4:7], s71 offen lds\n"
"s_add_u32 m0, s57, 15360\n"
"s_add_u32 s71, s56, 0x1e000\n"
"buffer_load_dwordx4 v57, s[4:7], s71 offen lds\n"
"s_waitcnt vmcnt(7)\n"
"v_xor_b32 v52, s64, v52\n"
"v_xor_b32 v53, s65, v53\n"
"v_or_b32 v52, v52, v53\n"
"v_cmp_ne_u32 vcc, 0, v52\n"
"s_nop 1\n"
"s_cbranch_vccnz Lr_32\n"
"Lk_33:\n"
"s_memrealtime s[70:71]\n"
"s_waitcnt lgkmcnt(0)\n"
"v_subrev_u32 v53, s70, v54\n"
"s_nop 1\n"
"v_max_i32_dpp v53, v53, v53 quad_perm:[1,0,3,2] row_mask:0xf bank_mask:0xf\n"
"s_nop 1\n"
"v_max_i32_dpp v53, v53, v53 quad_perm:[2,3,0,1] row_mask:0xf bank_mask:0xf\n"
"s_nop 1\n"
"v_max_i32_dpp v53, v53, v53 row_half_mirror row_mask:0xf bank_mask:0xf\n"
"s_nop 0\n"
"v_readfirstlane_b32 s68, v53\n"
"s_nop 1\n"
"s_add_i32 s68, s68, 150\n"
"s_min_i32 s68, s68, 150\n"
"s_cmp_le_i32 s68, 2\n"
"s_cbranch_scc1 La_35\n"
"s_add_u32 s62, s68, s70\n"
"s_mov_b32 s69, 0\n"
"La_34:\n"
"s_memrealtime s[70:71]\n"
"s_add_u32 s69, s69, 1\n"
"s_waitcnt lgkmcnt(0)\n"
"s_cmp_gt_u32 s69, 200\n"
"s_cbranch_scc1 La_35\n"
"s_sub_u32 s68, s70, s62\n"
"s_cmp_lt_i32 s68, 0\n"
"s_cbranch_scc1 La_34\n"
"La_35:\n"
"s_waitcnt vmcnt(0)\n"
"s_barrier\n"
"ds_read_b128 v[20:23], v62 offset:0\n"
"ds_read_b64 v[28:29], v62 offset:8192\n"
"ds_read_b32 v32, v3 offset:268\n"
"s_waitcnt lgkmcnt(2)\n"
"v_mfma_f32_16x16x16_bf16 v[4:7], v[20:21], v[30:31], v[34:37]\n"
"s_nop 7\n"
"v_cndmask_b32_e64 v42, v42, v4, s[36:37]\n"
"v_cndmask_b32_e64 v43, v43, v5, s[38:39]\n"
"v_cndmask_b32_e64 v44, v44, v6, s[40:41]\n"
"v_cndmask_b32_e64 v45, v45, v7, s[42:43]\n"
"v_max3_f32 v8, v42, v43, v44\n"
"v_max_f32 v8, v8, v45\n"
"v_mov_b32 v9, v8\n"
"s_nop 1\n"
"v_permlane32_swap_b32 v8, v9\n"
"v_max_f32 v8, v8, v9\n"
"v_mov_b32 v9, v8\n"
"s_nop 1\n"
"v_permlane16_swap_b32 v8, v9\n"
"v_max_f32 v8, v8, v9\n"
"v_mul_f32 v8, v8, v33\n"
"v_fma_f32 v42, v42, v33, -v8\n"
"v_fma_f32 v43, v43, v33, -v8\n"
"v_fma_f32 v44, v44, v33, -v8\n"
"v_fma_f32 v45, v45, v33, -v8\n"
"v_exp_f32 v42, v42\n"
"v_exp_f32 v43, v43\n"
"v_exp_f32 v44, v44\n"
"v_exp_f32 v45, v45\n"
"v_cvt_pk_bf16_f32 v10, v42, v43\n"
"v_cvt_pk_bf16_f32 v11, v44, v45\n"
"v_and_b32 v12, v10, v38\n"
"v_and_b32 v13, v11, v39\n"
"v_mfma_f32_16x16x16_bf16 v[4:7], v[40:41], v[10:11], 0\n"
"v_xor_b32 v14, v10, v12\n"
"v_xor_b32 v15, v11, v13\n"
"s_nop 1\n"
"v_mfma_f32_16x16x32_bf16 v[42:45], v[22:25], v[12:15], 0\n"
"s_add_u32 s69, s50, 0xac8000\n"
"s_nop 1\n"
"v_rcp_f32 v8, v4\n"
"s_nop 3\n"
"v_mul_f32 v42, v42, v8\n"
"v_mul_f32 v43, v43, v8\n"
"v_mul_f32 v44, v44, v8\n"
"v_mul_f32 v45, v45, v8\n"
"v_cvt_pk_bf16_f32 v10, v42, v43\n"
"v_cvt_pk_bf16_f32 v11, v44, v45\n"
"v_or_b32 v10, 1, v10\n"
"buffer_store_dwordx2 v[10:11], v1, s[12:15], s69 offen sc1\n"
"s_waitcnt lgkmcnt(0)\n"
"v_mfma_f32_16x16x16_bf16 v[42:45], v[20:21], v[28:29], v[34:37]\n"
"v_or_b32 v46, s60, v0\n"
"v_lshrrev_b32 v50, 1, v0\n"
"v_or_b32 v50, s60, v50\n"
"s_add_u32 s69, s54, 0x1f0000\n"
"buffer_load_dwordx4 v[46:49], v46, s[12:15], s69 offen sc1\n"
"buffer_load_dwordx2 v[50:51], v50, s[12:15], s69 offen offset:1024 sc1\n"
"s_barrier\n"
"ds_read_b128 v[24:27], v62 offset:4096\n"
"ds_read_b64 v[30:31], v62 offset:8200\n"
"ds_read_b32 v33, v3 offset:12\n"
"s_waitcnt lgkmcnt(2)\n"
"v_mfma_f32_16x16x16_bf16 v[4:7], v[26:27], v[28:29], v[34:37]\n"
"s_nop 7\n"
"v_cndmask_b32_e64 v42, v42, v4, s[36:37]\n"
"v_cndmask_b32_e64 v43, v43, v5, s[38:39]\n"
"v_cndmask_b32_e64 v44, v44, v6, s[40:41]\n"
"v_cndmask_b32_e64 v45, v45, v7, s[42:43]\n"
"v_max3_f32 v8, v42, v43, v44\n"
"v_max_f32 v8, v8, v45\n"
"v_mov_b32 v9, v8\n"
"s_nop 1\n"
"v_permlane32_swap_b32 v8, v9\n"
"v_max_f32 v8, v8, v9\n"
"v_mov_b32 v9, v8\n"
"s_nop 1\n"
"v_permlane16_swap_b32 v8, v9\n"
"v_max_f32 v8, v8, v9\n"
"v_mul_f32 v8, v8, v32\n"
"v_fma_f32 v42, v42, v32, -v8\n"
"v_fma_f32 v43, v43, v32, -v8\n"
"v_fma_f32 v44, v44, v32, -v8\n"
"v_fma_f32 v45, v45, v32, -v8\n"
"v_exp_f32 v42, v42\n"
"v_exp_f32 v43, v43\n"
"v_exp_f32 v44, v44\n"
"v_exp_f32 v45, v45\n"
"v_cvt_pk_bf16_f32 v10, v42, v43\n"
"v_cvt_pk_bf16_f32 v11, v44, v45\n"
"v_and_b32 v14, v10, v38\n"
"v_and_b32 v15, v11, v39\n"
"v_mfma_f32_16x16x16_bf16 v[4:7], v[40:41], v[10:11], 0\n"
"v_xor_b32 v12, v10, v14\n"
"v_xor_b32 v13, v11, v15\n"
"s_nop 1\n"
"v_mfma_f32_16x16x32_bf16 v[42:45], v[22:25], v[12:15], 0\n"
"s_add_u32 s69, s50, 0xac4000\n"
"s_nop 1\n"
"v_rcp_f32 v8, v4\n"
"s_nop 3\n"
"v_mul_f32 v42, v42, v8\n"
"v_mul_f32 v43, v43, v8\n"
"v_mul_f32 v44, v44, v8\n"
"v_mul_f32 v45, v45, v8\n"
"v_cvt_pk_bf16_f32 v10, v42, v43\n"
"v_cvt_pk_bf16_f32 v11, v44, v45\n"
"v_or_b32 v10, 1, v10\n"
"buffer_store_dwordx2 v[10:11], v1, s[12:15], s69 offen sc1\n"
"s_waitcnt lgkmcnt(0)\n"
"v_mfma_f32_16x16x16_bf16 v[42:45], v[26:27], v[30:31], v[34:37]\n"
"s_barrier\n"
"s_waitcnt vmcnt(1)\n"
"v_xor_b32 v4, s64, v49\n"
"v_xor_b32 v5, s64, v51\n"
"v_or_b32 v4, v4, v5\n"
"v_cmp_ne_u32 vcc, 0, v4\n"
"s_cmp_lg_u32 s60, 0\n"
"s_nop 0\n"
"s_cselect_b64 vcc, 0, vcc\n"
"s_cbranch_vccnz Lr_36\n"
"Lk_37:\n"
"v_min_f32 v4, s61, v34\n"
"v_min_f32 v5, s61, v35\n"
"v_min_f32 v6, s61, v36\n"
"v_min_f32 v7, s61, v37\n"
"v_mov_b32 v22, v48\n"
"v_mov_b32 v23, v50\n"
"v_mfma_f32_16x16x16_bf16 v[4:7], v[46:47], v[30:31], v[4:7]\n"
"s_nop 7\n"
"v_cndmask_b32_e64 v42, v42, v4, s[36:37]\n"
"v_cndmask_b32_e64 v43, v43, v5, s[38:39]\n"
"v_cndmask_b32_e64 v44, v44, v6, s[40:41]\n"
"v_cndmask_b32_e64 v45, v45, v7, s[42:43]\n"
"v_max3_f32 v8, v42, v43, v44\n"
"v_max_f32 v8, v8, v45\n"
"v_mov_b32 v9, v8\n"
"s_nop 1\n"
"v_permlane32_swap_b32 v8, v9\n"
"v_max_f32 v8, v8, v9\n"
"v_mov_b32 v9, v8\n"
"s_nop 1\n"
"v_permlane16_swap_b32 v8, v9\n"
"v_max_f32 v8, v8, v9\n"
"v_mul_f32 v8, v8, v33\n"
"v_fma_f32 v42, v42, v33, -v8\n"
"v_fma_f32 v43, v43, v33, -v8\n"
"v_fma_f32 v44, v44, v33, -v8\n"
"v_fma_f32 v45, v45, v33, -v8\n"
"v_exp_f32 v42, v42\n"
"v_exp_f32 v43, v43\n"
"v_exp_f32 v44, v44\n"
"v_exp_f32 v45, v45\n"
"v_cvt_pk_bf16_f32 v10, v42, v43\n"
"v_cvt_pk_bf16_f32 v11, v44, v45\n"
"v_and_b32 v12, v10, v38\n"
"v_and_b32 v13, v11, v39\n"
"v_mfma_f32_16x16x16_bf16 v[4:7], v[40:41], v[10:11], 0\n"
"v_xor_b32 v14, v10, v12\n"
"v_xor_b32 v15, v11, v13\n"
"s_nop 1\n"
"v_mfma_f32_16x16x32_bf16 v[42:45], v[22:25], v[12:15], 0\n"
"s_add_u32 s69, s50, 0xac0000\n"
"s_nop 1\n"
"v_rcp_f32 v8, v4\n"
"s_nop 3\n"
"v_mul_f32 v42, v42, v8\n"
"v_mul_f32 v43, v43, v8\n"
"v_mul_f32 v44, v44, v8\n"
"v_mul_f32 v45, v45, v8\n"
"v_cvt_pk_bf16_f32 v10, v42, v43\n"
"v_cvt_pk_bf16_f32 v11, v44, v45\n"
"v_or_b32 v10, 1, v10\n"
"buffer_store_dwordx2 v[10:11], v1, s[12:15], s69 offen sc1\n"
"v_mov_b32 v56, 0x3f803f80\n"
"v_mov_b32 v57, 0x3f803f80\n"
"v_mov_b32 v58, 0x3f803f80\n"
"v_mov_b32 v59, 0x3f803f80\n"
"v_lshrrev_b32 v61, 8, v0\n"
"v_lshlrev_b32 v61, 6, v61\n"
"v_add_u32 v61, 154624, v61\n"
"s_sleep 9\n"
"s_add_u32 m0, s58, 49152\n"
"s_add_u32 s69, s51, 0xccc000\n"
"buffer_load_dwordx4 v0, s[12:15], s69 offen offset:0 sc1 lds\n"
"buffer_load_dwordx4 v0, s[12:15], s69 offen offset:1024 sc1 lds\n"
"buffer_load_dwordx4 v0, s[12:15], s69 offen offset:2048 sc1 lds\n"
"buffer_load_dwordx4 v0, s[12:15], s69 offen offset:3072 sc1 lds\n"
"s_barrier\n"
"s_setprio 1\n"
"s_waitcnt vmcnt(0)\n"
"ds_read_b128 v[32:35], v60 offset:49152\n"
"ds_read_b128 v[36:39], v60 offset:50176\n"
"ds_read_b128 v[40:43], v60 offset:51200\n"
"ds_read_b128 v[44:47], v60 offset:52224\n"
"s_waitcnt lgkmcnt(2)\n"
"v_mfma_f32_16x16x32_bf16 v[48:51], v[56:59], v[32:35], 0\n"
"v_bitop3_b32 v63, 1, v32, v34 bitop3:0x80\n"
"v_bitop3_b32 v63, v63, v36, v38 bitop3:0x80\n"
"v_mfma_f32_16x16x32_bf16 v[52:55], v[32:35], v[32:35], 0\n"
"v_mfma_f32_16x16x32_bf16 v[48:51], v[56:59], v[36:39], v[48:51]\n"
"v_mfma_f32_16x16x32_bf16 v[52:55], v[36:39], v[36:39], v[52:55]\n"
"s_waitcnt lgkmcnt(1)\n"
"v_mfma_f32_16x16x32_bf16 v[48:51], v[56:59], v[40:43], v[48:51]\n"
"v_mfma_f32_16x16x32_bf16 v[52:55], v[40:43], v[40:43], v[52:55]\n"
"s_waitcnt lgkmcnt(0)\n"
"v_mfma_f32_16x16x32_bf16 v[48:51], v[56:59], v[44:47], v[48:51]\n"
"v_bitop3_b32 v63, v63, v40, v42 bitop3:0x80\n"
"v_bitop3_b32 v63, v63, v44, v46 bitop3:0x80\n"
"v_mfma_f32_16x16x32_bf16 v[52:55], v[44:47], v[44:47], v[52:55]\n"
"v_cmp_eq_u32 vcc, 0, v63\n"
"v_mfma_f32_16x16x32_bf16 v[4:7], v[68:71], v[32:35], 0\n"
"v_mfma_f32_16x16x32_bf16 v[8:11], v[32:35], v[72:75], 0\n"
"v_mov_b32 v16, 6\n"
"v_mfma_f32_16x16x32_bf16 v[12:15], v[64:67], v[32:35], 0\n"
"s_cbranch_vccnz LQ_38\n"
"LP_39:\n"
"s_lshl_b64 exec, 1, s28\n"
"ds_write_b32 v3, v16 offset:0\n"
"s_mov_b64 exec, -1\n"
"v_mfma_f32_16x16x32_bf16 v[4:7], v[80:83], v[36:39], v[4:7]\n"
"ds_read_b32 v17, v3 offset:0\n"
"v_cndmask_b32_e64 v52, v52, v53, s[44:45]\n"
"v_cndmask_b32_e64 v54, v54, v55, s[44:45]\n"
"v_mfma_f32_16x16x32_bf16 v[8:11], v[36:39], v[84:87], v[8:11]\n"
"v_cndmask_b32_e64 v49, v52, v54, s[46:47]\n"
"v_and_b32 v48, -2, v48\n"
"v_xor_b32 v2, 0x1000, v60\n"
"v_mfma_f32_16x16x32_bf16 v[12:15], v[76:79], v[36:39], v[12:15]\n"
"s_waitcnt lgkmcnt(0)\n"
"v_cmp_gt_u32 vcc, 6, v17\n"
"s_nop 1\n"
"s_and_b32 s68, vcc_lo, 15\n"
"s_cbranch_scc1 Lf_40\n"
"Lg_41:\n"
"ds_read_b128 v[32:35], v2 offset:49152\n"
"ds_read_b128 v[36:39], v2 offset:50176\n"
"v_add_u32 v63, s53, v3\n"
"v_mfma_f32_16x16x32_bf16 v[4:7], v[92:95], v[40:43], v[4:7]\n"
"s_mov_b64 exec, s[48:49]\n"
"ds_write_b64 v63, v[48:49] offset:6656\n"
"s_mov_b64 exec, -1\n"
"v_mfma_f32_16x16x32_bf16 v[8:11], v[40:43], v[96:99], v[8:11]\n"
"v_mfma_f32_16x16x32_bf16 v[12:15], v[88:91], v[40:43], v[12:15]\n"
"ds_read_b128 v[40:43], v2 offset:51200\n"
"v_mfma_f32_16x16x32_bf16 v[4:7], v[104:107], v[44:47], v[4:7]\n"
"v_mfma_f32_16x16x32_bf16 v[8:11], v[44:47], v[108:111], v[8:11]\n"
"v_mfma_f32_16x16x32_bf16 v[12:15], v[100:103], v[44:47], v[12:15]\n"
"ds_read_b128 v[44:47], v2 offset:52224\n"
"s_add_u32 m0, s58, 32768\n"
"s_add_u32 s69, s51, 0xcc8000\n"
"buffer_load_dwordx4 v0, s[12:15], s69 offen offset:0 sc1 lds\n"
"buffer_load_dwordx4 v0, s[12:15], s69 offen offset:1024 sc1 lds\n"
"buffer_load_dwordx4 v0, s[12:15], s69 offen offset:2048 sc1 lds\n"
"buffer_load_dwordx4 v0, s[12:15], s69 offen offset:3072 sc1 lds\n"
"s_waitcnt lgkmcnt(4)\n"
"v_mfma_f32_16x16x32_bf16 v[4:7], v[116:119], v[32:35], v[4:7]\n"
"ds_read_b128 v[48:51], v3 offset:6656\n"
"ds_read_b128 v[52:55], v3 offset:6912\n"
"v_mfma_f32_16x16x32_bf16 v[8:11], v[32:35], v[120:123], v[8:11]\n"
"v_xor_b32 v2, 0x2000, v60\n"
"v_mfma_f32_16x16x32_bf16 v[12:15], v[112:115], v[32:35], v[12:15]\n"
"ds_read_b128 v[32:35], v2 offset:49152\n"
"s_waitcnt lgkmcnt(6)\n"
"v_mfma_f32_16x16x32_bf16 v[4:7], v[128:131], v[36:39], v[4:7]\n"
"v_mfma_f32_16x16x32_bf16 v[8:11], v[36:39], v[132:135], v[8:11]\n"
"s_waitcnt lgkmcnt(1)\n"
"v_mfma_f32_16x16x32_bf16 v[12:15], v[124:127], v[36:39], v[12:15]\n"
"ds_read_b128 v[36:39], v2 offset:50176\n"
"v_bitop3_b32 v63, 1, v48, v50 bitop3:0x10\n"
"v_bitop3_b32 v63, v63, v52, v54 bitop3:0x10\n"
"v_mfma_f32_16x16x32_bf16 v[4:7], v[140:143], v[40:43], v[4:7]\n"
"v_cmp_eq_u32 vcc, 0, v63\n"
"s_nop 1\n"
"s_cbranch_vccnz LQ_42\n"
"LP_43:\n"
"v_mfma_f32_16x16x32_bf16 v[8:11], v[40:43], v[144:147], v[8:11]\n"
"v_add_f32 v48, v48, v50\n"
"v_add_f32 v52, v52, v54\n"
"v_mfma_f32_16x16x32_bf16 v[12:15], v[136:139], v[40:43], v[12:15]\n"
"ds_read_b128 v[40:43], v2 offset:51200\n"
"v_mfma_f32_16x16x32_bf16 v[4:7], v[152:155], v[44:47], v[4:7]\n"
"v_add_f32 v49, v49, v51\n"
"v_add_f32 v53, v53, v55\n"
"v_mfma_f32_16x16x32_bf16 v[8:11], v[44:47], v[156:159], v[8:11]\n"
"v_add_f32 v52, v52, v48\n"
"v_add_f32 v53, v53, v49\n"
"v_mfma_f32_16x16x32_bf16 v[12:15], v[148:151], v[44:47], v[12:15]\n"
"ds_read_b128 v[44:47], v2 offset:52224\n"
"s_waitcnt lgkmcnt(3)\n"
"v_mfma_f32_16x16x32_bf16 v[4:7], v[164:167], v[32:35], v[4:7]\n"
"v_mul_f32 v48, s72, v52\n"
"v_mul_f32 v52, 0xba000000, v52\n"
"v_mfma_f32_16x16x32_bf16 v[8:11], v[32:35], v[168:171], v[8:11]\n"
"v_xor_b32 v2, 0x3000, v60\n"
"v_mul_f32 v49, v48, v48\n"
"v_mfma_f32_16x16x32_bf16 v[12:15], v[160:163], v[32:35], v[12:15]\n"
"ds_read_b128 v[32:35], v2 offset:49152\n"
"s_waitcnt lgkmcnt(3)\n"
"v_mfma_f32_16x16x32_bf16 v[4:7], v[176:179], v[36:39], v[4:7]\n"
"v_fma_f32 v54, -v53, s72, -v49\n"
"v_cvt_pk_bf16_f32 v53, v52, v52\n"
"v_mfma_f32_16x16x32_bf16 v[8:11], v[36:39], v[180:183], v[8:11]\n"
"v_max_f32 v54, 0, v54\n"
"v_and_b32 v53, 0xffff0000, v53\n"
"v_mfma_f32_16x16x32_bf16 v[12:15], v[172:175], v[36:39], v[12:15]\n"
"ds_read_b128 v[36:39], v2 offset:50176\n"
"s_waitcnt lgkmcnt(3)\n"
"v_mfma_f32_16x16x32_bf16 v[4:7], v[188:191], v[40:43], v[4:7]\n"
"v_add_f32 v54, 0x3727c5ac, v54\n"
"v_sub_f32 v55, v52, v53\n"
"v_mfma_f32_16x16x32_bf16 v[8:11], v[40:43], v[192:195], v[8:11]\n"
"v_rsq_f32 v1, v54\n"
"v_cvt_pk_bf16_f32 v53, v52, v55\n"
"v_mfma_f32_16x16x32_bf16 v[12:15], v[184:187], v[40:43], v[12:15]\n"
"ds_read_b128 v[40:43], v2 offset:51200\n"
"s_waitcnt lgkmcnt(3)\n"
"v_mfma_f32_16x16x32_bf16 v[4:7], v[200:203], v[44:47], v[4:7]\n"
"v_cvt_pk_bf16_f32 v52, v52, v55\n"
"v_mul_f32 v55, s73, v1\n"
"v_mfma_f32_16x16x32_bf16 v[8:11], v[44:47], v[204:207], v[8:11]\n"
"s_mov_b64 exec, 0xffff\n"
"ds_write2_b32 v3, v1, v55 offset0:194 offset1:195\n"
"s_mov_b64 exec, -1\n"
"v_add_u32 v63, s52, v3\n"
"v_mfma_f32_16x16x32_bf16 v[12:15], v[196:199], v[44:47], v[12:15]\n"
"ds_read_b128 v[44:47], v2 offset:52224\n"
"ds_read_b128 v[48:51], v63\n"
"ds_read_b64 v[54:55], v63 offset:256\n"
"s_waitcnt lgkmcnt(6)\n"
"v_mfma_f32_16x16x32_bf16 v[4:7], v[212:215], v[32:35], v[4:7]\n"
"v_mfma_f32_16x16x32_bf16 v[8:11], v[32:35], v[216:219], v[8:11]\n"
"v_mfma_f32_16x16x32_bf16 v[12:15], v[208:211], v[32:35], v[12:15]\n"
"s_waitcnt lgkmcnt(5)\n"
"v_mfma_f32_16x16x32_bf16 v[4:7], v[224:227], v[36:39], v[4:7]\n"
"ds_read2_b32 v[16:17], v61 offset0:194 offset1:198\n"
"ds_read2_b32 v[18:19], v61 offset0:202 offset1:206\n"
"v_mfma_f32_16x16x32_bf16 v[8:11], v[36:39], v[228:231], v[8:11]\n"
"v_mfma_f32_16x16x32_bf16 v[12:15], v[220:223], v[36:39], v[12:15]\n"
"s_waitcnt lgkmcnt(6)\n"
"v_mfma_f32_16x16x32_bf16 v[4:7], v[236:239], v[40:43], v[4:7]\n"
"v_mfma_f32_16x16x32_bf16 v[8:11], v[40:43], v[240:243], v[8:11]\n"
"v_mfma_f32_16x16x32_bf16 v[12:15], v[232:235], v[40:43], v[12:15]\n"
"ds_read_b32 v20, v3 offset:4\n"
"s_waitcnt lgkmcnt(5)\n"
"s_waitcnt lgkmcnt(3)\n"
"v_mfma_f32_16x16x16_bf16 v[4:7], v[50:51], v[52:53], v[4:7]\n"
"v_mfma_f32_16x16x16_bf16 v[8:11], v[52:53], v[54:55], v[8:11]\n"
"v_mfma_f32_16x16x16_bf16 v[12:15], v[48:49], v[52:53], v[12:15]\n"
"v_mfma_f32_16x16x32_bf16 v[4:7], v[248:251], v[44:47], v[4:7]\n"
"v_mfma_f32_16x16x32_bf16 v[8:11], v[44:47], v[252:255], v[8:11]\n"
"v_mfma_f32_16x16x32_bf16 v[12:15], v[244:247], v[44:47], v[12:15]\n"
"s_waitcnt vmcnt(0)\n"
"ds_read_b128 v[32:35], v60 offset:32768\n"
"ds_read_b128 v[36:39], v60 offset:33792\n"
"ds_read_b128 v[40:43], v60 offset:34816\n"
"ds_read_b128 v[44:47], v60 offset:35840\n"
"s_waitcnt lgkmcnt(4)\n"
"v_cmp_gt_u32 vcc, 10, v20\n"
"s_nop 1\n"
"s_and_b32 s68, vcc_lo, 15\n"
"s_cbranch_scc1 Lf_44\n"
"Lg_45:\n"
"s_waitcnt lgkmcnt(2)\n"
"v_mfma_f32_16x16x32_bf16 v[48:51], v[56:59], v[32:35], 0\n"
"v_bitop3_b32 v63, 1, v32, v34 bitop3:0x80\n"
"v_bitop3_b32 v63, v63, v36, v38 bitop3:0x80\n"
"v_mfma_f32_16x16x32_bf16 v[52:55], v[32:35], v[32:35], 0\n"
"v_mul_f32 v4, v4, v1\n"
"v_mul_f32 v5, v5, v1\n"
"v_mfma_f32_16x16x32_bf16 v[48:51], v[56:59], v[36:39], v[48:51]\n"
"v_mul_f32 v6, v6, v1\n"
"v_mul_f32 v7, v7, v1\n"
"v_mfma_f32_16x16x32_bf16 v[52:55], v[36:39], v[36:39], v[52:55]\n"
"v_cvt_pk_bf16_f32 v4, v4, v5\n"
"v_cvt_pk_bf16_f32 v5, v6, v7\n"
"s_waitcnt lgkmcnt(1)\n"
"v_mfma_f32_16x16x32_bf16 v[48:51], v[56:59], v[40:43], v[48:51]\n"
"v_mul_f32 v8, v8, v16\n"
"v_mul_f32 v9, v9, v17\n"
"v_mfma_f32_16x16x32_bf16 v[52:55], v[40:43], v[40:43], v[52:55]\n"
"v_mul_f32 v10, v10, v18\n"
"v_mul_f32 v11, v11, v19\n"
"s_waitcnt lgkmcnt(0)\n"
"v_mfma_f32_16x16x32_bf16 v[48:51], v[56:59], v[44:47], v[48:51]\n"
"v_cvt_pk_bf16_f32 v6, v8, v9\n"
"v_cvt_pk_bf16_f32 v7, v10, v11\n"
"v_mfma_f32_16x16x32_bf16 v[52:55], v[44:47], v[44:47], v[52:55]\n"
"ds_write_b128 v62, v[4:7] offset:0\n"
"v_cvt_pk_bf16_f32 v12, v12, v13\n"
"v_cvt_pk_bf16_f32 v13, v14, v15\n"
"v_mfma_f32_16x16x32_bf16 v[24:27], v[68:71], v[32:35], 0\n"
"ds_write_b64 v62, v[12:13] offset:8192\n"
"v_bitop3_b32 v63, v63, v40, v42 bitop3:0x80\n"
"v_bitop3_b32 v63, v63, v44, v46 bitop3:0x80\n"
"v_mfma_f32_16x16x32_bf16 v[20:23], v[32:35], v[72:75], 0\n"
"v_cmp_eq_u32 vcc, 0, v63\n"
"v_mfma_f32_16x16x32_bf16 v[28:31], v[64:67], v[32:35], 0\n"
"s_nop 0\n"
"s_cbranch_vccnz LQ_46\n"
"LP_47:\n"
"s_add_u32 m0, s58, 16384\n"
"s_add_u32 s69, s51, 0xcc4000\n"
"buffer_load_dwordx4 v0, s[12:15], s69 offen offset:0 sc1 lds\n"
"buffer_load_dwordx4 v0, s[12:15], s69 offen offset:1024 sc1 lds\n"
"buffer_load_dwordx4 v0, s[12:15], s69 offen offset:2048 sc1 lds\n"
"buffer_load_dwordx4 v0, s[12:15], s69 offen offset:3072 sc1 lds\n"
"v_mov_b32 v18, 7\n"
"v_mfma_f32_16x16x32_bf16 v[24:27], v[80:83], v[36:39], v[24:27]\n"
"s_lshl_b64 exec, 1, s28\n"
"ds_write_b32 v3, v18 offset:0\n"
"s_mov_b64 exec, -1\n"
"v_cndmask_b32_e64 v52, v52, v53, s[44:45]\n"
"v_cndmask_b32_e64 v54, v54, v55, s[44:45]\n"
"v_mfma_f32_16x16x32_bf16 v[20:23], v[36:39], v[84:87], v[20:23]\n"
"ds_read_b32 v17, v3 offset:0\n"
"v_cndmask_b32_e64 v49, v52, v54, s[46:47]\n"
"v_and_b32 v48, -2, v48\n"
"v_xor_b32 v2, 0x1000, v60\n"
"v_mfma_f32_16x16x32_bf16 v[28:31], v[76:79], v[36:39], v[28:31]\n"
"s_waitcnt lgkmcnt(0)\n"
"v_cmp_gt_u32 vcc, 7, v17\n"
"s_nop 1\n"
"s_and_b32 s68, vcc_lo, 15\n"
"s_cbranch_scc1 Lf_48\n"
"Lg_49:\n"
"ds_read_b128 v[32:35], v2 offset:32768\n"
"ds_read_b128 v[36:39], v2 offset:33792\n"
"v_add_u32 v63, s53, v3\n"
"v_mfma_f32_16x16x32_bf16 v[24:27], v[92:95], v[40:43], v[24:27]\n"
"s_mov_b64 exec, s[48:49]\n"
"ds_write_b64 v63, v[48:49] offset:6144\n"
"s_mov_b64 exec, -1\n"
"v_mfma_f32_16x16x32_bf16 v[20:23], v[40:43], v[96:99], v[20:23]\n"
"v_mfma_f32_16x16x32_bf16 v[28:31], v[88:91], v[40:43], v[28:31]\n"
"ds_read_b128 v[40:43], v2 offset:34816\n"
"v_mfma_f32_16x16x32_bf16 v[24:27], v[104:107], v[44:47], v[24:27]\n"
"v_mfma_f32_16x16x32_bf16 v[20:23], v[44:47], v[108:111], v[20:23]\n"
"v_mfma_f32_16x16x32_bf16 v[28:31], v[100:103], v[44:47], v[28:31]\n"
"ds_read_b128 v[44:47], v2 offset:35840\n"
"s_waitcnt lgkmcnt(4)\n"
"v_mfma_f32_16x16x32_bf16 v[24:27], v[116:119], v[32:35], v[24:27]\n"
"ds_read_b128 v[48:51], v3 offset:6144\n"
"ds_read_b128 v[52:55], v3 offset:6400\n"
"v_mfma_f32_16x16x32_bf16 v[20:23], v[32:35], v[120:123], v[20:23]\n"
"v_xor_b32 v2, 0x2000, v60\n"
"v_mfma_f32_16x16x32_bf16 v[28:31], v[112:115], v[32:35], v[28:31]\n"
"ds_read_b128 v[32:35], v2 offset:32768\n"
"s_waitcnt lgkmcnt(6)\n"
"v_mfma_f32_16x16x32_bf16 v[24:27], v[128:131], v[36:39], v[24:27]\n"
"v_mfma_f32_16x16x32_bf16 v[20:23], v[36:39], v[132:135], v[20:23]\n"
"s_waitcnt lgkmcnt(1)\n"
"v_mfma_f32_16x16x32_bf16 v[28:31], v[124:127], v[36:39], v[28:31]\n"
"ds_read_b128 v[36:39], v2 offset:33792\n"
"v_bitop3_b32 v63, 1, v48, v50 bitop3:0x10\n"
"v_bitop3_b32 v63, v63, v52, v54 bitop3:0x10\n"
"v_mfma_f32_16x16x32_bf16 v[24:27], v[140:143], v[40:43], v[24:27]\n"
"v_cmp_eq_u32 vcc, 0, v63\n"
"s_nop 1\n"
"s_cbranch_vccnz LQ_50\n"
"LP_51:\n"
"v_mfma_f32_16x16x32_bf16 v[20:23], v[40:43], v[144:147], v[20:23]\n"
"v_add_f32 v48, v48, v50\n"
"v_add_f32 v52, v52, v54\n"
"v_mfma_f32_16x16x32_bf16 v[28:31], v[136:139], v[40:43], v[28:31]\n"
"ds_read_b128 v[40:43], v2 offset:34816\n"
"v_mfma_f32_16x16x32_bf16 v[24:27], v[152:155], v[44:47], v[24:27]\n"
"v_add_f32 v49, v49, v51\n"
"v_add_f32 v53, v53, v55\n"
"v_mfma_f32_16x16x32_bf16 v[20:23], v[44:47], v[156:159], v[20:23]\n"
"v_add_f32 v52, v52, v48\n"
"v_add_f32 v53, v53, v49\n"
"v_mfma_f32_16x16x32_bf16 v[28:31], v[148:151], v[44:47], v[28:31]\n"
"ds_read_b128 v[44:47], v2 offset:35840\n"
"s_waitcnt lgkmcnt(3)\n"
"v_mfma_f32_16x16x32_bf16 v[24:27], v[164:167], v[32:35], v[24:27]\n"
"v_mul_f32 v48, s72, v52\n"
"v_mul_f32 v52, 0xba000000, v52\n"
"v_mfma_f32_16x16x32_bf16 v[20:23], v[32:35], v[168:171], v[20:23]\n"
"v_xor_b32 v2, 0x3000, v60\n"
"v_mul_f32 v49, v48, v48\n"
"v_mfma_f32_16x16x32_bf16 v[28:31], v[160:163], v[32:35], v[28:31]\n"
"ds_read_b128 v[32:35], v2 offset:32768\n"
"s_waitcnt lgkmcnt(3)\n"
"v_mfma_f32_16x16x32_bf16 v[24:27], v[176:179], v[36:39], v[24:27]\n"
"v_fma_f32 v54, -v53, s72, -v49\n"
"v_cvt_pk_bf16_f32 v53, v52, v52\n"
"v_mfma_f32_16x16x32_bf16 v[20:23], v[36:39], v[180:183], v[20:23]\n"
"v_max_f32 v54, 0, v54\n"
"v_and_b32 v53, 0xffff0000, v53\n"
"v_mfma_f32_16x16x32_bf16 v[28:31], v[172:175], v[36:39], v[28:31]\n"
"ds_read_b128 v[36:39], v2 offset:33792\n"
"s_waitcnt lgkmcnt(3)\n"
"v_mfma_f32_16x16x32_bf16 v[24:27], v[188:191], v[40:43], v[24:27]\n"
"v_add_f32 v54, 0x3727c5ac, v54\n"
"v_sub_f32 v55, v52, v53\n"
"v_mfma_f32_16x16x32_bf16 v[20:23], v[40:43], v[192:195], v[20:23]\n"
"v_rsq_f32 v1, v54\n"
"v_cvt_pk_bf16_f32 v53, v52, v55\n"
"v_mfma_f32_16x16x32_bf16 v[28:31], v[184:187], v[40:43], v[28:31]\n"
"ds_read_b128 v[40:43], v2 offset:34816\n"
"s_waitcnt lgkmcnt(3)\n"
"v_mfma_f32_16x16x32_bf16 v[24:27], v[200:203], v[44:47], v[24:27]\n"
"v_cvt_pk_bf16_f32 v52, v52, v55\n"
"v_mul_f32 v55, s73, v1\n"
"v_mfma_f32_16x16x32_bf16 v[20:23], v[44:47], v[204:207], v[20:23]\n"
"s_mov_b64 exec, 0xffff\n"
"ds_write2_b32 v3, v1, v55 offset0:130 offset1:131\n"
"s_mov_b64 exec, -1\n"
"v_add_u32 v63, s52, v3\n"
"v_mfma_f32_16x16x32_bf16 v[28:31], v[196:199], v[44:47], v[28:31]\n"
"ds_read_b128 v[44:47], v2 offset:35840\n"
"ds_read_b128 v[48:51], v63\n"
"ds_read_b64 v[54:55], v63 offset:256\n"
"s_waitcnt lgkmcnt(6)\n"
"v_mfma_f32_16x16x32_bf16 v[24:27], v[212:215], v[32:35], v[24:27]\n"
"v_mfma_f32_16x16x32_bf16 v[20:23], v[32:35], v[216:219], v[20:23]\n"
"v_mfma_f32_16x16x32_bf16 v[28:31], v[208:211], v[32:35], v[28:31]\n"
"s_waitcnt lgkmcnt(5)\n"
"v_mfma_f32_16x16x32_bf16 v[24:27], v[224:227], v[36:39], v[24:27]\n"
"ds_read2_b32 v[16:17], v61 offset0:130 offset1:134\n"
"ds_read2_b32 v[18:19], v61 offset0:138 offset1:142\n"
"v_mfma_f32_16x16x32_bf16 v[20:23], v[36:39], v[228:231], v[20:23]\n"
"v_mfma_f32_16x16x32_bf16 v[28:31], v[220:223], v[36:39], v[28:31]\n"
"s_waitcnt lgkmcnt(6)\n"
"v_mfma_f32_16x16x32_bf16 v[24:27], v[236:239], v[40:43], v[24:27]\n"
"v_mfma_f32_16x16x32_bf16 v[20:23], v[40:43], v[240:243], v[20:23]\n"
"v_mfma_f32_16x16x32_bf16 v[28:31], v[232:235], v[40:43], v[28:31]\n"
"s_waitcnt lgkmcnt(4)\n"
"s_waitcnt lgkmcnt(2)\n"
"v_mfma_f32_16x16x16_bf16 v[24:27], v[50:51], v[52:53], v[24:27]\n"
"v_mfma_f32_16x16x16_bf16 v[20:23], v[52:53], v[54:55], v[20:23]\n"
"v_mfma_f32_16x16x16_bf16 v[28:31], v[48:49], v[52:53], v[28:31]\n"
"v_mfma_f32_16x16x32_bf16 v[24:27], v[248:251], v[44:47], v[24:27]\n"
"v_mfma_f32_16x16x32_bf16 v[20:23], v[44:47], v[252:255], v[20:23]\n"
"v_mfma_f32_16x16x32_bf16 v[28:31], v[244:247], v[44:47], v[28:31]\n"
"s_waitcnt vmcnt(0)\n"
"ds_read_b128 v[32:35], v60 offset:16384\n"
"ds_read_b128 v[36:39], v60 offset:17408\n"
"ds_read_b128 v[40:43], v60 offset:18432\n"
"ds_read_b128 v[44:47], v60 offset:19456\n"
"s_waitcnt lgkmcnt(4)\n"
"s_waitcnt lgkmcnt(2)\n"
"v_mfma_f32_16x16x32_bf16 v[48:51], v[56:59], v[32:35], 0\n"
"v_bitop3_b32 v63, 1, v32, v34 bitop3:0x80\n"
"v_bitop3_b32 v63, v63, v36, v38 bitop3:0x80\n"
"v_mfma_f32_16x16x32_bf16 v[52:55], v[32:35], v[32:35], 0\n"
"v_mul_f32 v24, v24, v1\n"
"v_mul_f32 v25, v25, v1\n"
"v_mfma_f32_16x16x32_bf16 v[48:51], v[56:59], v[36:39], v[48:51]\n"
"v_mul_f32 v26, v26, v1\n"
"v_mul_f32 v27, v27, v1\n"
"v_mfma_f32_16x16x32_bf16 v[52:55], v[36:39], v[36:39], v[52:55]\n"
"v_mul_f32 v20, v20, v16\n"
"v_mul_f32 v21, v21, v17\n"
"s_waitcnt lgkmcnt(1)\n"
"v_mfma_f32_16x16x32_bf16 v[48:51], v[56:59], v[40:43], v[48:51]\n"
"v_mul_f32 v22, v22, v18\n"
"v_mul_f32 v23, v23, v19\n"
"v_mfma_f32_16x16x32_bf16 v[52:55], v[40:43], v[40:43], v[52:55]\n"
"v_cvt_pk_bf16_f32 v20, v20, v21\n"
"v_cvt_pk_bf16_f32 v21, v22, v23\n"
"s_waitcnt lgkmcnt(0)\n"
"v_mfma_f32_16x16x32_bf16 v[48:51], v[56:59], v[44:47], v[48:51]\n"
"v_cvt_pk_bf16_f32 v22, v24, v25\n"
"v_cvt_pk_bf16_f32 v23, v26, v27\n"
"v_mfma_f32_16x16x32_bf16 v[52:55], v[44:47], v[44:47], v[52:55]\n"
"ds_write_b128 v62, v[20:23] offset:4096\n"
"v_cvt_pk_bf16_f32 v28, v28, v29\n"
"v_cvt_pk_bf16_f32 v29, v30, v31\n"
"v_mfma_f32_16x16x32_bf16 v[4:7], v[68:71], v[32:35], 0\n"
"ds_write_b64 v62, v[28:29] offset:8200\n"
"v_bitop3_b32 v63, v63, v40, v42 bitop3:0x80\n"
"v_bitop3_b32 v63, v63, v44, v46 bitop3:0x80\n"
"v_mfma_f32_16x16x32_bf16 v[8:11], v[32:35], v[72:75], 0\n"
"v_cmp_eq_u32 vcc, 0, v63\n"
"v_mfma_f32_16x16x32_bf16 v[12:15], v[64:67], v[32:35], 0\n"
"s_nop 0\n"
"s_cbranch_vccnz LQ_52\n"
"LP_53:\n"
"v_mfma_f32_16x16x32_bf16 v[4:7], v[80:83], v[36:39], v[4:7]\n"
"v_cndmask_b32_e64 v52, v52, v53, s[44:45]\n"
"v_cndmask_b32_e64 v54, v54, v55, s[44:45]\n"
"v_mfma_f32_16x16x32_bf16 v[8:11], v[36:39], v[84:87], v[8:11]\n"
"v_cndmask_b32_e64 v49, v52, v54, s[46:47]\n"
"v_and_b32 v48, -2, v48\n"
"v_xor_b32 v2, 0x1000, v60\n"
"v_mfma_f32_16x16x32_bf16 v[12:15], v[76:79], v[36:39], v[12:15]\n"
"s_waitcnt lgkmcnt(0)\n"
"s_barrier\n"
"ds_read_b128 v[32:35], v2 offset:16384\n"
"ds_read_b128 v[36:39], v2 offset:17408\n"
"v_add_u32 v63, s53, v3\n"
"v_mfma_f32_16x16x32_bf16 v[4:7], v[92:95], v[40:43], v[4:7]\n"
"s_mov_b64 exec, s[48:49]\n"
"ds_write_b64 v63, v[48:49] offset:5632\n"
"s_mov_b64 exec, -1\n"
"v_mfma_f32_16x16x32_bf16 v[8:11], v[40:43], v[96:99], v[8:11]\n"
"v_mfma_f32_16x16x32_bf16 v[12:15], v[88:91], v[40:43], v[12:15]\n"
"ds_read_b128 v[40:43], v2 offset:18432\n"
"s_add_u32 m0, s58, 0\n"
"s_add_u32 s69, s51, 0xcc0000\n"
"buffer_load_dwordx4 v0, s[12:15], s69 offen offset:0 sc1 lds\n"
"buffer_load_dwordx4 v0, s[12:15], s69 offen offset:1024 sc1 lds\n"
"buffer_load_dwordx4 v0, s[12:15], s69 offen offset:2048 sc1 lds\n"
"buffer_load_dwordx4 v0, s[12:15], s69 offen offset:3072 sc1 lds\n"
"v_mfma_f32_16x16x32_bf16 v[4:7], v[104:107], v[44:47], v[4:7]\n"
"v_mfma_f32_16x16x32_bf16 v[8:11], v[44:47], v[108:111], v[8:11]\n"
"v_mfma_f32_16x16x32_bf16 v[12:15], v[100:103], v[44:47], v[12:15]\n"
"ds_read_b128 v[44:47], v2 offset:19456\n"
"s_waitcnt lgkmcnt(4)\n"
"v_mfma_f32_16x16x32_bf16 v[4:7], v[116:119], v[32:35], v[4:7]\n"
"ds_read_b128 v[48:51], v3 offset:5632\n"
"ds_read_b128 v[52:55], v3 offset:5888\n"
"v_mfma_f32_16x16x32_bf16 v[8:11], v[32:35], v[120:123], v[8:11]\n"
"v_xor_b32 v2, 0x2000, v60\n"
"v_mfma_f32_16x16x32_bf16 v[12:15], v[112:115], v[32:35], v[12:15]\n"
"ds_read_b128 v[32:35], v2 offset:16384\n"
"s_waitcnt lgkmcnt(6)\n"
"v_mfma_f32_16x16x32_bf16 v[4:7], v[128:131], v[36:39], v[4:7]\n"
"v_mfma_f32_16x16x32_bf16 v[8:11], v[36:39], v[132:135], v[8:11]\n"
"s_waitcnt lgkmcnt(1)\n"
"v_mfma_f32_16x16x32_bf16 v[12:15], v[124:127], v[36:39], v[12:15]\n"
"ds_read_b128 v[36:39], v2 offset:17408\n"
"v_bitop3_b32 v63, 1, v48, v50 bitop3:0x10\n"
"v_bitop3_b32 v63, v63, v52, v54 bitop3:0x10\n"
"v_mfma_f32_16x16x32_bf16 v[4:7], v[140:143], v[40:43], v[4:7]\n"
"v_cmp_eq_u32 vcc, 0, v63\n"
"s_nop 1\n"
"s_cbranch_vccnz LQ_54\n"
"LP_55:\n"
"v_mfma_f32_16x16x32_bf16 v[8:11], v[40:43], v[144:147], v[8:11]\n"
"v_add_f32 v48, v48, v50\n"
"v_add_f32 v52, v52, v54\n"
"v_mfma_f32_16x16x32_bf16 v[12:15], v[136:139], v[40:43], v[12:15]\n"
"ds_read_b128 v[40:43], v2 offset:18432\n"
"v_mfma_f32_16x16x32_bf16 v[4:7], v[152:155], v[44:47], v[4:7]\n"
"v_add_f32 v49, v49, v51\n"
"v_add_f32 v53, v53, v55\n"
"v_mfma_f32_16x16x32_bf16 v[8:11], v[44:47], v[156:159], v[8:11]\n"
"v_add_f32 v52, v52, v48\n"
"v_add_f32 v53, v53, v49\n"
"v_mfma_f32_16x16x32_bf16 v[12:15], v[148:151], v[44:47], v[12:15]\n"
"ds_read_b128 v[44:47], v2 offset:19456\n"
"s_waitcnt lgkmcnt(3)\n"
"v_mfma_f32_16x16x32_bf16 v[4:7], v[164:167], v[32:35], v[4:7]\n"
"v_mul_f32 v48, s72, v52\n"
"v_mul_f32 v52, 0xba000000, v52\n"
"v_mfma_f32_16x16x32_bf16 v[8:11], v[32:35], v[168:171], v[8:11]\n"
"v_xor_b32 v2, 0x3000, v60\n"
"v_mul_f32 v49, v48, v48\n"
"v_mfma_f32_16x16x32_bf16 v[12:15], v[160:163], v[32:35], v[12:15]\n"
"ds_read_b128 v[32:35], v2 offset:16384\n"
"s_waitcnt lgkmcnt(3)\n"
"v_mfma_f32_16x16x32_bf16 v[4:7], v[176:179], v[36:39], v[4:7]\n"
"v_fma_f32 v54, -v53, s72, -v49\n"
"v_cvt_pk_bf16_f32 v53, v52, v52\n"
"v_mfma_f32_16x16x32_bf16 v[8:11], v[36:39], v[180:183], v[8:11]\n"
"v_max_f32 v54, 0, v54\n"
"v_and_b32 v53, 0xffff0000, v53\n"
"v_mfma_f32_16x16x32_bf16 v[12:15], v[172:175], v[36:39], v[12:15]\n"
"ds_read_b128 v[36:39], v2 offset:17408\n"
"s_waitcnt lgkmcnt(3)\n"
"v_mfma_f32_16x16x32_bf16 v[4:7], v[188:191], v[40:43], v[4:7]\n"
"v_add_f32 v54, 0x3727c5ac, v54\n"
"v_sub_f32 v55, v52, v53\n"
"v_mfma_f32_16x16x32_bf16 v[8:11], v[40:43], v[192:195], v[8:11]\n"
"v_rsq_f32 v1, v54\n"
"v_cvt_pk_bf16_f32 v53, v52, v55\n"
"v_mfma_f32_16x16x32_bf16 v[12:15], v[184:187], v[40:43], v[12:15]\n"
"ds_read_b128 v[40:43], v2 offset:18432\n"
"s_waitcnt lgkmcnt(3)\n"
"v_mfma_f32_16x16x32_bf16 v[4:7], v[200:203], v[44:47], v[4:7]\n"
"v_cvt_pk_bf16_f32 v52, v52, v55\n"
"v_mul_f32 v55, s73, v1\n"
"v_mfma_f32_16x16x32_bf16 v[8:11], v[44:47], v[204:207], v[8:11]\n"
"s_mov_b64 exec, 0xffff\n"
"ds_write2_b32 v3, v1, v55 offset0:66 offset1:67\n"
"s_mov_b64 exec, -1\n"
"v_add_u32 v63, s52, v3\n"
"v_mfma_f32_16x16x32_bf16 v[12:15], v[196:199], v[44:47], v[12:15]\n"
"ds_read_b128 v[44:47], v2 offset:19456\n"
"ds_read_b128 v[48:51], v63\n"
"ds_read_b64 v[54:55], v63 offset:256\n"
"s_waitcnt lgkmcnt(6)\n"
"v_mfma_f32_16x16x32_bf16 v[4:7], v[212:215], v[32:35], v[4:7]\n"
"v_mfma_f32_16x16x32_bf16 v[8:11], v[32:35], v[216:219], v[8:11]\n"
"v_mfma_f32_16x16x32_bf16 v[12:15], v[208:211], v[32:35], v[12:15]\n"
"s_waitcnt lgkmcnt(5)\n"
"v_mfma_f32_16x16x32_bf16 v[4:7], v[224:227], v[36:39], v[4:7]\n"
"ds_read2_b32 v[16:17], v61 offset0:66 offset1:70\n"
"ds_read2_b32 v[18:19], v61 offset0:74 offset1:78\n"
"v_mfma_f32_16x16x32_bf16 v[8:11], v[36:39], v[228:231], v[8:11]\n"
"v_mfma_f32_16x16x32_bf16 v[12:15], v[220:223], v[36:39], v[12:15]\n"
"s_waitcnt lgkmcnt(6)\n"
"v_mfma_f32_16x16x32_bf16 v[4:7], v[236:239], v[40:43], v[4:7]\n"
"v_mfma_f32_16x16x32_bf16 v[8:11], v[40:43], v[240:243], v[8:11]\n"
"v_mfma_f32_16x16x32_bf16 v[12:15], v[232:235], v[40:43], v[12:15]\n"
"s_waitcnt lgkmcnt(4)\n"
"s_waitcnt lgkmcnt(2)\n"
"v_mfma_f32_16x16x16_bf16 v[4:7], v[50:51], v[52:53], v[4:7]\n"
"v_mfma_f32_16x16x16_bf16 v[8:11], v[52:53], v[54:55], v[8:11]\n"
"v_mfma_f32_16x16x16_bf16 v[12:15], v[48:49], v[52:53], v[12:15]\n"
"v_mfma_f32_16x16x32_bf16 v[4:7], v[248:251], v[44:47], v[4:7]\n"
"v_mfma_f32_16x16x32_bf16 v[8:11], v[44:47], v[252:255], v[8:11]\n"
"v_mfma_f32_16x16x32_bf16 v[12:15], v[244:247], v[44:47], v[12:15]\n"
"s_waitcnt vmcnt(0)\n"
"ds_read_b128 v[32:35], v60 offset:0\n"
"ds_read_b128 v[36:39], v60 offset:1024\n"
"ds_read_b128 v[40:43], v60 offset:2048\n"
"ds_read_b128 v[44:47], v60 offset:3072\n"
"s_waitcnt lgkmcnt(4)\n"
"s_waitcnt lgkmcnt(2)\n"
"v_mfma_f32_16x16x32_bf16 v[48:51], v[56:59], v[32:35], 0\n"
"v_bitop3_b32 v63, 1, v32, v34 bitop3:0x80\n"
"v_bitop3_b32 v63, v63, v36, v38 bitop3:0x80\n"
"v_mfma_f32_16x16x32_bf16 v[52:55], v[32:35], v[32:35], 0\n"
"v_mul_f32 v4, v4, v1\n"
"v_mul_f32 v5, v5, v1\n"
"v_mfma_f32_16x16x32_bf16 v[48:51], v[56:59], v[36:39], v[48:51]\n"
"v_mul_f32 v6, v6, v1\n"
"v_mul_f32 v7, v7, v1\n"
"v_mfma_f32_16x16x32_bf16 v[52:55], v[36:39], v[36:39], v[52:55]\n"
"v_cvt_pk_bf16_f32 v4, v4, v5\n"
"v_cvt_pk_bf16_f32 v5, v6, v7\n"
"s_waitcnt lgkmcnt(1)\n"
"v_mfma_f32_16x16x32_bf16 v[48:51], v[56:59], v[40:43], v[48:51]\n"
"v_mul_f32 v8, v8, v16\n"
"v_mul_f32 v9, v9, v17\n"
"v_mfma_f32_16x16x32_bf16 v[52:55], v[40:43], v[40:43], v[52:55]\n"
"v_mul_f32 v10, v10, v18\n"
"v_mul_f32 v11, v11, v19\n"
"s_waitcnt lgkmcnt(0)\n"
"v_mfma_f32_16x16x32_bf16 v[48:51], v[56:59], v[44:47], v[48:51]\n"
"v_cvt_pk_bf16_f32 v6, v8, v9\n"
"v_cvt_pk_bf16_f32 v7, v10, v11\n"
"v_mfma_f32_16x16x32_bf16 v[52:55], v[44:47], v[44:47], v[52:55]\n"
"ds_write_b128 v62, v[4:7] offset:0\n"
"v_cvt_pk_bf16_f32 v12, v12, v13\n"
"v_cvt_pk_bf16_f32 v13, v14, v15\n"
"v_mfma_f32_16x16x32_bf16 v[24:27], v[68:71], v[32:35], 0\n"
"ds_write_b64 v62, v[12:13] offset:8192\n"
"v_bitop3_b32 v63, v63, v40, v42 bitop3:0x80\n"
"v_bitop3_b32 v63, v63, v44, v46 bitop3:0x80\n"
"v_mfma_f32_16x16x32_bf16 v[20:23], v[32:35], v[72:75], 0\n"
"v_cmp_eq_u32 vcc, 0, v63\n"
"v_mfma_f32_16x16x32_bf16 v[28:31], v[64:67], v[32:35], 0\n"
"s_nop 0\n"
"s_cbranch_vccnz LQ_56\n"
"LP_57:\n"
"v_mfma_f32_16x16x32_bf16 v[24:27], v[80:83], v[36:39], v[24:27]\n"
"v_cndmask_b32_e64 v52, v52, v53, s[44:45]\n"
"v_cndmask_b32_e64 v54, v54, v55, s[44:45]\n"
"v_mfma_f32_16x16x32_bf16 v[20:23], v[36:39], v[84:87], v[20:23]\n"
"v_cndmask_b32_e64 v49, v52, v54, s[46:47]\n"
"v_and_b32 v48, -2, v48\n"
"v_xor_b32 v2, 0x1000, v60\n"
"v_mfma_f32_16x16x32_bf16 v[28:31], v[76:79], v[36:39], v[28:31]\n"
"s_waitcnt lgkmcnt(0)\n"
"s_barrier\n"
"ds_read_b128 v[32:35], v2 offset:0\n"
"ds_read_b128 v[36:39], v2 offset:1024\n"
"s_add_u32 s69, s51, 0xacc000\n"
"buffer_load_dwordx4 v[4:7], v0, s[12:15], s69 offen offset:0 sc1\n"
"buffer_load_dwordx4 v[8:11], v0, s[12:15], s69 offen offset:1024 sc1\n"
"buffer_load_dwordx4 v[12:15], v0, s[12:15], s69 offen offset:2048 sc1\n"
"buffer_load_dwordx4 v[16:19], v0, s[12:15], s69 offen offset:3072 sc1\n"
"v_add_u32 v63, s53, v3\n"
"v_mfma_f32_16x16x32_bf16 v[24:27], v[92:95], v[40:43], v[24:27]\n"
"s_mov_b64 exec, s[48:49]\n"
"ds_write_b64 v63, v[48:49] offset:5120\n"
"s_mov_b64 exec, -1\n"
"v_mfma_f32_16x16x32_bf16 v[20:23], v[40:43], v[96:99], v[20:23]\n"
"v_mfma_f32_16x16x32_bf16 v[28:31], v[88:91], v[40:43], v[28:31]\n"
"ds_read_b128 v[40:43], v2 offset:2048\n"
"v_mfma_f32_16x16x32_bf16 v[24:27], v[104:107], v[44:47], v[24:27]\n"
"v_mfma_f32_16x16x32_bf16 v[20:23], v[44:47], v[108:111], v[20:23]\n"
"v_mfma_f32_16x16x32_bf16 v[28:31], v[100:103], v[44:47], v[28:31]\n"
"ds_read_b128 v[44:47], v2 offset:3072\n"
"s_waitcnt lgkmcnt(4)\n"
"v_mfma_f32_16x16x32_bf16 v[24:27], v[116:119], v[32:35], v[24:27]\n"
"ds_read_b128 v[48:51], v3 offset:5120\n"
"ds_read_b128 v[52:55], v3 offset:5376\n"
"v_mfma_f32_16x16x32_bf16 v[20:23], v[32:35], v[120:123], v[20:23]\n"
"v_xor_b32 v2, 0x2000, v60\n"
"v_mfma_f32_16x16x32_bf16 v[28:31], v[112:115], v[32:35], v[28:31]\n"
"ds_read_b128 v[32:35], v2 offset:0\n"
"s_waitcnt lgkmcnt(6)\n"
"v_mfma_f32_16x16x32_bf16 v[24:27], v[128:131], v[36:39], v[24:27]\n"
"v_mfma_f32_16x16x32_bf16 v[20:23], v[36:39], v[132:135], v[20:23]\n"
"s_waitcnt lgkmcnt(1)\n"
"v_mfma_f32_16x16x32_bf16 v[28:31], v[124:127], v[36:39], v[28:31]\n"
"ds_read_b128 v[36:39], v2 offset:1024\n"
"v_bitop3_b32 v63, 1, v48, v50 bitop3:0x10\n"
"v_bitop3_b32 v63, v63, v52, v54 bitop3:0x10\n"
"v_mfma_f32_16x16x32_bf16 v[24:27], v[140:143], v[40:43], v[24:27]\n"
"v_cmp_eq_u32 vcc, 0, v63\n"
"s_nop 1\n"
"s_cbranch_vccnz LQ_58\n"
"LP_59:\n"
"v_mfma_f32_16x16x32_bf16 v[20:23], v[40:43], v[144:147], v[20:23]\n"
"v_add_f32 v48, v48, v50\n"
"v_add_f32 v52, v52, v54\n"
"v_mfma_f32_16x16x32_bf16 v[28:31], v[136:139], v[40:43], v[28:31]\n"
"ds_read_b128 v[40:43], v2 offset:2048\n"
"v_mfma_f32_16x16x32_bf16 v[24:27], v[152:155], v[44:47], v[24:27]\n"
"v_add_f32 v49, v49, v51\n"
"v_add_f32 v53, v53, v55\n"
"v_mfma_f32_16x16x32_bf16 v[20:23], v[44:47], v[156:159], v[20:23]\n"
"v_add_f32 v52, v52, v48\n"
"v_add_f32 v53, v53, v49\n"
"v_mfma_f32_16x16x32_bf16 v[28:31], v[148:151], v[44:47], v[28:31]\n"
"ds_read_b128 v[44:47], v2 offset:3072\n"
"s_waitcnt lgkmcnt(3)\n"
"v_mfma_f32_16x16x32_bf16 v[24:27], v[164:167], v[32:35], v[24:27]\n"
"v_mul_f32 v48, s72, v52\n"
"v_mul_f32 v52, 0xba000000, v52\n"
"v_mfma_f32_16x16x32_bf16 v[20:23], v[32:35], v[168:171], v[20:23]\n"
"v_xor_b32 v2, 0x3000, v60\n"
"v_mul_f32 v49, v48, v48\n"
"v_mfma_f32_16x16x32_bf16 v[28:31], v[160:163], v[32:35], v[28:31]\n"
"ds_read_b128 v[32:35], v2 offset:0\n"
"s_waitcnt lgkmcnt(3)\n"
"v_mfma_f32_16x16x32_bf16 v[24:27], v[176:179], v[36:39], v[24:27]\n"
"v_fma_f32 v54, -v53, s72, -v49\n"
"v_cvt_pk_bf16_f32 v53, v52, v52\n"
"v_mfma_f32_16x16x32_bf16 v[20:23], v[36:39], v[180:183], v[20:23]\n"
"v_max_f32 v54, 0, v54\n"
"v_and_b32 v53, 0xffff0000, v53\n"
"v_mfma_f32_16x16x32_bf16 v[28:31], v[172:175], v[36:39], v[28:31]\n"
"ds_read_b128 v[36:39], v2 offset:1024\n"
"s_waitcnt lgkmcnt(3)\n"
"v_mfma_f32_16x16x32_bf16 v[24:27], v[188:191], v[40:43], v[24:27]\n"
"v_add_f32 v54, 0x3727c5ac, v54\n"
"v_sub_f32 v55, v52, v53\n"
"v_mfma_f32_16x16x32_bf16 v[20:23], v[40:43], v[192:195], v[20:23]\n"
"v_rsq_f32 v1, v54\n"
"v_cvt_pk_bf16_f32 v53, v52, v55\n"
"v_mfma_f32_16x16x32_bf16 v[28:31], v[184:187], v[40:43], v[28:31]\n"
"ds_read_b128 v[40:43], v2 offset:2048\n"
"s_waitcnt lgkmcnt(3)\n"
"v_mfma_f32_16x16x32_bf16 v[24:27], v[200:203], v[44:47], v[24:27]\n"
"v_cvt_pk_bf16_f32 v52, v52, v55\n"
"v_mul_f32 v55, s73, v1\n"
"v_mfma_f32_16x16x32_bf16 v[20:23], v[44:47], v[204:207], v[20:23]\n"
"s_mov_b64 exec, 0xffff\n"
"ds_write2_b32 v3, v1, v55 offset0:2 offset1:3\n"
"s_mov_b64 exec, -1\n"
"v_add_u32 v63, s52, v3\n"
"v_mfma_f32_16x16x32_bf16 v[28:31], v[196:199], v[44:47], v[28:31]\n"
"ds_read_b128 v[44:47], v2 offset:3072\n"
"ds_read_b128 v[48:51], v63\n"
"ds_read_b64 v[54:55], v63 offset:256\n"
"s_waitcnt lgkmcnt(6)\n"
"v_mfma_f32_16x16x32_bf16 v[24:27], v[212:215], v[32:35], v[24:27]\n"
"v_mfma_f32_16x16x32_bf16 v[20:23], v[32:35], v[216:219], v[20:23]\n"
"v_mfma_f32_16x16x32_bf16 v[28:31], v[208:211], v[32:35], v[28:31]\n"
"s_waitcnt lgkmcnt(5)\n"
"v_mfma_f32_16x16x32_bf16 v[24:27], v[224:227], v[36:39], v[24:27]\n"
"ds_read2_b32 v[32:33], v61 offset0:2 offset1:6\n"
"ds_read2_b32 v[34:35], v61 offset0:10 offset1:14\n"
"v_mfma_f32_16x16x32_bf16 v[20:23], v[36:39], v[228:231], v[20:23]\n"
"v_add_u32 v63, 153600, v0\n"
"ds_read_b32 v61, v63 offset:4\n"
"v_mfma_f32_16x16x32_bf16 v[28:31], v[220:223], v[36:39], v[28:31]\n"
"s_waitcnt lgkmcnt(7)\n"
"v_mfma_f32_16x16x32_bf16 v[24:27], v[236:239], v[40:43], v[24:27]\n"
"s_xor_b32 s68, s28, 1\n"
"s_lshl_b32 s68, s68, 5\n"
"v_mfma_f32_16x16x32_bf16 v[20:23], v[40:43], v[240:243], v[20:23]\n"
"s_waitcnt lgkmcnt(0)\n"
"v_xor_b32 v61, s68, v61\n"
"v_add_u32 v61, s57, v61\n"
"v_mfma_f32_16x16x32_bf16 v[28:31], v[232:235], v[40:43], v[28:31]\n"
"v_mfma_f32_16x16x16_bf16 v[24:27], v[50:51], v[52:53], v[24:27]\n"
"v_mfma_f32_16x16x16_bf16 v[20:23], v[52:53], v[54:55], v[20:23]\n"
"v_mfma_f32_16x16x16_bf16 v[28:31], v[48:49], v[52:53], v[28:31]\n"
"v_mfma_f32_16x16x32_bf16 v[24:27], v[248:251], v[44:47], v[24:27]\n"
"ds_read_b64_tr_b16 v[36:37], v61 offset:4096\n"
"ds_read_b64_tr_b16 v[38:39], v61 offset:4608\n"
"ds_read_b64_tr_b16 v[40:41], v61 offset:8192\n"
"ds_read_b64_tr_b16 v[42:43], v61 offset:8704\n"
"v_mfma_f32_16x16x32_bf16 v[20:23], v[44:47], v[252:255], v[20:23]\n"
"v_mfma_f32_16x16x32_bf16 v[28:31], v[244:247], v[44:47], v[28:31]\n"
"v_add_u32 v63, 152576, v0\n"
"ds_read_b64 v[50:51], v62 offset:16392\n"
"ds_read_b64 v[48:49], v63 offset:8\n"
"ds_read_b64_tr_b16 v[44:45], v61 offset:12288\n"
"ds_read_b64_tr_b16 v[46:47], v61 offset:12800\n"
"v_add_u32 v63, 153600, v0\n"
"v_mul_f32 v24, v24, v1\n"
"v_mul_f32 v25, v25, v1\n"
"v_mul_f32 v26, v26, v1\n"
"v_mul_f32 v27, v27, v1\n"
"v_mul_f32 v20, v20, v32\n"
"v_mul_f32 v21, v21, v33\n"
"v_mul_f32 v22, v22, v34\n"
"v_mul_f32 v23, v23, v35\n"
"v_cvt_pk_bf16_f32 v20, v20, v21\n"
"v_cvt_pk_bf16_f32 v21, v22, v23\n"
"v_cvt_pk_bf16_f32 v22, v24, v25\n"
"v_cvt_pk_bf16_f32 v23, v26, v27\n"
"ds_write_b128 v62, v[20:23] offset:4096\n"
"v_cvt_pk_bf16_f32 v28, v28, v29\n"
"v_cvt_pk_bf16_f32 v29, v30, v31\n"
"ds_write_b64 v62, v[28:29] offset:8200\n"
"ds_read_b64_tr_b16 v[32:33], v61 offset:0\n"
"ds_read_b64_tr_b16 v[34:35], v61 offset:512\n"
"ds_read_b32 v1, v63\n"
"s_waitcnt lgkmcnt(3)\n"
"s_barrier\n"
"s_setprio 3\n"
"v_mfma_f32_16x16x16_bf16 v[28:31], v[48:49], v[50:51], 0\n"
"v_xor_b32 v61, 96, v61\n"
"ds_read_b64_tr_b16 v[48:49], v61 offset:0\n"
"ds_read_b64_tr_b16 v[50:51], v61 offset:512\n"
"s_waitcnt vmcnt(0)\n"
"LB_60:\n"
"s_waitcnt lgkmcnt(3)\n"
"v_mfma_f32_16x16x32_bf16 v[20:23], v[32:35], v[4:7], 0\n"
"ds_read_b64_tr_b16 v[32:33], v61 offset:4096\n"
"ds_read_b64_tr_b16 v[34:35], v61 offset:4608\n"
"v_bitop3_b32 v63, 1, v4, v6 bitop3:0x10\n"
"v_bitop3_b32 v63, v63, v8, v10 bitop3:0x10\n"
"v_mfma_f32_16x16x32_bf16 v[20:23], v[36:39], v[8:11], v[20:23]\n"
"ds_read_b64_tr_b16 v[36:37], v61 offset:8192\n"
"ds_read_b64_tr_b16 v[38:39], v61 offset:8704\n"
"v_bitop3_b32 v63, v63, v12, v14 bitop3:0x10\n"
"v_bitop3_b32 v63, v63, v16, v18 bitop3:0x10\n"
"v_mfma_f32_16x16x32_bf16 v[20:23], v[40:43], v[12:15], v[20:23]\n"
"ds_read_b64_tr_b16 v[40:41], v61 offset:12288\n"
"ds_read_b64_tr_b16 v[42:43], v61 offset:12800\n"
"v_cmp_eq_u32 vcc, 0, v63\n"
"v_mfma_f32_16x16x32_bf16 v[20:23], v[44:47], v[16:19], v[20:23]\n"
"v_xor_b32 v61, 32, v61\n"
"ds_read_b64_tr_b16 v[44:45], v61 offset:0\n"
"ds_read_b64_tr_b16 v[46:47], v61 offset:512\n"
"s_cbranch_vccnz LC_61\n"
"s_waitcnt lgkmcnt(8)\n"
"v_mfma_f32_16x16x32_bf16 v[24:27], v[48:51], v[4:7], 0\n"
"ds_read_b64_tr_b16 v[48:49], v61 offset:4096\n"
"ds_read_b64_tr_b16 v[50:51], v61 offset:4608\n"
"s_waitcnt lgkmcnt(8)\n"
"v_mfma_f32_16x16x32_bf16 v[24:27], v[32:35], v[8:11], v[24:27]\n"
"ds_read_b64_tr_b16 v[32:33], v61 offset:8192\n"
"ds_read_b64_tr_b16 v[34:35], v61 offset:8704\n"
"s_waitcnt lgkmcnt(8)\n"
"v_mfma_f32_16x16x32_bf16 v[24:27], v[36:39], v[12:15], v[24:27]\n"
"ds_read_b64_tr_b16 v[36:37], v61 offset:12288\n"
"ds_read_b64_tr_b16 v[38:39], v61 offset:12800\n"
"ds_write_b128 v60, v[20:23] offset:32768\n"
"s_waitcnt lgkmcnt(9)\n"
"v_mfma_f32_16x16x32_bf16 v[24:27], v[40:43], v[16:19], v[24:27]\n"
"v_xor_b32 v61, 96, v61\n"
"ds_read_b64_tr_b16 v[40:41], v61 offset:0\n"
"ds_read_b64_tr_b16 v[42:43], v61 offset:512\n"
"s_waitcnt lgkmcnt(9)\n"
"v_mfma_f32_16x16x32_bf16 v[20:23], v[44:47], v[4:7], 0\n"
"ds_read_b64_tr_b16 v[44:45], v61 offset:4096\n"
"ds_read_b64_tr_b16 v[46:47], v61 offset:4608\n"
"s_waitcnt lgkmcnt(9)\n"
"v_mfma_f32_16x16x32_bf16 v[20:23], v[48:51], v[8:11], v[20:23]\n"
"ds_read_b64_tr_b16 v[48:49], v61 offset:8192\n"
"ds_read_b64_tr_b16 v[50:51], v61 offset:8704\n"
"s_add_u32 m0, s58, 16384\n"
"s_add_u32 s69, s51, 0xac8000\n"
"buffer_load_dwordx4 v0, s[12:15], s69 offen offset:0 sc1 lds\n"
"buffer_load_dwordx4 v0, s[12:15], s69 offen offset:1024 sc1 lds\n"
"buffer_load_dwordx4 v0, s[12:15], s69 offen offset:2048 sc1 lds\n"
"buffer_load_dwordx4 v0, s[12:15], s69 offen offset:3072 sc1 lds\n"
"s_waitcnt lgkmcnt(9)\n"
"v_mfma_f32_16x16x32_bf16 v[20:23], v[32:35], v[12:15], v[20:23]\n"
"ds_read_b64_tr_b16 v[32:33], v61 offset:12288\n"
"ds_read_b64_tr_b16 v[34:35], v61 offset:12800\n"
"v_xor_b32 v61, 32, v61\n"
"ds_write_b128 v60, v[24:27] offset:33792\n"
"s_waitcnt lgkmcnt(10)\n"
"v_mfma_f32_16x16x32_bf16 v[20:23], v[36:39], v[16:19], v[20:23]\n"
"s_waitcnt lgkmcnt(7)\n"
"v_mfma_f32_16x16x32_bf16 v[28:31], v[40:43], v[4:7], v[28:31]\n"
"s_waitcnt lgkmcnt(5)\n"
"v_mfma_f32_16x16x32_bf16 v[28:31], v[44:47], v[8:11], v[28:31]\n"
"s_waitcnt lgkmcnt(3)\n"
"v_mfma_f32_16x16x32_bf16 v[28:31], v[48:51], v[12:15], v[28:31]\n"
"s_nop 1\n"
"ds_write_b128 v60, v[20:23] offset:34816\n"
"s_waitcnt lgkmcnt(2)\n"
"v_mfma_f32_16x16x32_bf16 v[28:31], v[32:35], v[16:19], v[28:31]\n"
"s_waitcnt lgkmcnt(0)\n"
"s_barrier\n"
"v_xor_b32 v47, 0x3000, v60\n"
"v_xor_b32 v45, 0x1000, v60\n"
"v_xor_b32 v46, 0x2000, v60\n"
"ds_read_b128 v[40:43], v47 offset:34816\n"
"ds_read_b128 v[32:35], v45 offset:32768\n"
"ds_read_b128 v[36:39], v46 offset:33792\n"
"s_add_u32 s69, s50, 0xccc000\n"
"s_waitcnt lgkmcnt(2)\n"
"v_add_f32 v28, v28, v40\n"
"v_add_f32 v29, v29, v41\n"
"v_add_f32 v30, v30, v42\n"
"v_add_f32 v31, v31, v43\n"
"s_waitcnt lgkmcnt(0)\n"
"v_add_f32 v32, v32, v36\n"
"v_add_f32 v33, v33, v37\n"
"v_add_f32 v34, v34, v38\n"
"v_add_f32 v35, v35, v39\n"
"v_add_f32 v28, v28, v32\n"
"v_add_f32 v29, v29, v33\n"
"v_cvt_pk_bf16_f32 v46, v28, v29\n"
"v_add_f32 v30, v30, v34\n"
"v_add_f32 v31, v31, v35\n"
"v_and_b32 v44, -2, v46\n"
"v_cvt_pk_bf16_f32 v45, v30, v31\n"
"buffer_store_dwordx2 v[44:45], v1, s[12:15], s69 offen sc1\n"
"v_mov_b32 v47, v45\n"
"ds_write_b64 v62, v[46:47] offset:16392\n"
"v_add_u32 v63, 152576, v0\n"
"ds_read_b64 v[50:51], v62 offset:16384\n"
"ds_read_b64 v[48:49], v63 offset:8\n"
"ds_read_b64_tr_b16 v[32:33], v61 offset:0\n"
"ds_read_b64_tr_b16 v[34:35], v61 offset:512\n"
"ds_read_b64_tr_b16 v[36:37], v61 offset:4096\n"
"ds_read_b64_tr_b16 v[38:39], v61 offset:4608\n"
"ds_read_b64_tr_b16 v[40:41], v61 offset:8192\n"
"ds_read_b64_tr_b16 v[42:43], v61 offset:8704\n"
"ds_read_b64_tr_b16 v[44:45], v61 offset:12288\n"
"ds_read_b64_tr_b16 v[46:47], v61 offset:12800\n"
"s_waitcnt lgkmcnt(8)\n"
"v_mfma_f32_16x16x16_bf16 v[28:31], v[48:49], v[50:51], 0\n"
"v_xor_b32 v61, 96, v61\n"
"ds_read_b64_tr_b16 v[48:49], v61 offset:0\n"
"ds_read_b64_tr_b16 v[50:51], v61 offset:512\n"
"s_waitcnt vmcnt(1)\n"
"ds_read_b128 v[4:7], v60 offset:16384\n"
"ds_read_b128 v[8:11], v60 offset:17408\n"
"ds_read_b128 v[12:15], v60 offset:18432\n"
"ds_read_b128 v[16:19], v60 offset:19456\n"
"LB_66:\n"
"s_waitcnt lgkmcnt(3)\n"
"v_mfma_f32_16x16x32_bf16 v[20:23], v[32:35], v[4:7], 0\n"
"ds_read_b64_tr_b16 v[32:33], v61 offset:4096\n"
"ds_read_b64_tr_b16 v[34:35], v61 offset:4608\n"
"v_bitop3_b32 v63, 1, v4, v6 bitop3:0x10\n"
"s_waitcnt lgkmcnt(4)\n"
"v_mfma_f32_16x16x32_bf16 v[20:23], v[36:39], v[8:11], v[20:23]\n"
"ds_read_b64_tr_b16 v[36:37], v61 offset:8192\n"
"ds_read_b64_tr_b16 v[38:39], v61 offset:8704\n"
"v_bitop3_b32 v63, v63, v8, v10 bitop3:0x10\n"
"s_waitcnt lgkmcnt(5)\n"
"v_mfma_f32_16x16x32_bf16 v[20:23], v[40:43], v[12:15], v[20:23]\n"
"ds_read_b64_tr_b16 v[40:41], v61 offset:12288\n"
"ds_read_b64_tr_b16 v[42:43], v61 offset:12800\n"
"v_bitop3_b32 v63, v63, v12, v14 bitop3:0x10\n"
"s_waitcnt lgkmcnt(6)\n"
"v_mfma_f32_16x16x32_bf16 v[20:23], v[44:47], v[16:19], v[20:23]\n"
"v_xor_b32 v61, 32, v61\n"
"ds_read_b64_tr_b16 v[44:45], v61 offset:0\n"
"ds_read_b64_tr_b16 v[46:47], v61 offset:512\n"
"v_bitop3_b32 v63, v63, v16, v18 bitop3:0x10\n"
"v_cmp_eq_u32 vcc, 0, v63\n"
"v_mfma_f32_16x16x32_bf16 v[24:27], v[48:51], v[4:7], 0\n"
"ds_read_b64_tr_b16 v[48:49], v61 offset:4096\n"
"ds_read_b64_tr_b16 v[50:51], v61 offset:4608\n"
"s_cbranch_vccnz LC_67\n"
"s_waitcnt lgkmcnt(8)\n"
"v_mfma_f32_16x16x32_bf16 v[24:27], v[32:35], v[8:11], v[24:27]\n"
"ds_read_b64_tr_b16 v[32:33], v61 offset:8192\n"
"ds_read_b64_tr_b16 v[34:35], v61 offset:8704\n"
"s_waitcnt lgkmcnt(8)\n"
"v_mfma_f32_16x16x32_bf16 v[24:27], v[36:39], v[12:15], v[24:27]\n"
"ds_read_b64_tr_b16 v[36:37], v61 offset:12288\n"
"ds_read_b64_tr_b16 v[38:39], v61 offset:12800\n"
"ds_write_b128 v60, v[20:23] offset:16384\n"
"s_waitcnt lgkmcnt(9)\n"
"v_mfma_f32_16x16x32_bf16 v[24:27], v[40:43], v[16:19], v[24:27]\n"
"v_xor_b32 v61, 96, v61\n"
"ds_read_b64_tr_b16 v[40:41], v61 offset:0\n"
"ds_read_b64_tr_b16 v[42:43], v61 offset:512\n"
"s_waitcnt lgkmcnt(9)\n"
"v_mfma_f32_16x16x32_bf16 v[20:23], v[44:47], v[4:7], 0\n"
"ds_read_b64_tr_b16 v[44:45], v61 offset:4096\n"
"ds_read_b64_tr_b16 v[46:47], v61 offset:4608\n"
"s_waitcnt lgkmcnt(9)\n"
"v_mfma_f32_16x16x32_bf16 v[20:23], v[48:51], v[8:11], v[20:23]\n"
"ds_read_b64_tr_b16 v[48:49], v61 offset:8192\n"
"ds_read_b64_tr_b16 v[50:51], v61 offset:8704\n"
"s_waitcnt lgkmcnt(9)\n"
"v_mfma_f32_16x16x32_bf16 v[20:23], v[32:35], v[12:15], v[20:23]\n"
"ds_read_b64_tr_b16 v[32:33], v61 offset:12288\n"
"ds_read_b64_tr_b16 v[34:35], v61 offset:12800\n"
"v_xor_b32 v61, 32, v61\n"
"ds_write_b128 v60, v[24:27] offset:17408\n"
"s_add_u32 m0, s58, 0\n"
"s_add_u32 s69, s51, 0xac4000\n"
"buffer_load_dwordx4 v0, s[12:15], s69 offen offset:0 sc1 lds\n"
"buffer_load_dwordx4 v0, s[12:15], s69 offen offset:1024 sc1 lds\n"
"buffer_load_dwordx4 v0, s[12:15], s69 offen offset:2048 sc1 lds\n"
"buffer_load_dwordx4 v0, s[12:15], s69 offen offset:3072 sc1 lds\n"
"s_waitcnt lgkmcnt(10)\n"
"v_mfma_f32_16x16x32_bf16 v[20:23], v[36:39], v[16:19], v[20:23]\n"
"s_waitcnt lgkmcnt(7)\n"
"v_mfma_f32_16x16x32_bf16 v[28:31], v[40:43], v[4:7], v[28:31]\n"
"s_waitcnt lgkmcnt(5)\n"
"v_mfma_f32_16x16x32_bf16 v[28:31], v[44:47], v[8:11], v[28:31]\n"
"s_waitcnt lgkmcnt(3)\n"
"v_mfma_f32_16x16x32_bf16 v[28:31], v[48:51], v[12:15], v[28:31]\n"
"s_nop 1\n"
"ds_write_b128 v60, v[20:23] offset:18432\n"
"s_waitcnt lgkmcnt(2)\n"
"v_mfma_f32_16x16x32_bf16 v[28:31], v[32:35], v[16:19], v[28:31]\n"
"s_waitcnt lgkmcnt(0)\n"
"s_barrier\n"
"v_xor_b32 v47, 0x3000, v60\n"
"v_xor_b32 v45, 0x1000, v60\n"
"v_xor_b32 v46, 0x2000, v60\n"
"ds_read_b128 v[40:43], v47 offset:18432\n"
"ds_read_b128 v[32:35], v45 offset:16384\n"
"ds_read_b128 v[36:39], v46 offset:17408\n"
"s_add_u32 s69, s50, 0xcc8000\n"
"s_waitcnt lgkmcnt(2)\n"
"v_add_f32 v28, v28, v40\n"
"v_add_f32 v29, v29, v41\n"
"v_add_f32 v30, v30, v42\n"
"v_add_f32 v31, v31, v43\n"
"s_waitcnt lgkmcnt(0)\n"
"v_add_f32 v32, v32, v36\n"
"v_add_f32 v33, v33, v37\n"
"v_add_f32 v34, v34, v38\n"
"v_add_f32 v35, v35, v39\n"
"v_add_f32 v28, v28, v32\n"
"v_add_f32 v29, v29, v33\n"
"v_cvt_pk_bf16_f32 v46, v28, v29\n"
"v_add_f32 v30, v30, v34\n"
"v_add_f32 v31, v31, v35\n"
"v_and_b32 v44, -2, v46\n"
"v_cvt_pk_bf16_f32 v45, v30, v31\n"
"buffer_store_dwordx2 v[44:45], v1, s[12:15], s69 offen sc1\n"
"v_mov_b32 v47, v45\n"
"ds_write_b64 v62, v[46:47] offset:16384\n"
"v_add_u32 v63, 152576, v0\n"
"ds_read_b64 v[50:51], v62 offset:12296\n"
"ds_read_b64 v[48:49], v63 offset:8\n"
"ds_read_b64_tr_b16 v[32:33], v61 offset:0\n"
"ds_read_b64_tr_b16 v[34:35], v61 offset:512\n"
"ds_read_b64_tr_b16 v[36:37], v61 offset:4096\n"
"ds_read_b64_tr_b16 v[38:39], v61 offset:4608\n"
"ds_read_b64_tr_b16 v[40:41], v61 offset:8192\n"
"ds_read_b64_tr_b16 v[42:43], v61 offset:8704\n"
"ds_read_b64_tr_b16 v[44:45], v61 offset:12288\n"
"ds_read_b64_tr_b16 v[46:47], v61 offset:12800\n"
"s_waitcnt lgkmcnt(8)\n"
"v_mfma_f32_16x16x16_bf16 v[28:31], v[48:49], v[50:51], 0\n"
"v_xor_b32 v61, 96, v61\n"
"ds_read_b64_tr_b16 v[48:49], v61 offset:0\n"
"ds_read_b64_tr_b16 v[50:51], v61 offset:512\n"
"s_waitcnt vmcnt(1)\n"
"ds_read_b128 v[4:7], v60 offset:0\n"
"ds_read_b128 v[8:11], v60 offset:1024\n"
"ds_read_b128 v[12:15], v60 offset:2048\n"
"ds_read_b128 v[16:19], v60 offset:3072\n"
"LB_68:\n"
"s_waitcnt lgkmcnt(3)\n"
"v_mfma_f32_16x16x32_bf16 v[20:23], v[32:35], v[4:7], 0\n"
"ds_read_b64_tr_b16 v[32:33], v61 offset:4096\n"
"ds_read_b64_tr_b16 v[34:35], v61 offset:4608\n"
"v_bitop3_b32 v63, 1, v4, v6 bitop3:0x10\n"
"s_waitcnt lgkmcnt(4)\n"
"v_mfma_f32_16x16x32_bf16 v[20:23], v[36:39], v[8:11], v[20:23]\n"
"ds_read_b64_tr_b16 v[36:37], v61 offset:8192\n"
"ds_read_b64_tr_b16 v[38:39], v61 offset:8704\n"
"v_bitop3_b32 v63, v63, v8, v10 bitop3:0x10\n"
"s_waitcnt lgkmcnt(5)\n"
"v_mfma_f32_16x16x32_bf16 v[20:23], v[40:43], v[12:15], v[20:23]\n"
"ds_read_b64_tr_b16 v[40:41], v61 offset:12288\n"
"ds_read_b64_tr_b16 v[42:43], v61 offset:12800\n"
"v_bitop3_b32 v63, v63, v12, v14 bitop3:0x10\n"
"s_waitcnt lgkmcnt(6)\n"
"v_mfma_f32_16x16x32_bf16 v[20:23], v[44:47], v[16:19], v[20:23]\n"
"v_xor_b32 v61, 32, v61\n"
"ds_read_b64_tr_b16 v[44:45], v61 offset:0\n"
"ds_read_b64_tr_b16 v[46:47], v61 offset:512\n"
"v_bitop3_b32 v63, v63, v16, v18 bitop3:0x10\n"
"v_cmp_eq_u32 vcc, 0, v63\n"
"v_mfma_f32_16x16x32_bf16 v[24:27], v[48:51], v[4:7], 0\n"
"ds_read_b64_tr_b16 v[48:49], v61 offset:4096\n"
"ds_read_b64_tr_b16 v[50:51], v61 offset:4608\n"
"s_cbranch_vccnz LC_69\n"
"v_xor_b32 v63, 0x400, v62\n"
"s_waitcnt lgkmcnt(8)\n"
"v_mfma_f32_16x16x32_bf16 v[24:27], v[32:35], v[8:11], v[24:27]\n"
"ds_read_b64_tr_b16 v[32:33], v61 offset:8192\n"
"ds_read_b64_tr_b16 v[34:35], v61 offset:8704\n"
"s_waitcnt lgkmcnt(8)\n"
"v_mfma_f32_16x16x32_bf16 v[24:27], v[36:39], v[12:15], v[24:27]\n"
"ds_read_b64_tr_b16 v[36:37], v61 offset:12288\n"
"ds_read_b64_tr_b16 v[38:39], v61 offset:12800\n"
"ds_write_b128 v63, v[20:23] offset:0\n"
"s_waitcnt lgkmcnt(9)\n"
"v_mfma_f32_16x16x32_bf16 v[24:27], v[40:43], v[16:19], v[24:27]\n"
"v_xor_b32 v61, 96, v61\n"
"ds_read_b64_tr_b16 v[40:41], v61 offset:0\n"
"ds_read_b64_tr_b16 v[42:43], v61 offset:512\n"
"v_xor_b32 v63, 0x800, v62\n"
"s_waitcnt lgkmcnt(9)\n"
"v_mfma_f32_16x16x32_bf16 v[20:23], v[44:47], v[4:7], 0\n"
"ds_read_b64_tr_b16 v[44:45], v61 offset:4096\n"
"ds_read_b64_tr_b16 v[46:47], v61 offset:4608\n"
"s_add_u32 m0, s58, 0\n"
"s_add_u32 s69, s51, 0xac0000\n"
"buffer_load_dwordx4 v0, s[12:15], s69 offen offset:0 sc1 lds\n"
"buffer_load_dwordx4 v0, s[12:15], s69 offen offset:1024 sc1 lds\n"
"buffer_load_dwordx4 v0, s[12:15], s69 offen offset:2048 sc1 lds\n"
"buffer_load_dwordx4 v0, s[12:15], s69 offen offset:3072 sc1 lds\n"
"s_waitcnt lgkmcnt(9)\n"
"v_mfma_f32_16x16x32_bf16 v[20:23], v[48:51], v[8:11], v[20:23]\n"
"ds_read_b64_tr_b16 v[48:49], v61 offset:8192\n"
"ds_read_b64_tr_b16 v[50:51], v61 offset:8704\n"
"s_waitcnt lgkmcnt(9)\n"
"v_mfma_f32_16x16x32_bf16 v[20:23], v[32:35], v[12:15], v[20:23]\n"
"ds_read_b64_tr_b16 v[32:33], v61 offset:12288\n"
"ds_read_b64_tr_b16 v[34:35], v61 offset:12800\n"
"v_xor_b32 v61, 32, v61\n"
"ds_write_b128 v63, v[24:27] offset:4096\n"
"s_waitcnt lgkmcnt(10)\n"
"v_mfma_f32_16x16x32_bf16 v[20:23], v[36:39], v[16:19], v[20:23]\n"
"v_xor_b32 v63, 0xc00, v62\n"
"s_waitcnt lgkmcnt(7)\n"
"v_mfma_f32_16x16x32_bf16 v[28:31], v[40:43], v[4:7], v[28:31]\n"
"s_waitcnt lgkmcnt(5)\n"
"v_mfma_f32_16x16x32_bf16 v[28:31], v[44:47], v[8:11], v[28:31]\n"
"s_waitcnt lgkmcnt(3)\n"
"v_mfma_f32_16x16x32_bf16 v[28:31], v[48:51], v[12:15], v[28:31]\n"
"s_nop 0\n"
"ds_write_b128 v63, v[20:23] offset:8192\n"
"s_waitcnt lgkmcnt(2)\n"
"v_mfma_f32_16x16x32_bf16 v[28:31], v[32:35], v[16:19], v[28:31]\n"
"v_mov_b32 v63, 15\n"
"s_lshl_b64 exec, 1, s28\n"
"ds_write_b32 v3, v63 offset:4\n"
"s_mov_b64 exec, -1\n"
"ds_read_b32 v63, v3 offset:4\n"
"s_waitcnt lgkmcnt(0)\n"
"v_cmp_gt_u32 vcc, 15, v63\n"
"s_nop 1\n"
"s_and_b32 s68, vcc_lo, 15\n"
"s_cbranch_scc1 Lf_70\n"
"Lg_71:\n"
"ds_read_b128 v[40:43], v62 offset:8192\n"
"ds_read_b128 v[32:35], v62 offset:0\n"
"ds_read_b128 v[36:39], v62 offset:4096\n"
"s_add_u32 s69, s50, 0xcc4000\n"
"s_waitcnt lgkmcnt(2)\n"
"v_add_f32 v28, v28, v40\n"
"v_add_f32 v29, v29, v41\n"
"v_add_f32 v30, v30, v42\n"
"v_add_f32 v31, v31, v43\n"
"s_waitcnt lgkmcnt(0)\n"
"v_mov_b32 v63, 16\n"
"s_lshl_b64 exec, 1, s28\n"
"ds_write_b32 v3, v63 offset:4\n"
"s_mov_b64 exec, -1\n"
"v_add_f32 v32, v32, v36\n"
"v_add_f32 v33, v33, v37\n"
"v_add_f32 v34, v34, v38\n"
"v_add_f32 v35, v35, v39\n"
"v_add_f32 v28, v28, v32\n"
"v_add_f32 v29, v29, v33\n"
"v_cvt_pk_bf16_f32 v46, v28, v29\n"
"v_add_f32 v30, v30, v34\n"
"v_add_f32 v31, v31, v35\n"
"v_and_b32 v44, -2, v46\n"
"v_cvt_pk_bf16_f32 v45, v30, v31\n"
"buffer_store_dwordx2 v[44:45], v1, s[12:15], s69 offen sc1\n"
"v_mov_b32 v47, v45\n"
"ds_write_b64 v62, v[46:47] offset:12296\n"
"s_setprio 3\n"
"v_add_u32 v63, 152576, v0\n"
"ds_read_b64 v[50:51], v62 offset:12288\n"
"ds_read_b64 v[48:49], v63 offset:8\n"
"ds_read_b64_tr_b16 v[32:33], v61 offset:0\n"
"ds_read_b64_tr_b16 v[34:35], v61 offset:512\n"
"ds_read_b64_tr_b16 v[36:37], v61 offset:4096\n"
"ds_read_b64_tr_b16 v[38:39], v61 offset:4608\n"
"ds_read_b64_tr_b16 v[40:41], v61 offset:8192\n"
"ds_read_b64_tr_b16 v[42:43], v61 offset:8704\n"
"ds_read_b64_tr_b16 v[44:45], v61 offset:12288\n"
"ds_read_b64_tr_b16 v[46:47], v61 offset:12800\n"
"s_waitcnt lgkmcnt(8)\n"
"v_mfma_f32_16x16x16_bf16 v[28:31], v[48:49], v[50:51], 0\n"
"v_xor_b32 v61, 96, v61\n"
"ds_read_b64_tr_b16 v[48:49], v61 offset:0\n"
"ds_read_b64_tr_b16 v[50:51], v61 offset:512\n"
"s_waitcnt vmcnt(1)\n"
"ds_read_b128 v[4:7], v60 offset:0\n"
"ds_read_b128 v[8:11], v60 offset:1024\n"
"ds_read_b128 v[12:15], v60 offset:2048\n"
"ds_read_b128 v[16:19], v60 offset:3072\n"
"LB_72:\n"
"s_waitcnt lgkmcnt(3)\n"
"v_mfma_f32_16x16x32_bf16 v[20:23], v[32:35], v[4:7], 0\n"
"ds_read_b64_tr_b16 v[32:33], v61 offset:4096\n"
"ds_read_b64_tr_b16 v[34:35], v61 offset:4608\n"
"v_bitop3_b32 v63, 1, v4, v6 bitop3:0x10\n"
"s_waitcnt lgkmcnt(4)\n"
"v_mfma_f32_16x16x32_bf16 v[20:23], v[36:39], v[8:11], v[20:23]\n"
"ds_read_b64_tr_b16 v[36:37], v61 offset:8192\n"
"ds_read_b64_tr_b16 v[38:39], v61 offset:8704\n"
"v_bitop3_b32 v63, v63, v8, v10 bitop3:0x10\n"
"s_waitcnt lgkmcnt(5)\n"
"v_mfma_f32_16x16x32_bf16 v[20:23], v[40:43], v[12:15], v[20:23]\n"
"ds_read_b64_tr_b16 v[40:41], v61 offset:12288\n"
"ds_read_b64_tr_b16 v[42:43], v61 offset:12800\n"
"v_bitop3_b32 v63, v63, v12, v14 bitop3:0x10\n"
"s_waitcnt lgkmcnt(6)\n"
"v_mfma_f32_16x16x32_bf16 v[20:23], v[44:47], v[16:19], v[20:23]\n"
"v_xor_b32 v61, 32, v61\n"
"ds_read_b64_tr_b16 v[44:45], v61 offset:0\n"
"ds_read_b64_tr_b16 v[46:47], v61 offset:512\n"
"v_bitop3_b32 v63, v63, v16, v18 bitop3:0x10\n"
"v_cmp_eq_u32 vcc, 0, v63\n"
"v_mfma_f32_16x16x32_bf16 v[24:27], v[48:51], v[4:7], 0\n"
"ds_read_b64_tr_b16 v[48:49], v61 offset:4096\n"
"ds_read_b64_tr_b16 v[50:51], v61 offset:4608\n"
"s_cbranch_vccnz LC_73\n"
"s_waitcnt lgkmcnt(8)\n"
"v_mfma_f32_16x16x32_bf16 v[24:27], v[32:35], v[8:11], v[24:27]\n"
"ds_read_b64_tr_b16 v[32:33], v61 offset:8192\n"
"ds_read_b64_tr_b16 v[34:35], v61 offset:8704\n"
"s_waitcnt lgkmcnt(8)\n"
"v_mfma_f32_16x16x32_bf16 v[24:27], v[36:39], v[12:15], v[24:27]\n"
"ds_read_b64_tr_b16 v[36:37], v61 offset:12288\n"
"ds_read_b64_tr_b16 v[38:39], v61 offset:12800\n"
"ds_write_b128 v60, v[20:23] offset:0\n"
"s_waitcnt lgkmcnt(9)\n"
"v_mfma_f32_16x16x32_bf16 v[24:27], v[40:43], v[16:19], v[24:27]\n"
"v_xor_b32 v61, 96, v61\n"
"ds_read_b64_tr_b16 v[40:41], v61 offset:0\n"
"ds_read_b64_tr_b16 v[42:43], v61 offset:512\n"
"s_waitcnt lgkmcnt(9)\n"
"v_mfma_f32_16x16x32_bf16 v[20:23], v[44:47], v[4:7], 0\n"
"ds_read_b64_tr_b16 v[44:45], v61 offset:4096\n"
"ds_read_b64_tr_b16 v[46:47], v61 offset:4608\n"
"s_waitcnt lgkmcnt(9)\n"
"v_mfma_f32_16x16x32_bf16 v[20:23], v[48:51], v[8:11], v[20:23]\n"
"ds_read_b64_tr_b16 v[48:49], v61 offset:8192\n"
"ds_read_b64_tr_b16 v[50:51], v61 offset:8704\n"
"s_waitcnt lgkmcnt(9)\n"
"v_mfma_f32_16x16x32_bf16 v[20:23], v[32:35], v[12:15], v[20:23]\n"
"ds_read_b64_tr_b16 v[32:33], v61 offset:12288\n"
"ds_read_b64_tr_b16 v[34:35], v61 offset:12800\n"
"v_xor_b32 v61, 32, v61\n"
"ds_write_b128 v60, v[24:27] offset:1024\n"
"s_waitcnt lgkmcnt(10)\n"
"v_mfma_f32_16x16x32_bf16 v[20:23], v[36:39], v[16:19], v[20:23]\n"
"s_waitcnt lgkmcnt(7)\n"
"v_mfma_f32_16x16x32_bf16 v[28:31], v[40:43], v[4:7], v[28:31]\n"
"s_waitcnt lgkmcnt(5)\n"
"v_mfma_f32_16x16x32_bf16 v[28:31], v[44:47], v[8:11], v[28:31]\n"
"s_waitcnt lgkmcnt(3)\n"
"v_mfma_f32_16x16x32_bf16 v[28:31], v[48:51], v[12:15], v[28:31]\n"
"s_nop 1\n"
"ds_write_b128 v60, v[20:23] offset:2048\n"
"s_waitcnt lgkmcnt(2)\n"
"v_mfma_f32_16x16x32_bf16 v[28:31], v[32:35], v[16:19], v[28:31]\n"
"v_mov_b32 v63, 17\n"
"s_lshl_b64 exec, 1, s28\n"
"ds_write_b32 v3, v63 offset:4\n"
"s_mov_b64 exec, -1\n"
"ds_read_b32 v63, v3 offset:4\n"
"s_waitcnt lgkmcnt(0)\n"
"v_cmp_gt_u32 vcc, 17, v63\n"
"s_nop 1\n"
"s_and_b32 s68, vcc_lo, 15\n"
"s_cbranch_scc1 Lf_74\n"
"Lg_75:\n"
"v_xor_b32 v47, 0x3000, v60\n"
"v_xor_b32 v45, 0x1000, v60\n"
"v_xor_b32 v46, 0x2000, v60\n"
"ds_read_b128 v[40:43], v47 offset:2048\n"
"ds_read_b128 v[32:35], v45 offset:0\n"
"ds_read_b128 v[36:39], v46 offset:1024\n"
"s_add_u32 s69, s50, 0xcc0000\n"
"s_waitcnt lgkmcnt(2)\n"
"v_add_f32 v28, v28, v40\n"
"v_add_f32 v29, v29, v41\n"
"v_add_f32 v30, v30, v42\n"
"v_add_f32 v31, v31, v43\n"
"s_waitcnt lgkmcnt(0)\n"
"v_mov_b32 v63, 18\n"
"s_lshl_b64 exec, 1, s28\n"
"ds_write_b32 v3, v63 offset:4\n"
"s_mov_b64 exec, -1\n"
"v_add_f32 v32, v32, v36\n"
"v_add_f32 v33, v33, v37\n"
"v_add_f32 v34, v34, v38\n"
"v_add_f32 v35, v35, v39\n"
"v_add_f32 v28, v28, v32\n"
"v_add_f32 v29, v29, v33\n"
"v_cvt_pk_bf16_f32 v46, v28, v29\n"
"v_add_f32 v30, v30, v34\n"
"v_add_f32 v31, v31, v35\n"
"v_and_b32 v44, -2, v46\n"
"v_cvt_pk_bf16_f32 v45, v30, v31\n"
"buffer_store_dwordx2 v[44:45], v1, s[12:15], s69 offen sc1\n"
"v_mov_b32 v47, v45\n"
"ds_write_b64 v62, v[46:47] offset:12288\n"
"v_bfe_u32 v56, v0, 5, 2\n"
"v_bfe_u32 v57, v0, 8, 1\n"
"v_xor_b32 v56, v56, v57\n"
"v_and_b32 v57, 16, v0\n"
"v_lshl_add_u32 v56, v56, 5, v57\n"
"v_lshrrev_b32 v57, 7, v0\n"
"v_lshl_add_u32 v56, v57, 10, v56\n"
"v_xor_b32 v57, 64, v56\n"
"s_add_u32 m0, s57, 0\n"
"s_nop 0\n"
"buffer_load_dwordx4 v56, s[4:7], s56 offen lds\n"
"s_add_u32 m0, s57, 1024\n"
"s_add_u32 s71, s56, 0x2000\n"
"buffer_load_dwordx4 v57, s[4:7], s71 offen lds\n"
"v_add_u32 v63, 151552, v0\n"
"v_mov_b32 v40, 0x3f803f80\n"
"v_mov_b32 v41, 0x3f803f80\n"
"ds_read_b128 v[34:37], v63\n"
"ds_read_b64 v[38:39], v63 offset:1024\n"
"s_setprio 2\n"
"ds_read_b32 v63, v3 offset:0\n"
"s_waitcnt lgkmcnt(0)\n"
"v_cmp_gt_u32 vcc, 10, v63\n"
"s_nop 1\n"
"s_and_b32 s68, vcc_lo, 15\n"
"s_cbranch_scc1 Lf_76\n"
"Lg_77:\n"
"s_memrealtime s[70:71]\n"
"ds_read_b128 v[20:23], v62 offset:0\n"
"ds_read_b64 v[28:29], v62 offset:8192\n"
"ds_read_b32 v32, v3 offset:780\n"
"ds_read_b96 v[46:48], v62\n"
"ds_read_b32 v50, v62 offset:12\n"
"v_mov_b32 v49, s64\n"
"v_mov_b32 v51, s64\n"
"v_lshrrev_b32 v63, 1, v0\n"
"s_add_u32 s69, s54, 0x600000\n"
"s_waitcnt lgkmcnt(0)\n"
"buffer_store_dwordx4 v[46:49], v0, s[12:15], s69 offen sc1\n"
"buffer_store_dwordx2 v[50:51], v63, s[12:15], s69 offen offset:1024 sc1\n"
"s_lshl_b32 s68, s30, 10\n"
"s_add_u32 s68, s68, 0xef0000\n"
"s_lshl_b32 s69, s31, 7\n"
"s_add_u32 s68, s68, s69\n"
"s_cmp_eq_u32 s28, 0\n"
"s_cselect_b32 s69, 0, 0x80000000\n"
"v_mov_b32 v52, s64\n"
"v_mov_b32 v53, s65\n"
"v_mov_b32 v54, s70\n"
"v_mov_b32 v55, s69\n"
"s_mov_b64 exec, 1\n"
"buffer_store_dwordx3 v[52:54], v55, s[12:15], s68 offen sc1\n"
"s_mov_b64 exec, -1\n"
"v_mfma_f32_16x16x16_bf16 v[42:45], v[20:21], v[28:29], v[34:37]\n"
"s_add_u32 m0, s57, 2048\n"
"s_add_u32 s71, s56, 0x4000\n"
"buffer_load_dwordx4 v56, s[4:7], s71 offen lds\n"
"s_add_u32 m0, s57, 3072\n"
"s_add_u32 s71, s56, 0x6000\n"
"buffer_load_dwordx4 v57, s[4:7], s71 offen lds\n"
"s_add_u32 m0, s57, 4096\n"
"s_add_u32 s71, s56, 0x8000\n"
"buffer_load_dwordx4 v56, s[4:7], s71 offen lds\n"
"s_add_u32 m0, s57, 5120\n"
"s_add_u32 s71, s56, 0xa000\n"
"buffer_load_dwordx4 v57, s[4:7], s71 offen lds\n"
"s_add_u32 m0, s57, 6144\n"
"s_add_u32 s71, s56, 0xc000\n"
"buffer_load_dwordx4 v56, s[4:7], s71 offen lds\n"
"s_add_u32 m0, s57, 7168\n"
"s_add_u32 s71, s56, 0xe000\n"
"buffer_load_dwordx4 v57, s[4:7], s71 offen lds\n"
"s_add_u32 m0, s57, 8192\n"
"s_add_u32 s71, s56, 0x10000\n"
"buffer_load_dwordx4 v56, s[4:7], s71 offen lds\n"
"s_add_u32 m0, s57, 9216\n"
"s_add_u32 s71, s56, 0x12000\n"
"buffer_load_dwordx4 v57, s[4:7], s71 offen lds\n"
"s_barrier\n"
"ds_read_b128 v[24:27], v62 offset:4096\n"
"ds_read_b64 v[30:31], v62 offset:8200\n"
"ds_read_b32 v33, v3 offset:524\n"
"s_waitcnt lgkmcnt(2)\n"
"v_mfma_f32_16x16x16_bf16 v[4:7], v[26:27], v[28:29], v[34:37]\n"
"s_nop 7\n"
"v_cndmask_b32_e64 v42, v42, v4, s[36:37]\n"
"v_cndmask_b32_e64 v43, v43, v5, s[38:39]\n"
"v_cndmask_b32_e64 v44, v44, v6, s[40:41]\n"
"v_cndmask_b32_e64 v45, v45, v7, s[42:43]\n"
"v_max3_f32 v8, v42, v43, v44\n"
"v_max_f32 v8, v8, v45\n"
"v_mov_b32 v9, v8\n"
"s_nop 1\n"
"v_permlane32_swap_b32 v8, v9\n"
"v_max_f32 v8, v8, v9\n"
"v_mov_b32 v9, v8\n"
"s_nop 1\n"
"v_permlane16_swap_b32 v8, v9\n"
"v_max_f32 v8, v8, v9\n"
"v_mul_f32 v8, v8, v32\n"
"v_fma_f32 v42, v42, v32, -v8\n"
"v_fma_f32 v43, v43, v32, -v8\n"
"v_fma_f32 v44, v44, v32, -v8\n"
"v_fma_f32 v45, v45, v32, -v8\n"
"v_exp_f32 v42, v42\n"
"v_exp_f32 v43, v43\n"
"v_exp_f32 v44, v44\n"
"v_exp_f32 v45, v45\n"
"v_cvt_pk_bf16_f32 v10, v42, v43\n"
"v_cvt_pk_bf16_f32 v11, v44, v45\n"
"v_and_b32 v14, v10, v38\n"
"v_and_b32 v15, v11, v39\n"
"v_mfma_f32_16x16x16_bf16 v[4:7], v[40:41], v[10:11], 0\n"
"v_xor_b32 v12, v10, v14\n"
"v_xor_b32 v13, v11, v15\n"
"s_nop 1\n"
"v_mfma_f32_16x16x32_bf16 v[42:45], v[22:25], v[12:15], 0\n"
"s_lshl_b32 s68, s30, 10\n"
"s_add_u32 s68, s68, 0xef0000\n"
"v_bfe_u32 v52, v0, 4, 3\n"
"v_lshlrev_b32 v52, 7, v52\n"
"buffer_load_dwordx3 v[52:54], v52, s[12:15], s68 offen sc1\n"
"s_add_u32 s69, s50, 0xacc000\n"
"v_rcp_f32 v8, v4\n"
"s_nop 0\n"
"v_mul_f32 v42, v42, v8\n"
"v_mul_f32 v43, v43, v8\n"
"v_mul_f32 v44, v44, v8\n"
"v_mul_f32 v45, v45, v8\n"
"v_cvt_pk_bf16_f32 v10, v42, v43\n"
"v_cvt_pk_bf16_f32 v11, v44, v45\n"
"v_or_b32 v10, 1, v10\n"
"buffer_store_dwordx2 v[10:11], v1, s[12:15], s69 offen sc1\n"
"s_waitcnt lgkmcnt(0)\n"
"v_mfma_f32_16x16x16_bf16 v[42:45], v[26:27], v[30:31], v[34:37]\n"
"s_add_u32 m0, s57, 10240\n"
"s_add_u32 s71, s56, 0x14000\n"
"buffer_load_dwordx4 v56, s[4:7], s71 offen lds\n"
"s_add_u32 m0, s57, 11264\n"
"s_add_u32 s71, s56, 0x16000\n"
"buffer_load_dwordx4 v57, s[4:7], s71 offen lds\n"
"s_add_u32 m0, s57, 12288\n"
"s_add_u32 s71, s56, 0x18000\n"
"buffer_load_dwordx4 v56, s[4:7], s71 offen lds\n"
"s_add_u32 m0, s57, 13312\n"
"s_add_u32 s71, s56, 0x1a000\n"
"buffer_load_dwordx4 v57, s[4:7], s71 offen lds\n"
"s_add_u32 m0, s57, 14336\n"
"s_add_u32 s71, s56, 0x1c000\n"
"buffer_load_dwordx4 v56, s[4:7], s71 offen lds\n"
"s_add_u32 m0, s57, 15360\n"
"s_add_u32 s71, s56, 0x1e000\n"
"buffer_load_dwordx4 v57, s[4:7], s71 offen lds\n"
"s_waitcnt vmcnt(7)\n"
"v_xor_b32 v52, s64, v52\n"
"v_xor_b32 v53, s65, v53\n"
"v_or_b32 v52, v52, v53\n"
"v_cmp_ne_u32 vcc, 0, v52\n"
"s_nop 1\n"
"s_cbranch_vccnz Lr_78\n"
"Lk_79:\n"
"s_memrealtime s[70:71]\n"
"s_waitcnt lgkmcnt(0)\n"
"v_subrev_u32 v53, s70, v54\n"
"s_nop 1\n"
"v_max_i32_dpp v53, v53, v53 quad_perm:[1,0,3,2] row_mask:0xf bank_mask:0xf\n"
"s_nop 1\n"
"v_max_i32_dpp v53, v53, v53 quad_perm:[2,3,0,1] row_mask:0xf bank_mask:0xf\n"
"s_nop 1\n"
"v_max_i32_dpp v53, v53, v53 row_half_mirror row_mask:0xf bank_mask:0xf\n"
"s_nop 0\n"
"v_readfirstlane_b32 s68, v53\n"
"s_nop 1\n"
"s_add_i32 s68, s68, 150\n"
"s_min_i32 s68, s68, 150\n"
"s_cmp_le_i32 s68, 2\n"
"s_cbranch_scc1 La_81\n"
"s_add_u32 s62, s68, s70\n"
"s_mov_b32 s69, 0\n"
"La_80:\n"
"s_memrealtime s[70:71]\n"
"s_add_u32 s69, s69, 1\n"
"s_waitcnt lgkmcnt(0)\n"
"s_cmp_gt_u32 s69, 200\n"
"s_cbranch_scc1 La_81\n"
"s_sub_u32 s68, s70, s62\n"
"s_cmp_lt_i32 s68, 0\n"
"s_cbranch_scc1 La_80\n"
"La_81:\n"
"s_waitcnt vmcnt(0)\n"
"s_barrier\n"
"ds_read_b128 v[20:23], v62 offset:0\n"
"ds_read_b64 v[28:29], v62 offset:8192\n"
"ds_read_b32 v32, v3 offset:268\n"
"s_waitcnt lgkmcnt(2)\n"
"v_mfma_f32_16x16x16_bf16 v[4:7], v[20:21], v[30:31], v[34:37]\n"
"s_nop 7\n"
"v_cndmask_b32_e64 v42, v42, v4, s[36:37]\n"
"v_cndmask_b32_e64 v43, v43, v5, s[38:39]\n"
"v_cndmask_b32_e64 v44, v44, v6, s[40:41]\n"
"v_cndmask_b32_e64 v45, v45, v7, s[42:43]\n"
"v_max3_f32 v8, v42, v43, v44\n"
"v_max_f32 v8, v8, v45\n"
"v_mov_b32 v9, v8\n"
"s_nop 1\n"
"v_permlane32_swap_b32 v8, v9\n"
"v_max_f32 v8, v8, v9\n"
"v_mov_b32 v9, v8\n"
"s_nop 1\n"
"v_permlane16_swap_b32 v8, v9\n"
"v_max_f32 v8, v8, v9\n"
"v_mul_f32 v8, v8, v33\n"
"v_fma_f32 v42, v42, v33, -v8\n"
"v_fma_f32 v43, v43, v33, -v8\n"
"v_fma_f32 v44, v44, v33, -v8\n"
"v_fma_f32 v45, v45, v33, -v8\n"
"v_exp_f32 v42, v42\n"
"v_exp_f32 v43, v43\n"
"v_exp_f32 v44, v44\n"
"v_exp_f32 v45, v45\n"
"v_cvt_pk_bf16_f32 v10, v42, v43\n"
"v_cvt_pk_bf16_f32 v11, v44, v45\n"
"v_and_b32 v12, v10, v38\n"
"v_and_b32 v13, v11, v39\n"
"v_mfma_f32_16x16x16_bf16 v[4:7], v[40:41], v[10:11], 0\n"
"v_xor_b32 v14, v10, v12\n"
"v_xor_b32 v15, v11, v13\n"
"s_nop 1\n"
"v_mfma_f32_16x16x32_bf16 v[42:45], v[22:25], v[12:15], 0\n"
"s_add_u32 s69, s50, 0xac8000\n"
"s_nop 1\n"
"v_rcp_f32 v8, v4\n"
"s_nop 3\n"
"v_mul_f32 v42, v42, v8\n"
"v_mul_f32 v43, v43, v8\n"
"v_mul_f32 v44, v44, v8\n"
"v_mul_f32 v45, v45, v8\n"
"v_cvt_pk_bf16_f32 v10, v42, v43\n"
"v_cvt_pk_bf16_f32 v11, v44, v45\n"
"v_or_b32 v10, 1, v10\n"
"buffer_store_dwordx2 v[10:11], v1, s[12:15], s69 offen sc1\n"
"s_waitcnt lgkmcnt(0)\n"
"v_mfma_f32_16x16x16_bf16 v[42:45], v[20:21], v[28:29], v[34:37]\n"
"v_or_b32 v46, s60, v0\n"
"v_lshrrev_b32 v50, 1, v0\n"
"v_or_b32 v50, s60, v50\n"
"s_add_u32 s69, s54, 0x5f0000\n"
"buffer_load_dwordx4 v[46:49], v46, s[12:15], s69 offen sc1\n"
"buffer_load_dwordx2 v[50:51], v50, s[12:15], s69 offen offset:1024 sc1\n"
"s_barrier\n"
"ds_read_b128 v[24:27], v62 offset:4096\n"
"ds_read_b64 v[30:31], v62 offset:8200\n"
"ds_read_b32 v33, v3 offset:12\n"
"s_waitcnt lgkmcnt(2)\n"
"v_mfma_f32_16x16x16_bf16 v[4:7], v[26:27], v[28:29], v[34:37]\n"
"s_nop 7\n"
"v_cndmask_b32_e64 v42, v42, v4, s[36:37]\n"
"v_cndmask_b32_e64 v43, v43, v5, s[38:39]\n"
"v_cndmask_b32_e64 v44, v44, v6, s[40:41]\n"
"v_cndmask_b32_e64 v45, v45, v7, s[42:43]\n"
"v_max3_f32 v8, v42, v43, v44\n"
"v_max_f32 v8, v8, v45\n"
"v_mov_b32 v9, v8\n"
"s_nop 1\n"
"v_permlane32_swap_b32 v8, v9\n"
"v_max_f32 v8, v8, v9\n"
"v_mov_b32 v9, v8\n"
"s_nop 1\n"
"v_permlane16_swap_b32 v8, v9\n"
"v_max_f32 v8, v8, v9\n"
"v_mul_f32 v8, v8, v32\n"
"v_fma_f32 v42, v42, v32, -v8\n"
"v_fma_f32 v43, v43, v32, -v8\n"
"v_fma_f32 v44, v44, v32, -v8\n"
"v_fma_f32 v45, v45, v32, -v8\n"
"v_exp_f32 v42, v42\n"
"v_exp_f32 v43, v43\n"
"v_exp_f32 v44, v44\n"
"v_exp_f32 v45, v45\n"
"v_cvt_pk_bf16_f32 v10, v42, v43\n"
"v_cvt_pk_bf16_f32 v11, v44, v45\n"
"v_and_b32 v14, v10, v38\n"
"v_and_b32 v15, v11, v39\n"
"v_mfma_f32_16x16x16_bf16 v[4:7], v[40:41], v[10:11], 0\n"
"v_xor_b32 v12, v10, v14\n"
"v_xor_b32 v13, v11, v15\n"
"s_nop 1\n"
"v_mfma_f32_16x16x32_bf16 v[42:45], v[22:25], v[12:15], 0\n"
"s_add_u32 s69, s50, 0xac4000\n"
"s_nop 1\n"
"v_rcp_f32 v8, v4\n"
"s_nop 3\n"
"v_mul_f32 v42, v42, v8\n"
"v_mul_f32 v43, v43, v8\n"
"v_mul_f32 v44, v44, v8\n"
"v_mul_f32 v45, v45, v8\n"
"v_cvt_pk_bf16_f32 v10, v42, v43\n"
"v_cvt_pk_bf16_f32 v11, v44, v45\n"
"v_or_b32 v10, 1, v10\n"
"buffer_store_dwordx2 v[10:11], v1, s[12:15], s69 offen sc1\n"
"s_waitcnt lgkmcnt(0)\n"
"v_mfma_f32_16x16x16_bf16 v[42:45], v[26:27], v[30:31], v[34:37]\n"
"s_barrier\n"
"s_waitcnt vmcnt(1)\n"
"v_xor_b32 v4, s64, v49\n"
"v_xor_b32 v5, s64, v51\n"
"v_or_b32 v4, v4, v5\n"
"v_cmp_ne_u32 vcc, 0, v4\n"
"s_cmp_lg_u32 s60, 0\n"
"s_nop 0\n"
"s_cselect_b64 vcc, 0, vcc\n"
"s_cbranch_vccnz Lr_82\n"
"Lk_83:\n"
"v_min_f32 v4, s61, v34\n"
"v_min_f32 v5, s61, v35\n"
"v_min_f32 v6, s61, v36\n"
"v_min_f32 v7, s61, v37\n"
"v_mov_b32 v22, v48\n"
"v_mov_b32 v23, v50\n"
"v_mfma_f32_16x16x16_bf16 v[4:7], v[46:47], v[30:31], v[4:7]\n"
"s_nop 7\n"
"v_cndmask_b32_e64 v42, v42, v4, s[36:37]\n"
"v_cndmask_b32_e64 v43, v43, v5, s[38:39]\n"
"v_cndmask_b32_e64 v44, v44, v6, s[40:41]\n"
"v_cndmask_b32_e64 v45, v45, v7, s[42:43]\n"
"v_max3_f32 v8, v42, v43, v44\n"
"v_max_f32 v8, v8, v45\n"
"v_mov_b32 v9, v8\n"
"s_nop 1\n"
"v_permlane32_swap_b32 v8, v9\n"
"v_max_f32 v8, v8, v9\n"
"v_mov_b32 v9, v8\n"
"s_nop 1\n"
"v_permlane16_swap_b32 v8, v9\n"
"v_max_f32 v8, v8, v9\n"
"v_mul_f32 v8, v8, v33\n"
"v_fma_f32 v42, v42, v33, -v8\n"
"v_fma_f32 v43, v43, v33, -v8\n"
"v_fma_f32 v44, v44, v33, -v8\n"
"v_fma_f32 v45, v45, v33, -v8\n"
"v_exp_f32 v42, v42\n"
"v_exp_f32 v43, v43\n"
"v_exp_f32 v44, v44\n"
"v_exp_f32 v45, v45\n"
"v_cvt_pk_bf16_f32 v10, v42, v43\n"
"v_cvt_pk_bf16_f32 v11, v44, v45\n"
"v_and_b32 v12, v10, v38\n"
"v_and_b32 v13, v11, v39\n"
"v_mfma_f32_16x16x16_bf16 v[4:7], v[40:41], v[10:11], 0\n"
"v_xor_b32 v14, v10, v12\n"
"v_xor_b32 v15, v11, v13\n"
"s_nop 1\n"
"v_mfma_f32_16x16x32_bf16 v[42:45], v[22:25], v[12:15], 0\n"
"s_add_u32 s69, s50, 0xac0000\n"
"s_nop 1\n"
"v_rcp_f32 v8, v4\n"
"s_nop 3\n"
"v_mul_f32 v42, v42, v8\n"
"v_mul_f32 v43, v43, v8\n"
"v_mul_f32 v44, v44, v8\n"
"v_mul_f32 v45, v45, v8\n"
"v_cvt_pk_bf16_f32 v10, v42, v43\n"
"v_cvt_pk_bf16_f32 v11, v44, v45\n"
"v_or_b32 v10, 1, v10\n"
"buffer_store_dwordx2 v[10:11], v1, s[12:15], s69 offen sc1\n"
"s_sleep 12\n"
"s_add_u32 m0, s58, 0\n"
"s_add_u32 s69, s51, 0xac0000\n"
"buffer_load_dwordx4 v0, s[12:15], s69 offen offset:0 sc1 lds\n"
"buffer_load_dwordx4 v0, s[12:15], s69 offen offset:1024 sc1 lds\n"
"buffer_load_dwordx4 v0, s[12:15], s69 offen offset:2048 sc1 lds\n"
"buffer_load_dwordx4 v0, s[12:15], s69 offen offset:3072 sc1 lds\n"
"s_barrier\n"
"v_add_u32 v63, 152576, v0\n"
"ds_read_b64 v[50:51], v62 offset:12288\n"
"ds_read_b64 v[48:49], v63 offset:8\n"
"ds_read_b64_tr_b16 v[32:33], v61 offset:0\n"
"ds_read_b64_tr_b16 v[34:35], v61 offset:512\n"
"ds_read_b64_tr_b16 v[36:37], v61 offset:4096\n"
"ds_read_b64_tr_b16 v[38:39], v61 offset:4608\n"
"ds_read_b64_tr_b16 v[40:41], v61 offset:8192\n"
"ds_read_b64_tr_b16 v[42:43], v61 offset:8704\n"
"ds_read_b64_tr_b16 v[44:45], v61 offset:12288\n"
"ds_read_b64_tr_b16 v[46:47], v61 offset:12800\n"
"s_waitcnt lgkmcnt(8)\n"
"v_mfma_f32_16x16x16_bf16 v[28:31], v[48:49], v[50:51], 0\n"
"v_xor_b32 v61, 96, v61\n"
"ds_read_b64_tr_b16 v[48:49], v61 offset:0\n"
"ds_read_b64_tr_b16 v[50:51], v61 offset:512\n"
"s_waitcnt vmcnt(0)\n"
"ds_read_b128 v[4:7], v60 offset:0\n"
"ds_read_b128 v[8:11], v60 offset:1024\n"
"ds_read_b128 v[12:15], v60 offset:2048\n"
"ds_read_b128 v[16:19], v60 offset:3072\n"
"LB_84:\n"
"s_waitcnt lgkmcnt(3)\n"
"v_mfma_f32_16x16x32_bf16 v[20:23], v[32:35], v[4:7], 0\n"
"ds_read_b64_tr_b16 v[32:33], v61 offset:4096\n"
"ds_read_b64_tr_b16 v[34:35], v61 offset:4608\n"
"v_bitop3_b32 v63, 1, v4, v6 bitop3:0x80\n"
"s_waitcnt lgkmcnt(4)\n"
"v_mfma_f32_16x16x32_bf16 v[20:23], v[36:39], v[8:11], v[20:23]\n"
"ds_read_b64_tr_b16 v[36:37], v61 offset:8192\n"
"ds_read_b64_tr_b16 v[38:39], v61 offset:8704\n"
"v_bitop3_b32 v63, v63, v8, v10 bitop3:0x80\n"
"s_waitcnt lgkmcnt(5)\n"
"v_mfma_f32_16x16x32_bf16 v[20:23], v[40:43], v[12:15], v[20:23]\n"
"ds_read_b64_tr_b16 v[40:41], v61 offset:12288\n"
"ds_read_b64_tr_b16 v[42:43], v61 offset:12800\n"
"v_bitop3_b32 v63, v63, v12, v14 bitop3:0x80\n"
"s_waitcnt lgkmcnt(6)\n"
"v_mfma_f32_16x16x32_bf16 v[20:23], v[44:47], v[16:19], v[20:23]\n"
"v_xor_b32 v61, 32, v61\n"
"ds_read_b64_tr_b16 v[44:45], v61 offset:0\n"
"ds_read_b64_tr_b16 v[46:47], v61 offset:512\n"
"v_bitop3_b32 v63, v63, v16, v18 bitop3:0x80\n"
"v_cmp_eq_u32 vcc, 0, v63\n"
"v_mfma_f32_16x16x32_bf16 v[24:27], v[48:51], v[4:7], 0\n"
"ds_read_b64_tr_b16 v[48:49], v61 offset:4096\n"
"ds_read_b64_tr_b16 v[50:51], v61 offset:4608\n"
"s_cbranch_vccnz LC_85\n"
"s_waitcnt lgkmcnt(8)\n"
"v_mfma_f32_16x16x32_bf16 v[24:27], v[32:35], v[8:11], v[24:27]\n"
"ds_read_b64_tr_b16 v[32:33], v61 offset:8192\n"
"ds_read_b64_tr_b16 v[34:35], v61 offset:8704\n"
"s_waitcnt lgkmcnt(8)\n"
"v_mfma_f32_16x16x32_bf16 v[24:27], v[36:39], v[12:15], v[24:27]\n"
"ds_read_b64_tr_b16 v[36:37], v61 offset:12288\n"
"ds_read_b64_tr_b16 v[38:39], v61 offset:12800\n"
"ds_write_b128 v60, v[20:23] offset:0\n"
"s_waitcnt lgkmcnt(9)\n"
"v_mfma_f32_16x16x32_bf16 v[24:27], v[40:43], v[16:19], v[24:27]\n"
"v_xor_b32 v61, 96, v61\n"
"ds_read_b64_tr_b16 v[40:41], v61 offset:0\n"
"ds_read_b64_tr_b16 v[42:43], v61 offset:512\n"
"s_waitcnt lgkmcnt(9)\n"
"v_mfma_f32_16x16x32_bf16 v[20:23], v[44:47], v[4:7], 0\n"
"ds_read_b64_tr_b16 v[44:45], v61 offset:4096\n"
"ds_read_b64_tr_b16 v[46:47], v61 offset:4608\n"
"s_waitcnt lgkmcnt(9)\n"
"v_mfma_f32_16x16x32_bf16 v[20:23], v[48:51], v[8:11], v[20:23]\n"
"ds_read_b64_tr_b16 v[48:49], v61 offset:8192\n"
"ds_read_b64_tr_b16 v[50:51], v61 offset:8704\n"
"s_waitcnt lgkmcnt(9)\n"
"v_mfma_f32_16x16x32_bf16 v[20:23], v[32:35], v[12:15], v[20:23]\n"
"ds_read_b64_tr_b16 v[32:33], v61 offset:12288\n"
"ds_read_b64_tr_b16 v[34:35], v61 offset:12800\n"
"v_xor_b32 v61, 32, v61\n"
"ds_write_b128 v60, v[24:27] offset:1024\n"
"s_waitcnt lgkmcnt(10)\n"
"v_mfma_f32_16x16x32_bf16 v[20:23], v[36:39], v[16:19], v[20:23]\n"
"s_waitcnt lgkmcnt(7)\n"
"v_mfma_f32_16x16x32_bf16 v[28:31], v[40:43], v[4:7], v[28:31]\n"
"s_waitcnt lgkmcnt(5)\n"
"v_mfma_f32_16x16x32_bf16 v[28:31], v[44:47], v[8:11], v[28:31]\n"
"s_waitcnt lgkmcnt(3)\n"
"v_mfma_f32_16x16x32_bf16 v[28:31], v[48:51], v[12:15], v[28:31]\n"
"s_nop 1\n"
"ds_write_b128 v60, v[20:23] offset:2048\n"
"s_waitcnt lgkmcnt(2)\n"
"v_mfma_f32_16x16x32_bf16 v[28:31], v[32:35], v[16:19], v[28:31]\n"
"s_waitcnt lgkmcnt(0)\n"
"s_barrier\n"
"v_xor_b32 v47, 0x3000, v60\n"
"v_xor_b32 v45, 0x1000, v60\n"
"v_xor_b32 v46, 0x2000, v60\n"
"ds_read_b128 v[40:43], v47 offset:2048\n"
"ds_read_b128 v[32:35], v45 offset:0\n"
"ds_read_b128 v[36:39], v46 offset:1024\n"
"v_bfe_u32 v48, v0, 4, 4\n"
"v_lshrrev_b32 v49, 8, v0\n"
"v_lshlrev_b32 v49, 3, v49\n"
"v_lshl_add_u32 v48, v48, 11, v49\n"
"s_waitcnt lgkmcnt(2)\n"
"v_add_f32 v28, v28, v40\n"
"v_add_f32 v29, v29, v41\n"
"v_add_f32 v30, v30, v42\n"
"v_add_f32 v31, v31, v43\n"
"s_waitcnt lgkmcnt(0)\n"
"v_add_f32 v32, v32, v36\n"
"v_add_f32 v33, v33, v37\n"
"v_add_f32 v34, v34, v38\n"
"v_add_f32 v35, v35, v39\n"
"v_add_f32 v28, v28, v32\n"
"v_add_f32 v29, v29, v33\n"
"v_add_f32 v30, v30, v34\n"
"v_add_f32 v31, v31, v35\n"
"v_cvt_pk_bf16_f32 v44, v28, v29\n"
"v_cvt_pk_bf16_f32 v45, v30, v31\n"
"buffer_store_dwordx2 v[44:45], v48, s[16:19], s55 offen\n"
"s_branch L_FIN\n"
"L_SETY:\n"
"s_nop 4\n"
"s_mov_b32 s24, s51\n"
"s_add_u32 s68, s24, 589824\n"
"buffer_load_dwordx4 v[64:67], v13, s[20:23], s68 offen\n"
"s_add_u32 s68, s24, 590848\n"
"buffer_load_dwordx4 v[68:71], v13, s[20:23], s68 offen\n"
"s_add_u32 s68, s24, 591872\n"
"buffer_load_dwordx4 v[72:75], v13, s[20:23], s68 offen\n"
"s_add_u32 s68, s24, 614400\n"
"buffer_load_dwordx4 v[76:79], v14, s[20:23], s68 offen\n"
"s_add_u32 s68, s24, 615424\n"
"buffer_load_dwordx4 v[80:83], v14, s[20:23], s68 offen\n"
"s_add_u32 s68, s24, 616448\n"
"buffer_load_dwordx4 v[84:87], v14, s[20:23], s68 offen\n"
"s_add_u32 s68, s24, 786432\n"
"buffer_load_dwordx4 v[88:91], v13, s[20:23], s68 offen\n"
"s_add_u32 s68, s24, 787456\n"
"buffer_load_dwordx4 v[92:95], v13, s[20:23], s68 offen\n"
"s_add_u32 s68, s24, 788480\n"
"buffer_load_dwordx4 v[96:99], v13, s[20:23], s68 offen\n"
"s_add_u32 s68, s24, 811008\n"
"buffer_load_dwordx4 v[100:103], v14, s[20:23], s68 offen\n"
"s_add_u32 s68, s24, 812032\n"
"buffer_load_dwordx4 v[104:107], v14, s[20:23], s68 offen\n"
"s_add_u32 s68, s24, 813056\n"
"buffer_load_dwordx4 v[108:111], v14, s[20:23], s68 offen\n"
"s_add_u32 s68, s24, 983040\n"
"buffer_load_dwordx4 v[112:115], v13, s[20:23], s68 offen\n"
"s_add_u32 s68, s24, 984064\n"
"buffer_load_dwordx4 v[116:119], v13, s[20:23], s68 offen\n"
"s_add_u32 s68, s24, 985088\n"
"buffer_load_dwordx4 v[120:123], v13, s[20:23], s68 offen\n"
"s_add_u32 s68, s24, 1007616\n"
"buffer_load_dwordx4 v[124:127], v14, s[20:23], s68 offen\n"
"s_add_u32 s68, s24, 1008640\n"
"buffer_load_dwordx4 v[128:131], v14, s[20:23], s68 offen\n"
"s_add_u32 s68, s24, 1009664\n"
"buffer_load_dwordx4 v[132:135], v14, s[20:23], s68 offen\n"
"s_mov_b32 s8, s80\n"
"s_and_b32 s9, s81, 0xffff\n"
"s_mov_b32 s10, 0x200000\n"
"s_mov_b32 s11, 0x20000\n"
"s_mov_b32 s4, s84\n"
"s_and_b32 s5, s85, 0xffff\n"
"s_mov_b32 s6, 0x80000\n"
"s_mov_b32 s7, 0x20000\n"
"s_mov_b32 s12, s92\n"
"s_and_b32 s13, s93, 0xffff\n"
"s_mov_b32 s14, 0xf00000\n"
"s_mov_b32 s15, 0x20000\n"
"s_and_b32 s68, s30, 15\n"
"s_lshr_b32 s69, s30, 4\n"
"s_lshl_b32 s62, s68, 17\n"
"s_lshl_b32 s69, s69, 10\n"
"s_add_u32 s62, s62, s69\n"
"v_lshrrev_b32 v10, 3, v1\n"
"v_lshrrev_b32 v11, 4, v1\n"
"v_xor_b32 v11, v11, v8\n"
"v_lshlrev_b32 v9, 11, v10\n"
"v_lshl_add_u32 v9, v11, 4, v9\n"
"s_lshl_b32 s25, s28, 12\n"
"s_lshl_b32 s26, s28, 8\n"
"s_add_u32 s26, s26, s62\n"
"s_add_u32 s68, s52, 65536\n"
"v_add_u32 v23, s68, v0\n"
"s_mov_b32 s16, s90\n"
"s_and_b32 s17, s91, 0xffff\n"
"s_mov_b32 s18, 0x200000\n"
"s_mov_b32 s19, 0x20000\n"
"s_and_b32 s70, s30, 15\n"
"s_cmp_eq_u32 s70, 0\n"
"s_cselect_b32 s60, 0x80000000, 0\n"
"s_or_b32 s61, s60, 0x7f800000\n"
"s_lshl_b32 s68, s30, 16\n"
"s_lshl_b32 s58, s28, 12\n"
"s_add_u32 s51, s68, s58\n"
"s_lshl_b32 s69, s31, 11\n"
"s_lshl_b32 s70, s28, 9\n"
"s_add_u32 s69, s69, s70\n"
"s_add_u32 s50, s68, s69\n"
"s_lshl_b32 s52, s34, 9\n"
"s_add_u32 s52, s52, 1024\n"
"s_and_b32 s68, s28, 1\n"
"s_lshl_b32 s68, s68, 3\n"
"s_lshr_b32 s70, s28, 1\n"
"s_lshl_b32 s70, s70, 8\n"
"s_add_u32 s53, s68, s70\n"
"s_lshl_b32 s70, s28, 5\n"
"s_lshl_b32 s69, s31, 7\n"
"s_add_u32 s55, s62, s69\n"
"s_add_u32 s55, s55, s70\n"
"s_lshl_b32 s70, s28, 14\n"
"s_add_u32 s57, s70, 65536\n"
"s_lshl_b32 s69, s31, 7\n"
"s_lshl_b32 s70, s28, 17\n"
"s_add_u32 s56, s69, s70\n"
"s_lshl_b32 s69, s31, 2\n"
"s_add_u32 s69, s69, s28\n"
"s_mul_i32 s59, s69, 49152\n"
"s_add_u32 s59, s59, 0x840000\n"
"s_lshl_b32 s68, s30, 5\n"
"s_add_u32 s68, s68, s69\n"
"s_lshl_b32 s68, s68, 11\n"
"s_add_u32 s54, s68, 0x40000\n"
"s_mov_b32 s72, 0xbb000000\n"
"s_mov_b32 s73, 0x3eb8aa3b\n"
"s_mov_b32 s63, 0\n"
"s_mov_b32 s27, 0\n"
"s_mov_b32 s44, 0xaaaaaaaa\n"
"s_mov_b32 s45, 0xaaaaaaaa\n"
"s_mov_b32 s46, 0xcccccccc\n"
"s_mov_b32 s47, 0xcccccccc\n"
"s_mov_b32 s48, 0x00f0000f\n"
"s_mov_b32 s49, 0xf0000f00\n"
"v_lshrrev_b32 v4, 4, v0\n"
"v_lshrrev_b32 v15, 4, v4\n"
"v_and_b32 v16, 15, v4\n"
"v_lshrrev_b32 v17, 5, v4\n"
"v_lshlrev_b32 v17, 8, v17\n"
"v_lshl_add_u32 v17, v16, 4, v17\n"
"v_and_b32 v18, 1, v15\n"
"v_lshl_add_u32 v1, v18, 3, v17\n"
"v_lshlrev_b32 v17, 4, v16\n"
"v_add_u32 v3, 154624, v17\n"
"v_add_u32 v60, s58, v0\n"
"s_lshl_b32 s68, s28, 10\n"
"s_add_u32 s68, s68, 131072\n"
"v_add_u32 v62, s68, v0\n"
"v_lshlrev_b32 v17, 2, v15\n"
"v_sub_u32 v17, v17, v16\n"
"v_cmp_lt_i32 s[36:37], 0, v17\n"
"v_cmp_lt_i32 s[38:39], -1, v17\n"
"v_cmp_lt_i32 s[40:41], -2, v17\n"
"v_cmp_lt_i32 s[42:43], -3, v17\n"
"s_barrier\n"
"v_mov_b32 v8, 153608\n"
"ds_read_b64 v[6:7], v8\n"
"s_add_u32 s68, s24, 1179648\n"
"buffer_load_dwordx4 v[136:139], v13, s[20:23], s68 offen\n"
"s_add_u32 s68, s24, 1180672\n"
"buffer_load_dwordx4 v[140:143], v13, s[20:23], s68 offen\n"
"s_add_u32 s68, s24, 1181696\n"
"buffer_load_dwordx4 v[144:147], v13, s[20:23], s68 offen\n"
"s_add_u32 s68, s24, 1204224\n"
"buffer_load_dwordx4 v[148:151], v14, s[20:23], s68 offen\n"
"s_add_u32 s68, s24, 1205248\n"
"buffer_load_dwordx4 v[152:155], v14, s[20:23], s68 offen\n"
"s_add_u32 s68, s24, 1206272\n"
"buffer_load_dwordx4 v[156:159], v14, s[20:23], s68 offen\n"
"s_waitcnt lgkmcnt(0)\n"
"s_barrier\n"
"v_readfirstlane_b32 s64, v6\n"
"v_readfirstlane_b32 s65, v7\n"
"s_nop 0\n"
"s_add_u32 s64, s64, 1\n"
"s_addc_u32 s65, s65, 0\n"
"s_add_u32 s68, s24, 1376256\n"
"buffer_load_dwordx4 v[160:163], v13, s[20:23], s68 offen\n"
"s_add_u32 s68, s24, 1377280\n"
"buffer_load_dwordx4 v[164:167], v13, s[20:23], s68 offen\n"
"s_add_u32 s68, s24, 1378304\n"
"buffer_load_dwordx4 v[168:171], v13, s[20:23], s68 offen\n"
"s_add_u32 s68, s24, 1400832\n"
"buffer_load_dwordx4 v[172:175], v14, s[20:23], s68 offen\n"
"s_add_u32 s68, s24, 1401856\n"
"buffer_load_dwordx4 v[176:179], v14, s[20:23], s68 offen\n"
"s_add_u32 s68, s24, 1402880\n"
"buffer_load_dwordx4 v[180:183], v14, s[20:23], s68 offen\n"
"s_barrier\n"
"s_add_u32 m0, s25, 49152\n"
"s_add_u32 s68, s26, 98304\n"
"buffer_load_dwordx4 v9, s[8:11], s68 offen lds\n"
"s_add_u32 m0, s25, 50176\n"
"s_add_u32 s68, s26, 114688\n"
"buffer_load_dwordx4 v9, s[8:11], s68 offen lds\n"
"s_add_u32 m0, s25, 51200\n"
"s_add_u32 s68, s26, 98432\n"
"buffer_load_dwordx4 v9, s[8:11], s68 offen lds\n"
"s_add_u32 m0, s25, 52224\n"
"s_add_u32 s68, s26, 114816\n"
"buffer_load_dwordx4 v9, s[8:11], s68 offen lds\n"
"s_waitcnt vmcnt(28)\n"
"ds_write_b128 v23, v[64:67] offset:0\n"
"ds_write_b128 v23, v[68:71] offset:8192\n"
"ds_write_b128 v23, v[72:75] offset:16384\n"
"ds_write_b128 v23, v[76:79] offset:1024\n"
"ds_write_b128 v23, v[80:83] offset:9216\n"
"ds_write_b128 v23, v[84:87] offset:17408\n"
"s_waitcnt lgkmcnt(0)\n"
"s_barrier\n"
"s_add_u32 m0, s25, 32768\n"
"s_add_u32 s68, s26, 65536\n"
"buffer_load_dwordx4 v9, s[8:11], s68 offen lds\n"
"s_add_u32 m0, s25, 33792\n"
"s_add_u32 s68, s26, 81920\n"
"buffer_load_dwordx4 v9, s[8:11], s68 offen lds\n"
"s_add_u32 m0, s25, 34816\n"
"s_add_u32 s68, s26, 65664\n"
"buffer_load_dwordx4 v9, s[8:11], s68 offen lds\n"
"s_add_u32 m0, s25, 35840\n"
"s_add_u32 s68, s26, 82048\n"
"buffer_load_dwordx4 v9, s[8:11], s68 offen lds\n"
"s_lshr_b32 s68, s35, 3\n"
"s_lshl_b32 s68, s68, 2\n"
"s_add_u32 s68, s68, s28\n"
"s_lshl_b32 s68, s68, 12\n"
"v_lshlrev_b32 v21, 3, v0\n"
"s_mov_b32 m0, 161792\n"
"s_mov_b32 exec_hi, 0\n"
"buffer_load_dword v21, s[4:7], s68 offen lds\n"
"s_mov_b32 exec_hi, -1\n"
"s_waitcnt vmcnt(27)\n"
"ds_write_b128 v23, v[88:91] offset:24576\n"
"ds_write_b128 v23, v[92:95] offset:32768\n"
"ds_write_b128 v23, v[96:99] offset:40960\n"
"ds_write_b128 v23, v[100:103] offset:25600\n"
"ds_write_b128 v23, v[104:107] offset:33792\n"
"ds_write_b128 v23, v[108:111] offset:41984\n"
"s_waitcnt lgkmcnt(0)\n"
"s_barrier\n"
"s_add_u32 m0, s25, 16384\n"
"s_add_u32 s68, s26, 32768\n"
"buffer_load_dwordx4 v9, s[8:11], s68 offen lds\n"
"s_add_u32 m0, s25, 17408\n"
"s_add_u32 s68, s26, 49152\n"
"buffer_load_dwordx4 v9, s[8:11], s68 offen lds\n"
"s_add_u32 m0, s25, 18432\n"
"s_add_u32 s68, s26, 32896\n"
"buffer_load_dwordx4 v9, s[8:11], s68 offen lds\n"
"s_add_u32 m0, s25, 19456\n"
"s_add_u32 s68, s26, 49280\n"
"buffer_load_dwordx4 v9, s[8:11], s68 offen lds\n"
"s_waitcnt vmcnt(25)\n"
"ds_write_b128 v23, v[112:115] offset:49152\n"
"ds_write_b128 v23, v[116:119] offset:57344\n"
"v_add_u32 v21, 0x8000, v23\n"
"ds_write_b128 v21, v[120:123] offset:32768\n"
"ds_write_b128 v23, v[124:127] offset:50176\n"
"ds_write_b128 v23, v[128:131] offset:58368\n"
"v_add_u32 v21, 0x8000, v23\n"
"ds_write_b128 v21, v[132:135] offset:33792\n"
"s_waitcnt lgkmcnt(0)\n"
"s_barrier\n"
"s_add_u32 m0, s25, 0\n"
"s_add_u32 s68, s26, 0\n"
"buffer_load_dwordx4 v9, s[8:11], s68 offen lds\n"
"s_add_u32 m0, s25, 1024\n"
"s_add_u32 s68, s26, 16384\n"
"buffer_load_dwordx4 v9, s[8:11], s68 offen lds\n"
"s_add_u32 m0, s25, 2048\n"
"s_add_u32 s68, s26, 128\n"
"buffer_load_dwordx4 v9, s[8:11], s68 offen lds\n"
"s_add_u32 m0, s25, 3072\n"
"s_add_u32 s68, s26, 16512\n"
"buffer_load_dwordx4 v9, s[8:11], s68 offen lds\n"
"s_waitcnt vmcnt(23)\n"
"ds_write_b128 v23, v[136:139] offset:0\n"
"ds_write_b128 v23, v[140:143] offset:8192\n"
"ds_write_b128 v23, v[144:147] offset:16384\n"
"ds_write_b128 v23, v[148:151] offset:1024\n"
"ds_write_b128 v23, v[152:155] offset:9216\n"
"ds_write_b128 v23, v[156:159] offset:17408\n"
"s_waitcnt lgkmcnt(0)\n"
"s_barrier\n"
"s_waitcnt vmcnt(17)\n"
"ds_write_b128 v23, v[160:163] offset:24576\n"
"ds_write_b128 v23, v[164:167] offset:32768\n"
"ds_write_b128 v23, v[168:171] offset:40960\n"
"ds_write_b128 v23, v[172:175] offset:25600\n"
"ds_write_b128 v23, v[176:179] offset:33792\n"
"ds_write_b128 v23, v[180:183] offset:41984\n"
"s_waitcnt vmcnt(13)\n"
"s_waitcnt lgkmcnt(0)\n"
"s_barrier\n"
"s_lshl_b32 s68, s31, 9\n"
"s_add_u32 s68, s68, 4096\n"
"v_and_b32 v50, 0x1f0, v0\n"
"buffer_load_dwordx2 v[50:51], v50, s[12:15], s68 offen sc1\n"
"s_setprio 2\n"
"v_lshrrev_b32 v21, 4, v0\n"
"v_mov_b32 v40, -1\n"
"v_mov_b32 v41, -1\n"
"v_mov_b32 v42, -1\n"
"v_mov_b32 v43, -1\n"
"v_and_b32 v44, 31, v21\n"
"v_lshlrev_b32 v44, 4, v44\n"
"v_lshrrev_b32 v23, 5, v21\n"
"v_lshl_add_u32 v44, v23, 14, v44\n"
"s_add_u32 s68, s50, 0xac0000\n"
"buffer_store_dwordx4 v[40:43], v44, s[12:15], s68 offen sc1\n"
"s_add_u32 s68, s50, 0xac8000\n"
"buffer_store_dwordx4 v[40:43], v44, s[12:15], s68 offen sc1\n"
"s_add_u32 s68, s50, 0xcc0000\n"
"buffer_store_dwordx4 v[40:43], v44, s[12:15], s68 offen sc1\n"
"s_add_u32 s68, s50, 0xcc8000\n"
"buffer_store_dwordx4 v[40:43], v44, s[12:15], s68 offen sc1\n"
"s_waitcnt vmcnt(14)\n"
"s_barrier\n"
"v_bfe_u32 v56, v0, 5, 2\n"
"v_bfe_u32 v57, v0, 8, 1\n"
"v_xor_b32 v56, v56, v57\n"
"v_and_b32 v57, 16, v0\n"
"v_lshl_add_u32 v56, v56, 5, v57\n"
"v_lshrrev_b32 v57, 7, v0\n"
"v_lshl_add_u32 v56, v57, 10, v56\n"
"v_xor_b32 v57, 64, v56\n"
"s_add_u32 m0, s57, 0\n"
"s_nop 0\n"
"buffer_load_dwordx4 v56, s[4:7], s56 offen lds\n"
"s_add_u32 m0, s57, 1024\n"
"s_add_u32 s71, s56, 0x2000\n"
"buffer_load_dwordx4 v57, s[4:7], s71 offen lds\n"
"s_waitcnt vmcnt(6)\n"
"v_xor_b32 v50, s64, v50\n"
"v_xor_b32 v51, s65, v51\n"
"v_or_b32 v50, v50, v51\n"
"v_cmp_ne_u32 vcc, 0, v50\n"
"s_nop 1\n"
"s_cbranch_vccnz Lr_86\n"
"Lk_87:\n"
"s_add_u32 s71, s59, 0x0\n"
"buffer_load_dwordx4 v[64:67], v0, s[12:15], s71 offen offset:0 sc1\n"
"buffer_load_dwordx4 v[68:71], v0, s[12:15], s71 offen offset:1024 sc1\n"
"buffer_load_dwordx4 v[72:75], v0, s[12:15], s71 offen offset:2048 sc1\n"
"buffer_load_dwordx4 v[76:79], v0, s[12:15], s71 offen offset:3072 sc1\n"
"s_add_u32 s71, s59, 0x1000\n"
"buffer_load_dwordx4 v[80:83], v0, s[12:15], s71 offen offset:0 sc1\n"
"buffer_load_dwordx4 v[84:87], v0, s[12:15], s71 offen offset:1024 sc1\n"
"buffer_load_dwordx4 v[88:91], v0, s[12:15], s71 offen offset:2048 sc1\n"
"buffer_load_dwordx4 v[92:95], v0, s[12:15], s71 offen offset:3072 sc1\n"
"v_add_u32 v63, 151552, v0\n"
"v_mov_b32 v40, 0x3f803f80\n"
"v_mov_b32 v41, 0x3f803f80\n"
"ds_read_b128 v[34:37], v63\n"
"ds_read_b64 v[38:39], v63 offset:1024\n"
"s_barrier\n"
"s_lshl_b32 s68, s31, 11\n"
"s_lshr_b32 s69, s28, 1\n"
"s_lshl_b32 s69, s69, 6\n"
"s_add_u32 s68, s68, s69\n"
"s_and_b32 s69, s28, 1\n"
"s_lshl_b32 s69, s69, 1\n"
"v_lshrrev_b32 v52, 9, v0\n"
"v_bfe_u32 v53, v0, 5, 2\n"
"v_xor_b32 v52, v52, v53\n"
"v_xor_b32 v52, s69, v52\n"
"v_bfe_u32 v53, v0, 4, 4\n"
"v_lshlrev_b32 v53, 7, v53\n"
"v_lshl_add_u32 v52, v52, 4, v53\n"
"v_bfe_u32 v53, v0, 8, 1\n"
"v_lshl_add_u32 v52, v53, 3, v52\n"
"v_add_u32 v63, s68, v52\n"
"ds_read_b64 v[4:5], v63 offset:0\n"
"ds_read_b64 v[6:7], v63 offset:16384\n"
"ds_read_b64 v[8:9], v63 offset:32768\n"
"ds_read_b64 v[10:11], v63 offset:49152\n"
"s_add_u32 m0, s57, 2048\n"
"s_add_u32 s71, s56, 0x4000\n"
"buffer_load_dwordx4 v56, s[4:7], s71 offen lds\n"
"s_add_u32 m0, s57, 3072\n"
"s_add_u32 s71, s56, 0x6000\n"
"buffer_load_dwordx4 v57, s[4:7], s71 offen lds\n"
"s_add_u32 s71, s59, 0x2000\n"
"buffer_load_dwordx4 v[96:99], v0, s[12:15], s71 offen offset:0 sc1\n"
"buffer_load_dwordx4 v[100:103], v0, s[12:15], s71 offen offset:1024 sc1\n"
"buffer_load_dwordx4 v[104:107], v0, s[12:15], s71 offen offset:2048 sc1\n"
"buffer_load_dwordx4 v[108:111], v0, s[12:15], s71 offen offset:3072 sc1\n"
"s_add_u32 s71, s59, 0x3000\n"
"buffer_load_dwordx4 v[112:115], v0, s[12:15], s71 offen offset:0 sc1\n"
"buffer_load_dwordx4 v[116:119], v0, s[12:15], s71 offen offset:1024 sc1\n"
"buffer_load_dwordx4 v[120:123], v0, s[12:15], s71 offen offset:2048 sc1\n"
"buffer_load_dwordx4 v[124:127], v0, s[12:15], s71 offen offset:3072 sc1\n"
"s_add_u32 s71, s59, 0x4000\n"
"buffer_load_dwordx4 v[128:131], v0, s[12:15], s71 offen offset:0 sc1\n"
"buffer_load_dwordx4 v[132:135], v0, s[12:15], s71 offen offset:1024 sc1\n"
"s_waitcnt lgkmcnt(0)\n"
"ds_write_b128 v62, v[4:7] offset:12288\n"
"ds_write_b128 v62, v[8:11] offset:16384\n"
"s_waitcnt vmcnt(22)\n"
"s_waitcnt lgkmcnt(0)\n"
"s_barrier\n"
"s_memrealtime s[70:71]\n"
"ds_read_b128 v[20:23], v62 offset:0\n"
"ds_read_b64 v[28:29], v62 offset:8192\n"
"ds_read_b32 v32, v3 offset:780\n"
"ds_read_b96 v[46:48], v62\n"
"ds_read_b32 v50, v62 offset:12\n"
"v_mov_b32 v49, s64\n"
"v_mov_b32 v51, s64\n"
"v_lshrrev_b32 v63, 1, v0\n"
"s_waitcnt lgkmcnt(0)\n"
"buffer_store_dwordx4 v[46:49], v0, s[12:15], s54 offen sc1\n"
"buffer_store_dwordx2 v[50:51], v63, s[12:15], s54 offen offset:1024 sc1\n"
"s_lshl_b32 s68, s30, 10\n"
"s_add_u32 s68, s68, 0xec0000\n"
"s_lshl_b32 s69, s31, 7\n"
"s_add_u32 s68, s68, s69\n"
"s_cmp_eq_u32 s28, 0\n"
"s_cselect_b32 s69, 0, 0x80000000\n"
"v_mov_b32 v52, s64\n"
"v_mov_b32 v53, s65\n"
"v_mov_b32 v54, s70\n"
"v_mov_b32 v55, s69\n"
"s_mov_b64 exec, 1\n"
"buffer_store_dwordx3 v[52:54], v55, s[12:15], s68 offen sc1\n"
"s_mov_b64 exec, -1\n"
"v_mfma_f32_16x16x16_bf16 v[42:45], v[20:21], v[28:29], v[34:37]\n"
"s_lshl_b32 s68, s30, 7\n"
"s_lshl_b32 s69, s31, 4\n"
"s_add_u32 s68, s68, s69\n"
"s_add_u32 s68, s68, 8192\n"
"s_cmp_eq_u32 s28, 0\n"
"s_cselect_b32 s69, 0, 0x80000000\n"
"v_mov_b32 v16, s64\n"
"v_mov_b32 v17, s65\n"
"v_mov_b32 v18, s69\n"
"s_mov_b64 exec, 1\n"
"buffer_store_dwordx2 v[16:17], v18, s[12:15], s68 offen sc1\n"
"s_mov_b64 exec, -1\n"
"s_add_u32 m0, s57, 4096\n"
"s_add_u32 s71, s56, 0x8000\n"
"buffer_load_dwordx4 v56, s[4:7], s71 offen lds\n"
"s_add_u32 m0, s57, 5120\n"
"s_add_u32 s71, s56, 0xa000\n"
"buffer_load_dwordx4 v57, s[4:7], s71 offen lds\n"
"s_add_u32 m0, s57, 6144\n"
"s_add_u32 s71, s56, 0xc000\n"
"buffer_load_dwordx4 v56, s[4:7], s71 offen lds\n"
"s_add_u32 m0, s57, 7168\n"
"s_add_u32 s71, s56, 0xe000\n"
"buffer_load_dwordx4 v57, s[4:7], s71 offen lds\n"
"s_add_u32 m0, s57, 8192\n"
"s_add_u32 s71, s56, 0x10000\n"
"buffer_load_dwordx4 v56, s[4:7], s71 offen lds\n"
"s_add_u32 m0, s57, 9216\n"
"s_add_u32 s71, s56, 0x12000\n"
"buffer_load_dwordx4 v57, s[4:7], s71 offen lds\n"
"s_add_u32 s71, s59, 0x4000\n"
"buffer_load_dwordx4 v[136:139], v0, s[12:15], s71 offen offset:2048 sc1\n"
"buffer_load_dwordx4 v[140:143], v0, s[12:15], s71 offen offset:3072 sc1\n"
"s_add_u32 s71, s59, 0x5000\n"
"buffer_load_dwordx4 v[144:147], v0, s[12:15], s71 offen offset:0 sc1\n"
"buffer_load_dwordx4 v[148:151], v0, s[12:15], s71 offen offset:1024 sc1\n"
"buffer_load_dwordx4 v[152:155], v0, s[12:15], s71 offen offset:2048 sc1\n"
"buffer_load_dwordx4 v[156:159], v0, s[12:15], s71 offen offset:3072 sc1\n"
"s_add_u32 s71, s59, 0x6000\n"
"buffer_load_dwordx4 v[160:163], v0, s[12:15], s71 offen offset:0 sc1\n"
"buffer_load_dwordx4 v[164:167], v0, s[12:15], s71 offen offset:1024 sc1\n"
"s_barrier\n"
"ds_read_b128 v[24:27], v62 offset:4096\n"
"ds_read_b64 v[30:31], v62 offset:8200\n"
"ds_read_b32 v33, v3 offset:524\n"
"s_waitcnt lgkmcnt(2)\n"
"v_mfma_f32_16x16x16_bf16 v[4:7], v[26:27], v[28:29], v[34:37]\n"
"s_nop 7\n"
"v_cndmask_b32_e64 v42, v42, v4, s[36:37]\n"
"v_cndmask_b32_e64 v43, v43, v5, s[38:39]\n"
"v_cndmask_b32_e64 v44, v44, v6, s[40:41]\n"
"v_cndmask_b32_e64 v45, v45, v7, s[42:43]\n"
"v_max3_f32 v8, v42, v43, v44\n"
"v_max_f32 v8, v8, v45\n"
"v_mov_b32 v9, v8\n"
"s_nop 1\n"
"v_permlane32_swap_b32 v8, v9\n"
"v_max_f32 v8, v8, v9\n"
"v_mov_b32 v9, v8\n"
"s_nop 1\n"
"v_permlane16_swap_b32 v8, v9\n"
"v_max_f32 v8, v8, v9\n"
"v_mul_f32 v8, v8, v32\n"
"v_fma_f32 v42, v42, v32, -v8\n"
"v_fma_f32 v43, v43, v32, -v8\n"
"v_fma_f32 v44, v44, v32, -v8\n"
"v_fma_f32 v45, v45, v32, -v8\n"
"v_exp_f32 v42, v42\n"
"v_exp_f32 v43, v43\n"
"v_exp_f32 v44, v44\n"
"v_exp_f32 v45, v45\n"
"v_cvt_pk_bf16_f32 v10, v42, v43\n"
"v_cvt_pk_bf16_f32 v11, v44, v45\n"
"v_and_b32 v14, v10, v38\n"
"v_and_b32 v15, v11, v39\n"
"v_mfma_f32_16x16x16_bf16 v[4:7], v[40:41], v[10:11], 0\n"
"v_xor_b32 v12, v10, v14\n"
"v_xor_b32 v13, v11, v15\n"
"s_nop 1\n"
"v_mfma_f32_16x16x32_bf16 v[42:45], v[22:25], v[12:15], 0\n"
"s_lshl_b32 s68, s30, 10\n"
"s_add_u32 s68, s68, 0xec0000\n"
"v_bfe_u32 v52, v0, 4, 3\n"
"v_lshlrev_b32 v52, 7, v52\n"
"buffer_load_dwordx3 v[52:54], v52, s[12:15], s68 offen sc1\n"
"s_add_u32 s69, s50, 0xacc000\n"
"v_rcp_f32 v8, v4\n"
"s_nop 0\n"
"v_mul_f32 v42, v42, v8\n"
"v_mul_f32 v43, v43, v8\n"
"v_mul_f32 v44, v44, v8\n"
"v_mul_f32 v45, v45, v8\n"
"v_cvt_pk_bf16_f32 v10, v42, v43\n"
"v_cvt_pk_bf16_f32 v11, v44, v45\n"
"v_and_b32 v10, -2, v10\n"
"buffer_store_dwordx2 v[10:11], v1, s[12:15], s69 offen sc1\n"
"s_waitcnt lgkmcnt(0)\n"
"v_mfma_f32_16x16x16_bf16 v[42:45], v[26:27], v[30:31], v[34:37]\n"
"s_add_u32 m0, s57, 10240\n"
"s_add_u32 s71, s56, 0x14000\n"
"buffer_load_dwordx4 v56, s[4:7], s71 offen lds\n"
"s_add_u32 m0, s57, 11264\n"
"s_add_u32 s71, s56, 0x16000\n"
"buffer_load_dwordx4 v57, s[4:7], s71 offen lds\n"
"s_add_u32 m0, s57, 12288\n"
"s_add_u32 s71, s56, 0x18000\n"
"buffer_load_dwordx4 v56, s[4:7], s71 offen lds\n"
"s_add_u32 m0, s57, 13312\n"
"s_add_u32 s71, s56, 0x1a000\n"
"buffer_load_dwordx4 v57, s[4:7], s71 offen lds\n"
"s_add_u32 m0, s57, 14336\n"
"s_add_u32 s71, s56, 0x1c000\n"
"buffer_load_dwordx4 v56, s[4:7], s71 offen lds\n"
"s_add_u32 m0, s57, 15360\n"
"s_add_u32 s71, s56, 0x1e000\n"
"buffer_load_dwordx4 v57, s[4:7], s71 offen lds\n"
"s_add_u32 s71, s59, 0x6000\n"
"buffer_load_dwordx4 v[168:171], v0, s[12:15], s71 offen offset:2048 sc1\n"
"buffer_load_dwordx4 v[172:175], v0, s[12:15], s71 offen offset:3072 sc1\n"
"s_add_u32 s71, s59, 0x7000\n"
"buffer_load_dwordx4 v[176:179], v0, s[12:15], s71 offen offset:0 sc1\n"
"buffer_load_dwordx4 v[180:183], v0, s[12:15], s71 offen offset:1024 sc1\n"
"buffer_load_dwordx4 v[184:187], v0, s[12:15], s71 offen offset:2048 sc1\n"
"buffer_load_dwordx4 v[188:191], v0, s[12:15], s71 offen offset:3072 sc1\n"
"s_add_u32 s71, s59, 0x8000\n"
"buffer_load_dwordx4 v[192:195], v0, s[12:15], s71 offen offset:0 sc1\n"
"buffer_load_dwordx4 v[196:199], v0, s[12:15], s71 offen offset:1024 sc1\n"
"buffer_load_dwordx4 v[200:203], v0, s[12:15], s71 offen offset:2048 sc1\n"
"buffer_load_dwordx4 v[204:207], v0, s[12:15], s71 offen offset:3072 sc1\n"
"s_waitcnt vmcnt(17)\n"
"v_xor_b32 v52, s64, v52\n"
"v_xor_b32 v53, s65, v53\n"
"v_or_b32 v52, v52, v53\n"
"v_cmp_ne_u32 vcc, 0, v52\n"
"s_nop 1\n"
"s_cbranch_vccnz Lr_88\n"
"Lk_89:\n"
"s_memrealtime s[70:71]\n"
"s_waitcnt lgkmcnt(0)\n"
"v_subrev_u32 v53, s70, v54\n"
"s_nop 1\n"
"v_max_i32_dpp v53, v53, v53 quad_perm:[1,0,3,2] row_mask:0xf bank_mask:0xf\n"
"s_nop 1\n"
"v_max_i32_dpp v53, v53, v53 quad_perm:[2,3,0,1] row_mask:0xf bank_mask:0xf\n"
"s_nop 1\n"
"v_max_i32_dpp v53, v53, v53 row_half_mirror row_mask:0xf bank_mask:0xf\n"
"s_nop 0\n"
"v_readfirstlane_b32 s68, v53\n"
"s_nop 1\n"
"s_add_i32 s68, s68, 130\n"
"s_min_i32 s68, s68, 130\n"
"s_cmp_le_i32 s68, 2\n"
"s_cbranch_scc1 La_91\n"
"s_add_u32 s62, s68, s70\n"
"s_mov_b32 s69, 0\n"
"La_90:\n"
"s_memrealtime s[70:71]\n"
"s_add_u32 s69, s69, 1\n"
"s_waitcnt lgkmcnt(0)\n"
"s_cmp_gt_u32 s69, 200\n"
"s_cbranch_scc1 La_91\n"
"s_sub_u32 s68, s70, s62\n"
"s_cmp_lt_i32 s68, 0\n"
"s_cbranch_scc1 La_90\n"
"La_91:\n"
"s_waitcnt vmcnt(10)\n"
"s_barrier\n"
"ds_read_b128 v[20:23], v62 offset:0\n"
"ds_read_b64 v[28:29], v62 offset:8192\n"
"ds_read_b32 v32, v3 offset:268\n"
"s_waitcnt lgkmcnt(2)\n"
"v_mfma_f32_16x16x16_bf16 v[4:7], v[20:21], v[30:31], v[34:37]\n"
"s_nop 7\n"
"v_cndmask_b32_e64 v42, v42, v4, s[36:37]\n"
"v_cndmask_b32_e64 v43, v43, v5, s[38:39]\n"
"v_cndmask_b32_e64 v44, v44, v6, s[40:41]\n"
"v_cndmask_b32_e64 v45, v45, v7, s[42:43]\n"
"v_max3_f32 v8, v42, v43, v44\n"
"v_max_f32 v8, v8, v45\n"
"v_mov_b32 v9, v8\n"
"s_nop 1\n"
"v_permlane32_swap_b32 v8, v9\n"
"v_max_f32 v8, v8, v9\n"
"v_mov_b32 v9, v8\n"
"s_nop 1\n"
"v_permlane16_swap_b32 v8, v9\n"
"v_max_f32 v8, v8, v9\n"
"v_mul_f32 v8, v8, v33\n"
"v_fma_f32 v42, v42, v33, -v8\n"
"v_fma_f32 v43, v43, v33, -v8\n"
"v_fma_f32 v44, v44, v33, -v8\n"
"v_fma_f32 v45, v45, v33, -v8\n"
"v_exp_f32 v42, v42\n"
"v_exp_f32 v43, v43\n"
"v_exp_f32 v44, v44\n"
"v_exp_f32 v45, v45\n"
"v_cvt_pk_bf16_f32 v10, v42, v43\n"
"v_cvt_pk_bf16_f32 v11, v44, v45\n"
"v_and_b32 v12, v10, v38\n"
"v_and_b32 v13, v11, v39\n"
"v_mfma_f32_16x16x16_bf16 v[4:7], v[40:41], v[10:11], 0\n"
"v_xor_b32 v14, v10, v12\n"
"v_xor_b32 v15, v11, v13\n"
"s_nop 1\n"
"v_mfma_f32_16x16x32_bf16 v[42:45], v[22:25], v[12:15], 0\n"
"s_add_u32 s69, s50, 0xac8000\n"
"s_nop 1\n"
"v_rcp_f32 v8, v4\n"
"s_nop 3\n"
"v_mul_f32 v42, v42, v8\n"
"v_mul_f32 v43, v43, v8\n"
"v_mul_f32 v44, v44, v8\n"
"v_mul_f32 v45, v45, v8\n"
"v_cvt_pk_bf16_f32 v10, v42, v43\n"
"v_cvt_pk_bf16_f32 v11, v44, v45\n"
"v_and_b32 v10, -2, v10\n"
"buffer_store_dwordx2 v[10:11], v1, s[12:15], s69 offen sc1\n"
"s_waitcnt lgkmcnt(0)\n"
"v_mfma_f32_16x16x16_bf16 v[42:45], v[20:21], v[28:29], v[34:37]\n"
"v_or_b32 v46, s60, v0\n"
"v_lshrrev_b32 v50, 1, v0\n"
"v_or_b32 v50, s60, v50\n"
"s_add_u32 s69, s54, 0xffff0000\n"
"buffer_load_dwordx4 v[46:49], v46, s[12:15], s69 offen sc1\n"
"buffer_load_dwordx2 v[50:51], v50, s[12:15], s69 offen offset:1024 sc1\n"
"s_add_u32 s71, s59, 0x9000\n"
"buffer_load_dwordx4 v[208:211], v0, s[12:15], s71 offen offset:0 sc1\n"
"buffer_load_dwordx4 v[212:215], v0, s[12:15], s71 offen offset:1024 sc1\n"
"buffer_load_dwordx4 v[216:219], v0, s[12:15], s71 offen offset:2048 sc1\n"
"buffer_load_dwordx4 v[220:223], v0, s[12:15], s71 offen offset:3072 sc1\n"
"s_add_u32 s71, s59, 0xa000\n"
"buffer_load_dwordx4 v[224:227], v0, s[12:15], s71 offen offset:0 sc1\n"
"buffer_load_dwordx4 v[228:231], v0, s[12:15], s71 offen offset:1024 sc1\n"
"buffer_load_dwordx4 v[232:235], v0, s[12:15], s71 offen offset:2048 sc1\n"
"buffer_load_dwordx4 v[236:239], v0, s[12:15], s71 offen offset:3072 sc1\n"
"s_add_u32 s71, s59, 0xb000\n"
"buffer_load_dwordx4 v[240:243], v0, s[12:15], s71 offen offset:0 sc1\n"
"buffer_load_dwordx4 v[244:247], v0, s[12:15], s71 offen offset:1024 sc1\n"
"buffer_load_dwordx4 v[248:251], v0, s[12:15], s71 offen offset:2048 sc1\n"
"buffer_load_dwordx4 v[252:255], v0, s[12:15], s71 offen offset:3072 sc1\n"
"s_barrier\n"
"ds_read_b128 v[24:27], v62 offset:4096\n"
"ds_read_b64 v[30:31], v62 offset:8200\n"
"ds_read_b32 v33, v3 offset:12\n"
"s_waitcnt lgkmcnt(2)\n"
"v_mfma_f32_16x16x16_bf16 v[4:7], v[26:27], v[28:29], v[34:37]\n"
"s_nop 7\n"
"v_cndmask_b32_e64 v42, v42, v4, s[36:37]\n"
"v_cndmask_b32_e64 v43, v43, v5, s[38:39]\n"
"v_cndmask_b32_e64 v44, v44, v6, s[40:41]\n"
"v_cndmask_b32_e64 v45, v45, v7, s[42:43]\n"
"v_max3_f32 v8, v42, v43, v44\n"
"v_max_f32 v8, v8, v45\n"
"v_mov_b32 v9, v8\n"
"s_nop 1\n"
"v_permlane32_swap_b32 v8, v9\n"
"v_max_f32 v8, v8, v9\n"
"v_mov_b32 v9, v8\n"
"s_nop 1\n"
"v_permlane16_swap_b32 v8, v9\n"
"v_max_f32 v8, v8, v9\n"
"v_mul_f32 v8, v8, v32\n"
"v_fma_f32 v42, v42, v32, -v8\n"
"v_fma_f32 v43, v43, v32, -v8\n"
"v_fma_f32 v44, v44, v32, -v8\n"
"v_fma_f32 v45, v45, v32, -v8\n"
"v_exp_f32 v42, v42\n"
"v_exp_f32 v43, v43\n"
"v_exp_f32 v44, v44\n"
"v_exp_f32 v45, v45\n"
"v_cvt_pk_bf16_f32 v10, v42, v43\n"
"v_cvt_pk_bf16_f32 v11, v44, v45\n"
"v_and_b32 v14, v10, v38\n"
"v_and_b32 v15, v11, v39\n"
"v_mfma_f32_16x16x16_bf16 v[4:7], v[40:41], v[10:11], 0\n"
"v_xor_b32 v12, v10, v14\n"
"v_xor_b32 v13, v11, v15\n"
"s_nop 1\n"
"v_mfma_f32_16x16x32_bf16 v[42:45], v[22:25], v[12:15], 0\n"
"s_add_u32 s69, s50, 0xac4000\n"
"s_nop 1\n"
"v_rcp_f32 v8, v4\n"
"s_nop 3\n"
"v_mul_f32 v42, v42, v8\n"
"v_mul_f32 v43, v43, v8\n"
"v_mul_f32 v44, v44, v8\n"
"v_mul_f32 v45, v45, v8\n"
"v_cvt_pk_bf16_f32 v10, v42, v43\n"
"v_cvt_pk_bf16_f32 v11, v44, v45\n"
"v_and_b32 v10, -2, v10\n"
"buffer_store_dwordx2 v[10:11], v1, s[12:15], s69 offen sc1\n"
"s_waitcnt lgkmcnt(0)\n"
"v_mfma_f32_16x16x16_bf16 v[42:45], v[26:27], v[30:31], v[34:37]\n"
"s_barrier\n"
"s_waitcnt vmcnt(13)\n"
"v_xor_b32 v4, s64, v49\n"
"v_xor_b32 v5, s64, v51\n"
"v_or_b32 v4, v4, v5\n"
"v_cmp_ne_u32 vcc, 0, v4\n"
"s_cmp_lg_u32 s60, 0\n"
"s_nop 0\n"
"s_cselect_b64 vcc, 0, vcc\n"
"s_cbranch_vccnz Lr_92\n"
"Lk_93:\n"
"v_min_f32 v4, s61, v34\n"
"v_min_f32 v5, s61, v35\n"
"v_min_f32 v6, s61, v36\n"
"v_min_f32 v7, s61, v37\n"
"v_mov_b32 v22, v48\n"
"v_mov_b32 v23, v50\n"
"v_mfma_f32_16x16x16_bf16 v[4:7], v[46:47], v[30:31], v[4:7]\n"
"s_nop 7\n"
"v_cndmask_b32_e64 v42, v42, v4, s[36:37]\n"
"v_cndmask_b32_e64 v43, v43, v5, s[38:39]\n"
"v_cndmask_b32_e64 v44, v44, v6, s[40:41]\n"
"v_cndmask_b32_e64 v45, v45, v7, s[42:43]\n"
"v_max3_f32 v8, v42, v43, v44\n"
"v_max_f32 v8, v8, v45\n"
"v_mov_b32 v9, v8\n"
"s_nop 1\n"
"v_permlane32_swap_b32 v8, v9\n"
"v_max_f32 v8, v8, v9\n"
"v_mov_b32 v9, v8\n"
"s_nop 1\n"
"v_permlane16_swap_b32 v8, v9\n"
"v_max_f32 v8, v8, v9\n"
"v_mul_f32 v8, v8, v33\n"
"v_fma_f32 v42, v42, v33, -v8\n"
"v_fma_f32 v43, v43, v33, -v8\n"
"v_fma_f32 v44, v44, v33, -v8\n"
"v_fma_f32 v45, v45, v33, -v8\n"
"v_exp_f32 v42, v42\n"
"v_exp_f32 v43, v43\n"
"v_exp_f32 v44, v44\n"
"v_exp_f32 v45, v45\n"
"v_cvt_pk_bf16_f32 v10, v42, v43\n"
"v_cvt_pk_bf16_f32 v11, v44, v45\n"
"v_and_b32 v12, v10, v38\n"
"v_and_b32 v13, v11, v39\n"
"v_mfma_f32_16x16x16_bf16 v[4:7], v[40:41], v[10:11], 0\n"
"v_xor_b32 v14, v10, v12\n"
"v_xor_b32 v15, v11, v13\n"
"s_nop 1\n"
"v_mfma_f32_16x16x32_bf16 v[42:45], v[22:25], v[12:15], 0\n"
"s_add_u32 s69, s50, 0xac0000\n"
"s_nop 1\n"
"v_rcp_f32 v8, v4\n"
"s_nop 3\n"
"v_mul_f32 v42, v42, v8\n"
"v_mul_f32 v43, v43, v8\n"
"v_mul_f32 v44, v44, v8\n"
"v_mul_f32 v45, v45, v8\n"
"v_cvt_pk_bf16_f32 v10, v42, v43\n"
"v_cvt_pk_bf16_f32 v11, v44, v45\n"
"v_and_b32 v10, -2, v10\n"
"buffer_store_dwordx2 v[10:11], v1, s[12:15], s69 offen sc1\n"
"v_mov_b32 v56, 0x3f803f80\n"
"v_mov_b32 v57, 0x3f803f80\n"
"v_mov_b32 v58, 0x3f803f80\n"
"v_mov_b32 v59, 0x3f803f80\n"
"v_lshrrev_b32 v61, 8, v0\n"
"v_lshlrev_b32 v61, 6, v61\n"
"v_add_u32 v61, 154624, v61\n"
"v_mfma_f32_16x16x32_bf16 v[4:7], v[56:59], v[64:67], 0\n"
"v_mfma_f32_16x16x32_bf16 v[8:11], v[56:59], v[68:71], 0\n"
"v_mfma_f32_16x16x32_bf16 v[12:15], v[56:59], v[72:75], 0\n"
"v_mfma_f32_16x16x32_bf16 v[4:7], v[56:59], v[76:79], v[4:7]\n"
"v_mfma_f32_16x16x32_bf16 v[8:11], v[56:59], v[80:83], v[8:11]\n"
"v_mfma_f32_16x16x32_bf16 v[12:15], v[56:59], v[84:87], v[12:15]\n"
"v_mfma_f32_16x16x32_bf16 v[4:7], v[56:59], v[88:91], v[4:7]\n"
"v_mfma_f32_16x16x32_bf16 v[8:11], v[56:59], v[92:95], v[8:11]\n"
"v_mfma_f32_16x16x32_bf16 v[12:15], v[56:59], v[96:99], v[12:15]\n"
"v_mfma_f32_16x16x32_bf16 v[4:7], v[56:59], v[100:103], v[4:7]\n"
"v_mfma_f32_16x16x32_bf16 v[8:11], v[56:59], v[104:107], v[8:11]\n"
"v_mfma_f32_16x16x32_bf16 v[12:15], v[56:59], v[108:111], v[12:15]\n"
"v_mfma_f32_16x16x32_bf16 v[4:7], v[56:59], v[112:115], v[4:7]\n"
"v_mfma_f32_16x16x32_bf16 v[8:11], v[56:59], v[116:119], v[8:11]\n"
"v_mfma_f32_16x16x32_bf16 v[12:15], v[56:59], v[120:123], v[12:15]\n"
"v_mfma_f32_16x16x32_bf16 v[4:7], v[56:59], v[124:127], v[4:7]\n"
"v_mfma_f32_16x16x32_bf16 v[8:11], v[56:59], v[128:131], v[8:11]\n"
"v_mfma_f32_16x16x32_bf16 v[12:15], v[56:59], v[132:135], v[12:15]\n"
"v_mfma_f32_16x16x32_bf16 v[4:7], v[56:59], v[136:139], v[4:7]\n"
"v_mfma_f32_16x16x32_bf16 v[8:11], v[56:59], v[140:143], v[8:11]\n"
"v_mfma_f32_16x16x32_bf16 v[12:15], v[56:59], v[144:147], v[12:15]\n"
"v_mfma_f32_16x16x32_bf16 v[4:7], v[56:59], v[148:151], v[4:7]\n"
"v_mfma_f32_16x16x32_bf16 v[8:11], v[56:59], v[152:155], v[8:11]\n"
"v_mfma_f32_16x16x32_bf16 v[12:15], v[56:59], v[156:159], v[12:15]\n"
"v_mfma_f32_16x16x32_bf16 v[4:7], v[56:59], v[160:163], v[4:7]\n"
"v_mfma_f32_16x16x32_bf16 v[8:11], v[56:59], v[164:167], v[8:11]\n"
"v_mfma_f32_16x16x32_bf16 v[12:15], v[56:59], v[168:171], v[12:15]\n"
"v_mfma_f32_16x16x32_bf16 v[4:7], v[56:59], v[172:175], v[4:7]\n"
"v_mfma_f32_16x16x32_bf16 v[8:11], v[56:59], v[176:179], v[8:11]\n"
"v_mfma_f32_16x16x32_bf16 v[12:15], v[56:59], v[180:183], v[12:15]\n"
"v_mfma_f32_16x16x32_bf16 v[4:7], v[56:59], v[184:187], v[4:7]\n"
"v_mfma_f32_16x16x32_bf16 v[8:11], v[56:59], v[188:191], v[8:11]\n"
"v_mfma_f32_16x16x32_bf16 v[12:15], v[56:59], v[192:195], v[12:15]\n"
"v_mfma_f32_16x16x32_bf16 v[4:7], v[56:59], v[196:199], v[4:7]\n"
"v_mfma_f32_16x16x32_bf16 v[8:11], v[56:59], v[200:203], v[8:11]\n"
"v_mfma_f32_16x16x32_bf16 v[12:15], v[56:59], v[204:207], v[12:15]\n"
"s_waitcnt vmcnt(13)\n"
"v_mfma_f32_16x16x32_bf16 v[4:7], v[56:59], v[208:211], v[4:7]\n"
"s_waitcnt vmcnt(12)\n"
"v_mfma_f32_16x16x32_bf16 v[8:11], v[56:59], v[212:215], v[8:11]\n"
"s_waitcnt vmcnt(11)\n"
"v_mfma_f32_16x16x32_bf16 v[12:15], v[56:59], v[216:219], v[12:15]\n"
"s_waitcnt vmcnt(10)\n"
"v_mfma_f32_16x16x32_bf16 v[4:7], v[56:59], v[220:223], v[4:7]\n"
"s_waitcnt vmcnt(9)\n"
"v_mfma_f32_16x16x32_bf16 v[8:11], v[56:59], v[224:227], v[8:11]\n"
"s_waitcnt vmcnt(8)\n"
"v_mfma_f32_16x16x32_bf16 v[12:15], v[56:59], v[228:231], v[12:15]\n"
"s_waitcnt vmcnt(7)\n"
"v_mfma_f32_16x16x32_bf16 v[4:7], v[56:59], v[232:235], v[4:7]\n"
"s_waitcnt vmcnt(6)\n"
"v_mfma_f32_16x16x32_bf16 v[8:11], v[56:59], v[236:239], v[8:11]\n"
"s_waitcnt vmcnt(5)\n"
"v_mfma_f32_16x16x32_bf16 v[12:15], v[56:59], v[240:243], v[12:15]\n"
"s_waitcnt vmcnt(4)\n"
"v_mfma_f32_16x16x32_bf16 v[4:7], v[56:59], v[244:247], v[4:7]\n"
"s_waitcnt vmcnt(3)\n"
"v_mfma_f32_16x16x32_bf16 v[8:11], v[56:59], v[248:251], v[8:11]\n"
"s_waitcnt vmcnt(2)\n"
"v_mfma_f32_16x16x32_bf16 v[12:15], v[56:59], v[252:255], v[12:15]\n"
"s_nop 3\n"
"v_cvt_pk_bf16_f32 v48, v4, v4\n"
"v_and_b32 v49, 0xffff0000, v48\n"
"v_sub_f32 v49, v4, v49\n"
"v_cvt_pk_bf16_f32 v49, v49, v49\n"
"v_cvt_pk_bf16_f32 v50, v8, v8\n"
"v_and_b32 v51, 0xffff0000, v50\n"
"v_sub_f32 v51, v8, v51\n"
"v_cvt_pk_bf16_f32 v51, v51, v51\n"
"v_cvt_pk_bf16_f32 v52, v12, v12\n"
"v_and_b32 v53, 0xffff0000, v52\n"
"v_sub_f32 v53, v12, v53\n"
"v_cvt_pk_bf16_f32 v53, v53, v53\n"
"v_add_u32 v63, s52, v3\n"
"s_mov_b64 exec, 0xffff\n"
"ds_write_b128 v63, v[48:51]\n"
"ds_write_b64 v63, v[52:53] offset:256\n"
"s_mov_b64 exec, -1\n"
"s_add_u32 m0, s58, 49152\n"
"s_add_u32 s69, s51, 0xccc000\n"
"buffer_load_dwordx4 v0, s[12:15], s69 offen offset:0 sc1 lds\n"
"buffer_load_dwordx4 v0, s[12:15], s69 offen offset:1024 sc1 lds\n"
"buffer_load_dwordx4 v0, s[12:15], s69 offen offset:2048 sc1 lds\n"
"buffer_load_dwordx4 v0, s[12:15], s69 offen offset:3072 sc1 lds\n"
"s_barrier\n"
"s_setprio 1\n"
"s_waitcnt vmcnt(0)\n"
"ds_read_b128 v[32:35], v60 offset:49152\n"
"ds_read_b128 v[36:39], v60 offset:50176\n"
"ds_read_b128 v[40:43], v60 offset:51200\n"
"ds_read_b128 v[44:47], v60 offset:52224\n"
"s_waitcnt lgkmcnt(2)\n"
"v_mfma_f32_16x16x32_bf16 v[48:51], v[56:59], v[32:35], 0\n"
"v_bitop3_b32 v63, 1, v32, v34 bitop3:0x10\n"
"v_bitop3_b32 v63, v63, v36, v38 bitop3:0x10\n"
"v_mfma_f32_16x16x32_bf16 v[52:55], v[32:35], v[32:35], 0\n"
"v_mfma_f32_16x16x32_bf16 v[48:51], v[56:59], v[36:39], v[48:51]\n"
"v_mfma_f32_16x16x32_bf16 v[52:55], v[36:39], v[36:39], v[52:55]\n"
"s_waitcnt lgkmcnt(1)\n"
"v_mfma_f32_16x16x32_bf16 v[48:51], v[56:59], v[40:43], v[48:51]\n"
"v_mfma_f32_16x16x32_bf16 v[52:55], v[40:43], v[40:43], v[52:55]\n"
"s_waitcnt lgkmcnt(0)\n"
"v_mfma_f32_16x16x32_bf16 v[48:51], v[56:59], v[44:47], v[48:51]\n"
"v_bitop3_b32 v63, v63, v40, v42 bitop3:0x10\n"
"v_bitop3_b32 v63, v63, v44, v46 bitop3:0x10\n"
"v_mfma_f32_16x16x32_bf16 v[52:55], v[44:47], v[44:47], v[52:55]\n"
"v_cmp_eq_u32 vcc, 0, v63\n"
"v_mfma_f32_16x16x32_bf16 v[4:7], v[68:71], v[32:35], 0\n"
"v_mfma_f32_16x16x32_bf16 v[8:11], v[32:35], v[72:75], 0\n"
"v_mov_b32 v16, 3\n"
"v_mfma_f32_16x16x32_bf16 v[12:15], v[64:67], v[32:35], 0\n"
"s_cbranch_vccnz LQ_94\n"
"LP_95:\n"
"s_lshl_b64 exec, 1, s28\n"
"ds_write_b32 v3, v16 offset:0\n"
"s_mov_b64 exec, -1\n"
"v_mfma_f32_16x16x32_bf16 v[4:7], v[80:83], v[36:39], v[4:7]\n"
"ds_read_b32 v17, v3 offset:0\n"
"v_cndmask_b32_e64 v52, v52, v53, s[44:45]\n"
"v_cndmask_b32_e64 v54, v54, v55, s[44:45]\n"
"v_mfma_f32_16x16x32_bf16 v[8:11], v[36:39], v[84:87], v[8:11]\n"
"v_cndmask_b32_e64 v49, v52, v54, s[46:47]\n"
"v_or_b32 v48, 1, v48\n"
"v_xor_b32 v2, 0x1000, v60\n"
"v_mfma_f32_16x16x32_bf16 v[12:15], v[76:79], v[36:39], v[12:15]\n"
"s_waitcnt lgkmcnt(0)\n"
"v_cmp_gt_u32 vcc, 3, v17\n"
"s_nop 1\n"
"s_and_b32 s68, vcc_lo, 15\n"
"s_cbranch_scc1 Lf_96\n"
"Lg_97:\n"
"ds_read_b128 v[32:35], v2 offset:49152\n"
"ds_read_b128 v[36:39], v2 offset:50176\n"
"v_add_u32 v63, s53, v3\n"
"v_mfma_f32_16x16x32_bf16 v[4:7], v[92:95], v[40:43], v[4:7]\n"
"s_mov_b64 exec, s[48:49]\n"
"ds_write_b64 v63, v[48:49] offset:6656\n"
"s_mov_b64 exec, -1\n"
"v_mfma_f32_16x16x32_bf16 v[8:11], v[40:43], v[96:99], v[8:11]\n"
"v_mfma_f32_16x16x32_bf16 v[12:15], v[88:91], v[40:43], v[12:15]\n"
"ds_read_b128 v[40:43], v2 offset:51200\n"
"v_mfma_f32_16x16x32_bf16 v[4:7], v[104:107], v[44:47], v[4:7]\n"
"v_mfma_f32_16x16x32_bf16 v[8:11], v[44:47], v[108:111], v[8:11]\n"
"v_mfma_f32_16x16x32_bf16 v[12:15], v[100:103], v[44:47], v[12:15]\n"
"ds_read_b128 v[44:47], v2 offset:52224\n"
"s_add_u32 m0, s58, 32768\n"
"s_add_u32 s69, s51, 0xcc8000\n"
"buffer_load_dwordx4 v0, s[12:15], s69 offen offset:0 sc1 lds\n"
"buffer_load_dwordx4 v0, s[12:15], s69 offen offset:1024 sc1 lds\n"
"buffer_load_dwordx4 v0, s[12:15], s69 offen offset:2048 sc1 lds\n"
"buffer_load_dwordx4 v0, s[12:15], s69 offen offset:3072 sc1 lds\n"
"s_waitcnt lgkmcnt(4)\n"
"v_mfma_f32_16x16x32_bf16 v[4:7], v[116:119], v[32:35], v[4:7]\n"
"ds_read_b128 v[48:51], v3 offset:6656\n"
"ds_read_b128 v[52:55], v3 offset:6912\n"
"v_mfma_f32_16x16x32_bf16 v[8:11], v[32:35], v[120:123], v[8:11]\n"
"v_xor_b32 v2, 0x2000, v60\n"
"v_mfma_f32_16x16x32_bf16 v[12:15], v[112:115], v[32:35], v[12:15]\n"
"ds_read_b128 v[32:35], v2 offset:49152\n"
"s_waitcnt lgkmcnt(6)\n"
"v_mfma_f32_16x16x32_bf16 v[4:7], v[128:131], v[36:39], v[4:7]\n"
"v_mfma_f32_16x16x32_bf16 v[8:11], v[36:39], v[132:135], v[8:11]\n"
"s_waitcnt lgkmcnt(1)\n"
"v_mfma_f32_16x16x32_bf16 v[12:15], v[124:127], v[36:39], v[12:15]\n"
"ds_read_b128 v[36:39], v2 offset:50176\n"
"v_bitop3_b32 v63, 1, v48, v50 bitop3:0x80\n"
"v_bitop3_b32 v63, v63, v52, v54 bitop3:0x80\n"
"v_mfma_f32_16x16x32_bf16 v[4:7], v[140:143], v[40:43], v[4:7]\n"
"v_cmp_eq_u32 vcc, 0, v63\n"
"s_nop 1\n"
"s_cbranch_vccnz LQ_98\n"
"LP_99:\n"
"v_mfma_f32_16x16x32_bf16 v[8:11], v[40:43], v[144:147], v[8:11]\n"
"v_add_f32 v48, v48, v50\n"
"v_add_f32 v52, v52, v54\n"
"v_mfma_f32_16x16x32_bf16 v[12:15], v[136:139], v[40:43], v[12:15]\n"
"ds_read_b128 v[40:43], v2 offset:51200\n"
"v_mfma_f32_16x16x32_bf16 v[4:7], v[152:155], v[44:47], v[4:7]\n"
"v_add_f32 v49, v49, v51\n"
"v_add_f32 v53, v53, v55\n"
"v_mfma_f32_16x16x32_bf16 v[8:11], v[44:47], v[156:159], v[8:11]\n"
"v_add_f32 v52, v52, v48\n"
"v_add_f32 v53, v53, v49\n"
"v_mfma_f32_16x16x32_bf16 v[12:15], v[148:151], v[44:47], v[12:15]\n"
"ds_read_b128 v[44:47], v2 offset:52224\n"
"s_waitcnt lgkmcnt(3)\n"
"v_mfma_f32_16x16x32_bf16 v[4:7], v[164:167], v[32:35], v[4:7]\n"
"v_mul_f32 v48, s72, v52\n"
"v_mul_f32 v52, 0xba000000, v52\n"
"v_mfma_f32_16x16x32_bf16 v[8:11], v[32:35], v[168:171], v[8:11]\n"
"v_xor_b32 v2, 0x3000, v60\n"
"v_mul_f32 v49, v48, v48\n"
"v_mfma_f32_16x16x32_bf16 v[12:15], v[160:163], v[32:35], v[12:15]\n"
"ds_read_b128 v[32:35], v2 offset:49152\n"
"s_waitcnt lgkmcnt(3)\n"
"v_mfma_f32_16x16x32_bf16 v[4:7], v[176:179], v[36:39], v[4:7]\n"
"v_fma_f32 v54, -v53, s72, -v49\n"
"v_cvt_pk_bf16_f32 v53, v52, v52\n"
"v_mfma_f32_16x16x32_bf16 v[8:11], v[36:39], v[180:183], v[8:11]\n"
"v_max_f32 v54, 0, v54\n"
"v_and_b32 v53, 0xffff0000, v53\n"
"v_mfma_f32_16x16x32_bf16 v[12:15], v[172:175], v[36:39], v[12:15]\n"
"ds_read_b128 v[36:39], v2 offset:50176\n"
"s_waitcnt lgkmcnt(3)\n"
"v_mfma_f32_16x16x32_bf16 v[4:7], v[188:191], v[40:43], v[4:7]\n"
"v_add_f32 v54, 0x3727c5ac, v54\n"
"v_sub_f32 v55, v52, v53\n"
"v_mfma_f32_16x16x32_bf16 v[8:11], v[40:43], v[192:195], v[8:11]\n"
"v_rsq_f32 v1, v54\n"
"v_cvt_pk_bf16_f32 v53, v52, v55\n"
"v_mfma_f32_16x16x32_bf16 v[12:15], v[184:187], v[40:43], v[12:15]\n"
"ds_read_b128 v[40:43], v2 offset:51200\n"
"s_waitcnt lgkmcnt(3)\n"
"v_mfma_f32_16x16x32_bf16 v[4:7], v[200:203], v[44:47], v[4:7]\n"
"v_cvt_pk_bf16_f32 v52, v52, v55\n"
"v_mul_f32 v55, s73, v1\n"
"v_mfma_f32_16x16x32_bf16 v[8:11], v[44:47], v[204:207], v[8:11]\n"
"s_mov_b64 exec, 0xffff\n"
"ds_write2_b32 v3, v1, v55 offset0:194 offset1:195\n"
"s_mov_b64 exec, -1\n"
"v_add_u32 v63, s52, v3\n"
"v_mfma_f32_16x16x32_bf16 v[12:15], v[196:199], v[44:47], v[12:15]\n"
"ds_read_b128 v[44:47], v2 offset:52224\n"
"ds_read_b128 v[48:51], v63\n"
"ds_read_b64 v[54:55], v63 offset:256\n"
"s_waitcnt lgkmcnt(6)\n"
"v_mfma_f32_16x16x32_bf16 v[4:7], v[212:215], v[32:35], v[4:7]\n"
"v_mfma_f32_16x16x32_bf16 v[8:11], v[32:35], v[216:219], v[8:11]\n"
"v_mfma_f32_16x16x32_bf16 v[12:15], v[208:211], v[32:35], v[12:15]\n"
"s_waitcnt lgkmcnt(5)\n"
"v_mfma_f32_16x16x32_bf16 v[4:7], v[224:227], v[36:39], v[4:7]\n"
"ds_read2_b32 v[16:17], v61 offset0:194 offset1:198\n"
"ds_read2_b32 v[18:19], v61 offset0:202 offset1:206\n"
"v_mfma_f32_16x16x32_bf16 v[8:11], v[36:39], v[228:231], v[8:11]\n"
"v_mfma_f32_16x16x32_bf16 v[12:15], v[220:223], v[36:39], v[12:15]\n"
"s_waitcnt lgkmcnt(6)\n"
"v_mfma_f32_16x16x32_bf16 v[4:7], v[236:239], v[40:43], v[4:7]\n"
"v_mfma_f32_16x16x32_bf16 v[8:11], v[40:43], v[240:243], v[8:11]\n"
"v_mfma_f32_16x16x32_bf16 v[12:15], v[232:235], v[40:43], v[12:15]\n"
"ds_read_b32 v20, v3 offset:4\n"
"s_waitcnt lgkmcnt(5)\n"
"s_waitcnt lgkmcnt(3)\n"
"v_mfma_f32_16x16x16_bf16 v[4:7], v[50:51], v[52:53], v[4:7]\n"
"v_mfma_f32_16x16x16_bf16 v[8:11], v[52:53], v[54:55], v[8:11]\n"
"v_mfma_f32_16x16x16_bf16 v[12:15], v[48:49], v[52:53], v[12:15]\n"
"v_mfma_f32_16x16x32_bf16 v[4:7], v[248:251], v[44:47], v[4:7]\n"
"v_mfma_f32_16x16x32_bf16 v[8:11], v[44:47], v[252:255], v[8:11]\n"
"v_mfma_f32_16x16x32_bf16 v[12:15], v[244:247], v[44:47], v[12:15]\n"
"s_waitcnt vmcnt(0)\n"
"ds_read_b128 v[32:35], v60 offset:32768\n"
"ds_read_b128 v[36:39], v60 offset:33792\n"
"ds_read_b128 v[40:43], v60 offset:34816\n"
"ds_read_b128 v[44:47], v60 offset:35840\n"
"s_waitcnt lgkmcnt(4)\n"
"v_cmp_gt_u32 vcc, 4, v20\n"
"s_nop 1\n"
"s_and_b32 s68, vcc_lo, 15\n"
"s_cbranch_scc1 Lf_100\n"
"Lg_101:\n"
"s_waitcnt lgkmcnt(2)\n"
"v_mfma_f32_16x16x32_bf16 v[48:51], v[56:59], v[32:35], 0\n"
"v_bitop3_b32 v63, 1, v32, v34 bitop3:0x10\n"
"v_bitop3_b32 v63, v63, v36, v38 bitop3:0x10\n"
"v_mfma_f32_16x16x32_bf16 v[52:55], v[32:35], v[32:35], 0\n"
"v_mul_f32 v4, v4, v1\n"
"v_mul_f32 v5, v5, v1\n"
"v_mfma_f32_16x16x32_bf16 v[48:51], v[56:59], v[36:39], v[48:51]\n"
"v_mul_f32 v6, v6, v1\n"
"v_mul_f32 v7, v7, v1\n"
"v_mfma_f32_16x16x32_bf16 v[52:55], v[36:39], v[36:39], v[52:55]\n"
"v_cvt_pk_bf16_f32 v4, v4, v5\n"
"v_cvt_pk_bf16_f32 v5, v6, v7\n"
"s_waitcnt lgkmcnt(1)\n"
"v_mfma_f32_16x16x32_bf16 v[48:51], v[56:59], v[40:43], v[48:51]\n"
"v_mul_f32 v8, v8, v16\n"
"v_mul_f32 v9, v9, v17\n"
"v_mfma_f32_16x16x32_bf16 v[52:55], v[40:43], v[40:43], v[52:55]\n"
"v_mul_f32 v10, v10, v18\n"
"v_mul_f32 v11, v11, v19\n"
"s_waitcnt lgkmcnt(0)\n"
"v_mfma_f32_16x16x32_bf16 v[48:51], v[56:59], v[44:47], v[48:51]\n"
"v_cvt_pk_bf16_f32 v6, v8, v9\n"
"v_cvt_pk_bf16_f32 v7, v10, v11\n"
"v_mfma_f32_16x16x32_bf16 v[52:55], v[44:47], v[44:47], v[52:55]\n"
"ds_write_b128 v62, v[4:7] offset:0\n"
"v_cvt_pk_bf16_f32 v12, v12, v13\n"
"v_cvt_pk_bf16_f32 v13, v14, v15\n"
"v_mfma_f32_16x16x32_bf16 v[24:27], v[68:71], v[32:35], 0\n"
"ds_write_b64 v62, v[12:13] offset:8192\n"
"v_bitop3_b32 v63, v63, v40, v42 bitop3:0x10\n"
"v_bitop3_b32 v63, v63, v44, v46 bitop3:0x10\n"
"v_mfma_f32_16x16x32_bf16 v[20:23], v[32:35], v[72:75], 0\n"
"v_cmp_eq_u32 vcc, 0, v63\n"
"v_mfma_f32_16x16x32_bf16 v[28:31], v[64:67], v[32:35], 0\n"
"s_nop 0\n"
"s_cbranch_vccnz LQ_102\n"
"LP_103:\n"
"s_add_u32 m0, s58, 16384\n"
"s_add_u32 s69, s51, 0xcc4000\n"
"buffer_load_dwordx4 v0, s[12:15], s69 offen offset:0 sc1 lds\n"
"buffer_load_dwordx4 v0, s[12:15], s69 offen offset:1024 sc1 lds\n"
"buffer_load_dwordx4 v0, s[12:15], s69 offen offset:2048 sc1 lds\n"
"buffer_load_dwordx4 v0, s[12:15], s69 offen offset:3072 sc1 lds\n"
"v_mov_b32 v18, 4\n"
"v_mfma_f32_16x16x32_bf16 v[24:27], v[80:83], v[36:39], v[24:27]\n"
"s_lshl_b64 exec, 1, s28\n"
"ds_write_b32 v3, v18 offset:0\n"
"s_mov_b64 exec, -1\n"
"v_cndmask_b32_e64 v52, v52, v53, s[44:45]\n"
"v_cndmask_b32_e64 v54, v54, v55, s[44:45]\n"
"v_mfma_f32_16x16x32_bf16 v[20:23], v[36:39], v[84:87], v[20:23]\n"
"ds_read_b32 v17, v3 offset:0\n"
"v_cndmask_b32_e64 v49, v52, v54, s[46:47]\n"
"v_or_b32 v48, 1, v48\n"
"v_xor_b32 v2, 0x1000, v60\n"
"v_mfma_f32_16x16x32_bf16 v[28:31], v[76:79], v[36:39], v[28:31]\n"
"s_waitcnt lgkmcnt(0)\n"
"v_cmp_gt_u32 vcc, 4, v17\n"
"s_nop 1\n"
"s_and_b32 s68, vcc_lo, 15\n"
"s_cbranch_scc1 Lf_104\n"
"Lg_105:\n"
"ds_read_b128 v[32:35], v2 offset:32768\n"
"ds_read_b128 v[36:39], v2 offset:33792\n"
"v_add_u32 v63, s53, v3\n"
"v_mfma_f32_16x16x32_bf16 v[24:27], v[92:95], v[40:43], v[24:27]\n"
"s_mov_b64 exec, s[48:49]\n"
"ds_write_b64 v63, v[48:49] offset:6144\n"
"s_mov_b64 exec, -1\n"
"v_mfma_f32_16x16x32_bf16 v[20:23], v[40:43], v[96:99], v[20:23]\n"
"v_mfma_f32_16x16x32_bf16 v[28:31], v[88:91], v[40:43], v[28:31]\n"
"ds_read_b128 v[40:43], v2 offset:34816\n"
"v_mfma_f32_16x16x32_bf16 v[24:27], v[104:107], v[44:47], v[24:27]\n"
"v_mfma_f32_16x16x32_bf16 v[20:23], v[44:47], v[108:111], v[20:23]\n"
"v_mfma_f32_16x16x32_bf16 v[28:31], v[100:103], v[44:47], v[28:31]\n"
"ds_read_b128 v[44:47], v2 offset:35840\n"
"s_waitcnt lgkmcnt(4)\n"
"v_mfma_f32_16x16x32_bf16 v[24:27], v[116:119], v[32:35], v[24:27]\n"
"ds_read_b128 v[48:51], v3 offset:6144\n"
"ds_read_b128 v[52:55], v3 offset:6400\n"
"v_mfma_f32_16x16x32_bf16 v[20:23], v[32:35], v[120:123], v[20:23]\n"
"v_xor_b32 v2, 0x2000, v60\n"
"v_mfma_f32_16x16x32_bf16 v[28:31], v[112:115], v[32:35], v[28:31]\n"
"ds_read_b128 v[32:35], v2 offset:32768\n"
"s_waitcnt lgkmcnt(6)\n"
"v_mfma_f32_16x16x32_bf16 v[24:27], v[128:131], v[36:39], v[24:27]\n"
"v_mfma_f32_16x16x32_bf16 v[20:23], v[36:39], v[132:135], v[20:23]\n"
"s_waitcnt lgkmcnt(1)\n"
"v_mfma_f32_16x16x32_bf16 v[28:31], v[124:127], v[36:39], v[28:31]\n"
"ds_read_b128 v[36:39], v2 offset:33792\n"
"v_bitop3_b32 v63, 1, v48, v50 bitop3:0x80\n"
"v_bitop3_b32 v63, v63, v52, v54 bitop3:0x80\n"
"v_mfma_f32_16x16x32_bf16 v[24:27], v[140:143], v[40:43], v[24:27]\n"
"v_cmp_eq_u32 vcc, 0, v63\n"
"s_nop 1\n"
"s_cbranch_vccnz LQ_106\n"
"LP_107:\n"
"v_mfma_f32_16x16x32_bf16 v[20:23], v[40:43], v[144:147], v[20:23]\n"
"v_add_f32 v48, v48, v50\n"
"v_add_f32 v52, v52, v54\n"
"v_mfma_f32_16x16x32_bf16 v[28:31], v[136:139], v[40:43], v[28:31]\n"
"ds_read_b128 v[40:43], v2 offset:34816\n"
"v_mfma_f32_16x16x32_bf16 v[24:27], v[152:155], v[44:47], v[24:27]\n"
"v_add_f32 v49, v49, v51\n"
"v_add_f32 v53, v53, v55\n"
"v_mfma_f32_16x16x32_bf16 v[20:23], v[44:47], v[156:159], v[20:23]\n"
"v_add_f32 v52, v52, v48\n"
"v_add_f32 v53, v53, v49\n"
"v_mfma_f32_16x16x32_bf16 v[28:31], v[148:151], v[44:47], v[28:31]\n"
"ds_read_b128 v[44:47], v2 offset:35840\n"
"s_waitcnt lgkmcnt(3)\n"
"v_mfma_f32_16x16x32_bf16 v[24:27], v[164:167], v[32:35], v[24:27]\n"
"v_mul_f32 v48, s72, v52\n"
"v_mul_f32 v52, 0xba000000, v52\n"
"v_mfma_f32_16x16x32_bf16 v[20:23], v[32:35], v[168:171], v[20:23]\n"
"v_xor_b32 v2, 0x3000, v60\n"
"v_mul_f32 v49, v48, v48\n"
"v_mfma_f32_16x16x32_bf16 v[28:31], v[160:163], v[32:35], v[28:31]\n"
"ds_read_b128 v[32:35], v2 offset:32768\n"
"s_waitcnt lgkmcnt(3)\n"
"v_mfma_f32_16x16x32_bf16 v[24:27], v[176:179], v[36:39], v[24:27]\n"
"v_fma_f32 v54, -v53, s72, -v49\n"
"v_cvt_pk_bf16_f32 v53, v52, v52\n"
"v_mfma_f32_16x16x32_bf16 v[20:23], v[36:39], v[180:183], v[20:23]\n"
"v_max_f32 v54, 0, v54\n"
"v_and_b32 v53, 0xffff0000, v53\n"
"v_mfma_f32_16x16x32_bf16 v[28:31], v[172:175], v[36:39], v[28:31]\n"
"ds_read_b128 v[36:39], v2 offset:33792\n"
"s_waitcnt lgkmcnt(3)\n"
"v_mfma_f32_16x16x32_bf16 v[24:27], v[188:191], v[40:43], v[24:27]\n"
"v_add_f32 v54, 0x3727c5ac, v54\n"
"v_sub_f32 v55, v52, v53\n"
"v_mfma_f32_16x16x32_bf16 v[20:23], v[40:43], v[192:195], v[20:23]\n"
"v_rsq_f32 v1, v54\n"
"v_cvt_pk_bf16_f32 v53, v52, v55\n"
"v_mfma_f32_16x16x32_bf16 v[28:31], v[184:187], v[40:43], v[28:31]\n"
"ds_read_b128 v[40:43], v2 offset:34816\n"
"s_waitcnt lgkmcnt(3)\n"
"v_mfma_f32_16x16x32_bf16 v[24:27], v[200:203], v[44:47], v[24:27]\n"
"v_cvt_pk_bf16_f32 v52, v52, v55\n"
"v_mul_f32 v55, s73, v1\n"
"v_mfma_f32_16x16x32_bf16 v[20:23], v[44:47], v[204:207], v[20:23]\n"
"s_mov_b64 exec, 0xffff\n"
"ds_write2_b32 v3, v1, v55 offset0:130 offset1:131\n"
"s_mov_b64 exec, -1\n"
"v_add_u32 v63, s52, v3\n"
"v_mfma_f32_16x16x32_bf16 v[28:31], v[196:199], v[44:47], v[28:31]\n"
"ds_read_b128 v[44:47], v2 offset:35840\n"
"ds_read_b128 v[48:51], v63\n"
"ds_read_b64 v[54:55], v63 offset:256\n"
"s_waitcnt lgkmcnt(6)\n"
"v_mfma_f32_16x16x32_bf16 v[24:27], v[212:215], v[32:35], v[24:27]\n"
"v_mfma_f32_16x16x32_bf16 v[20:23], v[32:35], v[216:219], v[20:23]\n"
"v_mfma_f32_16x16x32_bf16 v[28:31], v[208:211], v[32:35], v[28:31]\n"
"s_waitcnt lgkmcnt(5)\n"
"v_mfma_f32_16x16x32_bf16 v[24:27], v[224:227], v[36:39], v[24:27]\n"
"ds_read2_b32 v[16:17], v61 offset0:130 offset1:134\n"
"ds_read2_b32 v[18:19], v61 offset0:138 offset1:142\n"
"v_mfma_f32_16x16x32_bf16 v[20:23], v[36:39], v[228:231], v[20:23]\n"
"v_mfma_f32_16x16x32_bf16 v[28:31], v[220:223], v[36:39], v[28:31]\n"
"s_waitcnt lgkmcnt(6)\n"
"v_mfma_f32_16x16x32_bf16 v[24:27], v[236:239], v[40:43], v[24:27]\n"
"v_mfma_f32_16x16x32_bf16 v[20:23], v[40:43], v[240:243], v[20:23]\n"
"v_mfma_f32_16x16x32_bf16 v[28:31], v[232:235], v[40:43], v[28:31]\n"
"s_waitcnt lgkmcnt(4)\n"
"s_waitcnt lgkmcnt(2)\n"
"v_mfma_f32_16x16x16_bf16 v[24:27], v[50:51], v[52:53], v[24:27]\n"
"v_mfma_f32_16x16x16_bf16 v[20:23], v[52:53], v[54:55], v[20:23]\n"
"v_mfma_f32_16x16x16_bf16 v[28:31], v[48:49], v[52:53], v[28:31]\n"
"v_mfma_f32_16x16x32_bf16 v[24:27], v[248:251], v[44:47], v[24:27]\n"
"v_mfma_f32_16x16x32_bf16 v[20:23], v[44:47], v[252:255], v[20:23]\n"
"v_mfma_f32_16x16x32_bf16 v[28:31], v[244:247], v[44:47], v[28:31]\n"
"s_waitcnt vmcnt(0)\n"
"ds_read_b128 v[32:35], v60 offset:16384\n"
"ds_read_b128 v[36:39], v60 offset:17408\n"
"ds_read_b128 v[40:43], v60 offset:18432\n"
"ds_read_b128 v[44:47], v60 offset:19456\n"
"s_waitcnt lgkmcnt(4)\n"
"s_waitcnt lgkmcnt(2)\n"
"v_mfma_f32_16x16x32_bf16 v[48:51], v[56:59], v[32:35], 0\n"
"v_bitop3_b32 v63, 1, v32, v34 bitop3:0x10\n"
"v_bitop3_b32 v63, v63, v36, v38 bitop3:0x10\n"
"v_mfma_f32_16x16x32_bf16 v[52:55], v[32:35], v[32:35], 0\n"
"v_mul_f32 v24, v24, v1\n"
"v_mul_f32 v25, v25, v1\n"
"v_mfma_f32_16x16x32_bf16 v[48:51], v[56:59], v[36:39], v[48:51]\n"
"v_mul_f32 v26, v26, v1\n"
"v_mul_f32 v27, v27, v1\n"
"v_mfma_f32_16x16x32_bf16 v[52:55], v[36:39], v[36:39], v[52:55]\n"
"v_mul_f32 v20, v20, v16\n"
"v_mul_f32 v21, v21, v17\n"
"s_waitcnt lgkmcnt(1)\n"
"v_mfma_f32_16x16x32_bf16 v[48:51], v[56:59], v[40:43], v[48:51]\n"
"v_mul_f32 v22, v22, v18\n"
"v_mul_f32 v23, v23, v19\n"
"v_mfma_f32_16x16x32_bf16 v[52:55], v[40:43], v[40:43], v[52:55]\n"
"v_cvt_pk_bf16_f32 v20, v20, v21\n"
"v_cvt_pk_bf16_f32 v21, v22, v23\n"
"s_waitcnt lgkmcnt(0)\n"
"v_mfma_f32_16x16x32_bf16 v[48:51], v[56:59], v[44:47], v[48:51]\n"
"v_cvt_pk_bf16_f32 v22, v24, v25\n"
"v_cvt_pk_bf16_f32 v23, v26, v27\n"
"v_mfma_f32_16x16x32_bf16 v[52:55], v[44:47], v[44:47], v[52:55]\n"
"ds_write_b128 v62, v[20:23] offset:4096\n"
"v_cvt_pk_bf16_f32 v28, v28, v29\n"
"v_cvt_pk_bf16_f32 v29, v30, v31\n"
"v_mfma_f32_16x16x32_bf16 v[4:7], v[68:71], v[32:35], 0\n"
"ds_write_b64 v62, v[28:29] offset:8200\n"
"v_bitop3_b32 v63, v63, v40, v42 bitop3:0x10\n"
"v_bitop3_b32 v63, v63, v44, v46 bitop3:0x10\n"
"v_mfma_f32_16x16x32_bf16 v[8:11], v[32:35], v[72:75], 0\n"
"v_cmp_eq_u32 vcc, 0, v63\n"
"v_mfma_f32_16x16x32_bf16 v[12:15], v[64:67], v[32:35], 0\n"
"s_nop 0\n"
"s_cbranch_vccnz LQ_108\n"
"LP_109:\n"
"v_mfma_f32_16x16x32_bf16 v[4:7], v[80:83], v[36:39], v[4:7]\n"
"v_cndmask_b32_e64 v52, v52, v53, s[44:45]\n"
"v_cndmask_b32_e64 v54, v54, v55, s[44:45]\n"
"v_mfma_f32_16x16x32_bf16 v[8:11], v[36:39], v[84:87], v[8:11]\n"
"v_cndmask_b32_e64 v49, v52, v54, s[46:47]\n"
"v_or_b32 v48, 1, v48\n"
"v_xor_b32 v2, 0x1000, v60\n"
"v_mfma_f32_16x16x32_bf16 v[12:15], v[76:79], v[36:39], v[12:15]\n"
"s_waitcnt lgkmcnt(0)\n"
"s_barrier\n"
"ds_read_b128 v[32:35], v2 offset:16384\n"
"ds_read_b128 v[36:39], v2 offset:17408\n"
"v_add_u32 v63, s53, v3\n"
"v_mfma_f32_16x16x32_bf16 v[4:7], v[92:95], v[40:43], v[4:7]\n"
"s_mov_b64 exec, s[48:49]\n"
"ds_write_b64 v63, v[48:49] offset:5632\n"
"s_mov_b64 exec, -1\n"
"v_mfma_f32_16x16x32_bf16 v[8:11], v[40:43], v[96:99], v[8:11]\n"
"v_mfma_f32_16x16x32_bf16 v[12:15], v[88:91], v[40:43], v[12:15]\n"
"ds_read_b128 v[40:43], v2 offset:18432\n"
"s_add_u32 m0, s58, 0\n"
"s_add_u32 s69, s51, 0xcc0000\n"
"buffer_load_dwordx4 v0, s[12:15], s69 offen offset:0 sc1 lds\n"
"buffer_load_dwordx4 v0, s[12:15], s69 offen offset:1024 sc1 lds\n"
"buffer_load_dwordx4 v0, s[12:15], s69 offen offset:2048 sc1 lds\n"
"buffer_load_dwordx4 v0, s[12:15], s69 offen offset:3072 sc1 lds\n"
"v_mfma_f32_16x16x32_bf16 v[4:7], v[104:107], v[44:47], v[4:7]\n"
"v_mfma_f32_16x16x32_bf16 v[8:11], v[44:47], v[108:111], v[8:11]\n"
"v_mfma_f32_16x16x32_bf16 v[12:15], v[100:103], v[44:47], v[12:15]\n"
"ds_read_b128 v[44:47], v2 offset:19456\n"
"s_waitcnt lgkmcnt(4)\n"
"v_mfma_f32_16x16x32_bf16 v[4:7], v[116:119], v[32:35], v[4:7]\n"
"ds_read_b128 v[48:51], v3 offset:5632\n"
"ds_read_b128 v[52:55], v3 offset:5888\n"
"v_mfma_f32_16x16x32_bf16 v[8:11], v[32:35], v[120:123], v[8:11]\n"
"v_xor_b32 v2, 0x2000, v60\n"
"v_mfma_f32_16x16x32_bf16 v[12:15], v[112:115], v[32:35], v[12:15]\n"
"ds_read_b128 v[32:35], v2 offset:16384\n"
"s_waitcnt lgkmcnt(6)\n"
"v_mfma_f32_16x16x32_bf16 v[4:7], v[128:131], v[36:39], v[4:7]\n"
"v_mfma_f32_16x16x32_bf16 v[8:11], v[36:39], v[132:135], v[8:11]\n"
"s_waitcnt lgkmcnt(1)\n"
"v_mfma_f32_16x16x32_bf16 v[12:15], v[124:127], v[36:39], v[12:15]\n"
"ds_read_b128 v[36:39], v2 offset:17408\n"
"v_bitop3_b32 v63, 1, v48, v50 bitop3:0x80\n"
"v_bitop3_b32 v63, v63, v52, v54 bitop3:0x80\n"
"v_mfma_f32_16x16x32_bf16 v[4:7], v[140:143], v[40:43], v[4:7]\n"
"v_cmp_eq_u32 vcc, 0, v63\n"
"s_nop 1\n"
"s_cbranch_vccnz LQ_110\n"
"LP_111:\n"
"v_mfma_f32_16x16x32_bf16 v[8:11], v[40:43], v[144:147], v[8:11]\n"
"v_add_f32 v48, v48, v50\n"
"v_add_f32 v52, v52, v54\n"
"v_mfma_f32_16x16x32_bf16 v[12:15], v[136:139], v[40:43], v[12:15]\n"
"ds_read_b128 v[40:43], v2 offset:18432\n"
"v_mfma_f32_16x16x32_bf16 v[4:7], v[152:155], v[44:47], v[4:7]\n"
"v_add_f32 v49, v49, v51\n"
"v_add_f32 v53, v53, v55\n"
"v_mfma_f32_16x16x32_bf16 v[8:11], v[44:47], v[156:159], v[8:11]\n"
"v_add_f32 v52, v52, v48\n"
"v_add_f32 v53, v53, v49\n"
"v_mfma_f32_16x16x32_bf16 v[12:15], v[148:151], v[44:47], v[12:15]\n"
"ds_read_b128 v[44:47], v2 offset:19456\n"
"s_waitcnt lgkmcnt(3)\n"
"v_mfma_f32_16x16x32_bf16 v[4:7], v[164:167], v[32:35], v[4:7]\n"
"v_mul_f32 v48, s72, v52\n"
"v_mul_f32 v52, 0xba000000, v52\n"
"v_mfma_f32_16x16x32_bf16 v[8:11], v[32:35], v[168:171], v[8:11]\n"
"v_xor_b32 v2, 0x3000, v60\n"
"v_mul_f32 v49, v48, v48\n"
"v_mfma_f32_16x16x32_bf16 v[12:15], v[160:163], v[32:35], v[12:15]\n"
"ds_read_b128 v[32:35], v2 offset:16384\n"
"s_waitcnt lgkmcnt(3)\n"
"v_mfma_f32_16x16x32_bf16 v[4:7], v[176:179], v[36:39], v[4:7]\n"
"v_fma_f32 v54, -v53, s72, -v49\n"
"v_cvt_pk_bf16_f32 v53, v52, v52\n"
"v_mfma_f32_16x16x32_bf16 v[8:11], v[36:39], v[180:183], v[8:11]\n"
"v_max_f32 v54, 0, v54\n"
"v_and_b32 v53, 0xffff0000, v53\n"
"v_mfma_f32_16x16x32_bf16 v[12:15], v[172:175], v[36:39], v[12:15]\n"
"ds_read_b128 v[36:39], v2 offset:17408\n"
"s_waitcnt lgkmcnt(3)\n"
"v_mfma_f32_16x16x32_bf16 v[4:7], v[188:191], v[40:43], v[4:7]\n"
"v_add_f32 v54, 0x3727c5ac, v54\n"
"v_sub_f32 v55, v52, v53\n"
"v_mfma_f32_16x16x32_bf16 v[8:11], v[40:43], v[192:195], v[8:11]\n"
"v_rsq_f32 v1, v54\n"
"v_cvt_pk_bf16_f32 v53, v52, v55\n"
"v_mfma_f32_16x16x32_bf16 v[12:15], v[184:187], v[40:43], v[12:15]\n"
"ds_read_b128 v[40:43], v2 offset:18432\n"
"s_waitcnt lgkmcnt(3)\n"
"v_mfma_f32_16x16x32_bf16 v[4:7], v[200:203], v[44:47], v[4:7]\n"
"v_cvt_pk_bf16_f32 v52, v52, v55\n"
"v_mul_f32 v55, s73, v1\n"
"v_mfma_f32_16x16x32_bf16 v[8:11], v[44:47], v[204:207], v[8:11]\n"
"s_mov_b64 exec, 0xffff\n"
"ds_write2_b32 v3, v1, v55 offset0:66 offset1:67\n"
"s_mov_b64 exec, -1\n"
"v_add_u32 v63, s52, v3\n"
"v_mfma_f32_16x16x32_bf16 v[12:15], v[196:199], v[44:47], v[12:15]\n"
"ds_read_b128 v[44:47], v2 offset:19456\n"
"ds_read_b128 v[48:51], v63\n"
"ds_read_b64 v[54:55], v63 offset:256\n"
"s_waitcnt lgkmcnt(6)\n"
"v_mfma_f32_16x16x32_bf16 v[4:7], v[212:215], v[32:35], v[4:7]\n"
"v_mfma_f32_16x16x32_bf16 v[8:11], v[32:35], v[216:219], v[8:11]\n"
"v_mfma_f32_16x16x32_bf16 v[12:15], v[208:211], v[32:35], v[12:15]\n"
"s_waitcnt lgkmcnt(5)\n"
"v_mfma_f32_16x16x32_bf16 v[4:7], v[224:227], v[36:39], v[4:7]\n"
"ds_read2_b32 v[16:17], v61 offset0:66 offset1:70\n"
"ds_read2_b32 v[18:19], v61 offset0:74 offset1:78\n"
"v_mfma_f32_16x16x32_bf16 v[8:11], v[36:39], v[228:231], v[8:11]\n"
"v_mfma_f32_16x16x32_bf16 v[12:15], v[220:223], v[36:39], v[12:15]\n"
"s_waitcnt lgkmcnt(6)\n"
"v_mfma_f32_16x16x32_bf16 v[4:7], v[236:239], v[40:43], v[4:7]\n"
"v_mfma_f32_16x16x32_bf16 v[8:11], v[40:43], v[240:243], v[8:11]\n"
"v_mfma_f32_16x16x32_bf16 v[12:15], v[232:235], v[40:43], v[12:15]\n"
"s_waitcnt lgkmcnt(4)\n"
"s_waitcnt lgkmcnt(2)\n"
"v_mfma_f32_16x16x16_bf16 v[4:7], v[50:51], v[52:53], v[4:7]\n"
"v_mfma_f32_16x16x16_bf16 v[8:11], v[52:53], v[54:55], v[8:11]\n"
"v_mfma_f32_16x16x16_bf16 v[12:15], v[48:49], v[52:53], v[12:15]\n"
"v_mfma_f32_16x16x32_bf16 v[4:7], v[248:251], v[44:47], v[4:7]\n"
"v_mfma_f32_16x16x32_bf16 v[8:11], v[44:47], v[252:255], v[8:11]\n"
"v_mfma_f32_16x16x32_bf16 v[12:15], v[244:247], v[44:47], v[12:15]\n"
"s_waitcnt vmcnt(0)\n"
"ds_read_b128 v[32:35], v60 offset:0\n"
"ds_read_b128 v[36:39], v60 offset:1024\n"
"ds_read_b128 v[40:43], v60 offset:2048\n"
"ds_read_b128 v[44:47], v60 offset:3072\n"
"s_waitcnt lgkmcnt(4)\n"
"s_waitcnt lgkmcnt(2)\n"
"v_mfma_f32_16x16x32_bf16 v[48:51], v[56:59], v[32:35], 0\n"
"v_bitop3_b32 v63, 1, v32, v34 bitop3:0x10\n"
"v_bitop3_b32 v63, v63, v36, v38 bitop3:0x10\n"
"v_mfma_f32_16x16x32_bf16 v[52:55], v[32:35], v[32:35], 0\n"
"v_mul_f32 v4, v4, v1\n"
"v_mul_f32 v5, v5, v1\n"
"v_mfma_f32_16x16x32_bf16 v[48:51], v[56:59], v[36:39], v[48:51]\n"
"v_mul_f32 v6, v6, v1\n"
"v_mul_f32 v7, v7, v1\n"
"v_mfma_f32_16x16x32_bf16 v[52:55], v[36:39], v[36:39], v[52:55]\n"
"v_cvt_pk_bf16_f32 v4, v4, v5\n"
"v_cvt_pk_bf16_f32 v5, v6, v7\n"
"s_waitcnt lgkmcnt(1)\n"
"v_mfma_f32_16x16x32_bf16 v[48:51], v[56:59], v[40:43], v[48:51]\n"
"v_mul_f32 v8, v8, v16\n"
"v_mul_f32 v9, v9, v17\n"
"v_mfma_f32_16x16x32_bf16 v[52:55], v[40:43], v[40:43], v[52:55]\n"
"v_mul_f32 v10, v10, v18\n"
"v_mul_f32 v11, v11, v19\n"
"s_waitcnt lgkmcnt(0)\n"
"v_mfma_f32_16x16x32_bf16 v[48:51], v[56:59], v[44:47], v[48:51]\n"
"v_cvt_pk_bf16_f32 v6, v8, v9\n"
"v_cvt_pk_bf16_f32 v7, v10, v11\n"
"v_mfma_f32_16x16x32_bf16 v[52:55], v[44:47], v[44:47], v[52:55]\n"
"ds_write_b128 v62, v[4:7] offset:0\n"
"v_cvt_pk_bf16_f32 v12, v12, v13\n"
"v_cvt_pk_bf16_f32 v13, v14, v15\n"
"v_mfma_f32_16x16x32_bf16 v[24:27], v[68:71], v[32:35], 0\n"
"ds_write_b64 v62, v[12:13] offset:8192\n"
"v_bitop3_b32 v63, v63, v40, v42 bitop3:0x10\n"
"v_bitop3_b32 v63, v63, v44, v46 bitop3:0x10\n"
"v_mfma_f32_16x16x32_bf16 v[20:23], v[32:35], v[72:75], 0\n"
"v_cmp_eq_u32 vcc, 0, v63\n"
"v_mfma_f32_16x16x32_bf16 v[28:31], v[64:67], v[32:35], 0\n"
"s_nop 0\n"
"s_cbranch_vccnz LQ_112\n"
"LP_113:\n"
"v_mfma_f32_16x16x32_bf16 v[24:27], v[80:83], v[36:39], v[24:27]\n"
"v_cndmask_b32_e64 v52, v52, v53, s[44:45]\n"
"v_cndmask_b32_e64 v54, v54, v55, s[44:45]\n"
"v_mfma_f32_16x16x32_bf16 v[20:23], v[36:39], v[84:87], v[20:23]\n"
"v_cndmask_b32_e64 v49, v52, v54, s[46:47]\n"
"v_or_b32 v48, 1, v48\n"
"v_xor_b32 v2, 0x1000, v60\n"
"v_mfma_f32_16x16x32_bf16 v[28:31], v[76:79], v[36:39], v[28:31]\n"
"s_waitcnt lgkmcnt(0)\n"
"s_barrier\n"
"ds_read_b128 v[32:35], v2 offset:0\n"
"ds_read_b128 v[36:39], v2 offset:1024\n"
"s_add_u32 s69, s51, 0xacc000\n"
"buffer_load_dwordx4 v[4:7], v0, s[12:15], s69 offen offset:0 sc1\n"
"buffer_load_dwordx4 v[8:11], v0, s[12:15], s69 offen offset:1024 sc1\n"
"buffer_load_dwordx4 v[12:15], v0, s[12:15], s69 offen offset:2048 sc1\n"
"buffer_load_dwordx4 v[16:19], v0, s[12:15], s69 offen offset:3072 sc1\n"
"v_add_u32 v63, s53, v3\n"
"v_mfma_f32_16x16x32_bf16 v[24:27], v[92:95], v[40:43], v[24:27]\n"
"s_mov_b64 exec, s[48:49]\n"
"ds_write_b64 v63, v[48:49] offset:5120\n"
"s_mov_b64 exec, -1\n"
"v_mfma_f32_16x16x32_bf16 v[20:23], v[40:43], v[96:99], v[20:23]\n"
"v_mfma_f32_16x16x32_bf16 v[28:31], v[88:91], v[40:43], v[28:31]\n"
"ds_read_b128 v[40:43], v2 offset:2048\n"
"v_mfma_f32_16x16x32_bf16 v[24:27], v[104:107], v[44:47], v[24:27]\n"
"v_mfma_f32_16x16x32_bf16 v[20:23], v[44:47], v[108:111], v[20:23]\n"
"v_mfma_f32_16x16x32_bf16 v[28:31], v[100:103], v[44:47], v[28:31]\n"
"ds_read_b128 v[44:47], v2 offset:3072\n"
"s_waitcnt lgkmcnt(4)\n"
"v_mfma_f32_16x16x32_bf16 v[24:27], v[116:119], v[32:35], v[24:27]\n"
"ds_read_b128 v[48:51], v3 offset:5120\n"
"ds_read_b128 v[52:55], v3 offset:5376\n"
"v_mfma_f32_16x16x32_bf16 v[20:23], v[32:35], v[120:123], v[20:23]\n"
"v_xor_b32 v2, 0x2000, v60\n"
"v_mfma_f32_16x16x32_bf16 v[28:31], v[112:115], v[32:35], v[28:31]\n"
"ds_read_b128 v[32:35], v2 offset:0\n"
"s_waitcnt lgkmcnt(6)\n"
"v_mfma_f32_16x16x32_bf16 v[24:27], v[128:131], v[36:39], v[24:27]\n"
"v_mfma_f32_16x16x32_bf16 v[20:23], v[36:39], v[132:135], v[20:23]\n"
"s_waitcnt lgkmcnt(1)\n"
"v_mfma_f32_16x16x32_bf16 v[28:31], v[124:127], v[36:39], v[28:31]\n"
"ds_read_b128 v[36:39], v2 offset:1024\n"
"v_bitop3_b32 v63, 1, v48, v50 bitop3:0x80\n"
"v_bitop3_b32 v63, v63, v52, v54 bitop3:0x80\n"
"v_mfma_f32_16x16x32_bf16 v[24:27], v[140:143], v[40:43], v[24:27]\n"
"v_cmp_eq_u32 vcc, 0, v63\n"
"s_nop 1\n"
"s_cbranch_vccnz LQ_114\n"
"LP_115:\n"
"v_mfma_f32_16x16x32_bf16 v[20:23], v[40:43], v[144:147], v[20:23]\n"
"v_add_f32 v48, v48, v50\n"
"v_add_f32 v52, v52, v54\n"
"v_mfma_f32_16x16x32_bf16 v[28:31], v[136:139], v[40:43], v[28:31]\n"
"ds_read_b128 v[40:43], v2 offset:2048\n"
"v_mfma_f32_16x16x32_bf16 v[24:27], v[152:155], v[44:47], v[24:27]\n"
"v_add_f32 v49, v49, v51\n"
"v_add_f32 v53, v53, v55\n"
"v_mfma_f32_16x16x32_bf16 v[20:23], v[44:47], v[156:159], v[20:23]\n"
"v_add_f32 v52, v52, v48\n"
"v_add_f32 v53, v53, v49\n"
"v_mfma_f32_16x16x32_bf16 v[28:31], v[148:151], v[44:47], v[28:31]\n"
"ds_read_b128 v[44:47], v2 offset:3072\n"
"s_waitcnt lgkmcnt(3)\n"
"v_mfma_f32_16x16x32_bf16 v[24:27], v[164:167], v[32:35], v[24:27]\n"
"v_mul_f32 v48, s72, v52\n"
"v_mul_f32 v52, 0xba000000, v52\n"
"v_mfma_f32_16x16x32_bf16 v[20:23], v[32:35], v[168:171], v[20:23]\n"
"v_xor_b32 v2, 0x3000, v60\n"
"v_mul_f32 v49, v48, v48\n"
"v_mfma_f32_16x16x32_bf16 v[28:31], v[160:163], v[32:35], v[28:31]\n"
"ds_read_b128 v[32:35], v2 offset:0\n"
"s_waitcnt lgkmcnt(3)\n"
"v_mfma_f32_16x16x32_bf16 v[24:27], v[176:179], v[36:39], v[24:27]\n"
"v_fma_f32 v54, -v53, s72, -v49\n"
"v_cvt_pk_bf16_f32 v53, v52, v52\n"
"v_mfma_f32_16x16x32_bf16 v[20:23], v[36:39], v[180:183], v[20:23]\n"
"v_max_f32 v54, 0, v54\n"
"v_and_b32 v53, 0xffff0000, v53\n"
"v_mfma_f32_16x16x32_bf16 v[28:31], v[172:175], v[36:39], v[28:31]\n"
"ds_read_b128 v[36:39], v2 offset:1024\n"
"s_waitcnt lgkmcnt(3)\n"
"v_mfma_f32_16x16x32_bf16 v[24:27], v[188:191], v[40:43], v[24:27]\n"
"v_add_f32 v54, 0x3727c5ac, v54\n"
"v_sub_f32 v55, v52, v53\n"
"v_mfma_f32_16x16x32_bf16 v[20:23], v[40:43], v[192:195], v[20:23]\n"
"v_rsq_f32 v1, v54\n"
"v_cvt_pk_bf16_f32 v53, v52, v55\n"
"v_mfma_f32_16x16x32_bf16 v[28:31], v[184:187], v[40:43], v[28:31]\n"
"ds_read_b128 v[40:43], v2 offset:2048\n"
"s_waitcnt lgkmcnt(3)\n"
"v_mfma_f32_16x16x32_bf16 v[24:27], v[200:203], v[44:47], v[24:27]\n"
"v_cvt_pk_bf16_f32 v52, v52, v55\n"
"v_mul_f32 v55, s73, v1\n"
"v_mfma_f32_16x16x32_bf16 v[20:23], v[44:47], v[204:207], v[20:23]\n"
"s_mov_b64 exec, 0xffff\n"
"ds_write2_b32 v3, v1, v55 offset0:2 offset1:3\n"
"s_mov_b64 exec, -1\n"
"v_add_u32 v63, s52, v3\n"
"v_mfma_f32_16x16x32_bf16 v[28:31], v[196:199], v[44:47], v[28:31]\n"
"ds_read_b128 v[44:47], v2 offset:3072\n"
"ds_read_b128 v[48:51], v63\n"
"ds_read_b64 v[54:55], v63 offset:256\n"
"s_waitcnt lgkmcnt(6)\n"
"v_mfma_f32_16x16x32_bf16 v[24:27], v[212:215], v[32:35], v[24:27]\n"
"v_mfma_f32_16x16x32_bf16 v[20:23], v[32:35], v[216:219], v[20:23]\n"
"v_mfma_f32_16x16x32_bf16 v[28:31], v[208:211], v[32:35], v[28:31]\n"
"s_waitcnt lgkmcnt(5)\n"
"v_mfma_f32_16x16x32_bf16 v[24:27], v[224:227], v[36:39], v[24:27]\n"
"ds_read2_b32 v[32:33], v61 offset0:2 offset1:6\n"
"ds_read2_b32 v[34:35], v61 offset0:10 offset1:14\n"
"v_mfma_f32_16x16x32_bf16 v[20:23], v[36:39], v[228:231], v[20:23]\n"
"v_add_u32 v63, 153600, v0\n"
"ds_read_b32 v61, v63 offset:4\n"
"v_mfma_f32_16x16x32_bf16 v[28:31], v[220:223], v[36:39], v[28:31]\n"
"s_waitcnt lgkmcnt(7)\n"
"v_mfma_f32_16x16x32_bf16 v[24:27], v[236:239], v[40:43], v[24:27]\n"
"s_xor_b32 s68, s28, 1\n"
"s_lshl_b32 s68, s68, 5\n"
"v_mfma_f32_16x16x32_bf16 v[20:23], v[40:43], v[240:243], v[20:23]\n"
"s_waitcnt lgkmcnt(0)\n"
"v_xor_b32 v61, s68, v61\n"
"v_add_u32 v61, s57, v61\n"
"v_mfma_f32_16x16x32_bf16 v[28:31], v[232:235], v[40:43], v[28:31]\n"
"v_mfma_f32_16x16x16_bf16 v[24:27], v[50:51], v[52:53], v[24:27]\n"
"v_mfma_f32_16x16x16_bf16 v[20:23], v[52:53], v[54:55], v[20:23]\n"
"v_mfma_f32_16x16x16_bf16 v[28:31], v[48:49], v[52:53], v[28:31]\n"
"v_mfma_f32_16x16x32_bf16 v[24:27], v[248:251], v[44:47], v[24:27]\n"
"ds_read_b64_tr_b16 v[36:37], v61 offset:4096\n"
"ds_read_b64_tr_b16 v[38:39], v61 offset:4608\n"
"ds_read_b64_tr_b16 v[40:41], v61 offset:8192\n"
"ds_read_b64_tr_b16 v[42:43], v61 offset:8704\n"
"v_mfma_f32_16x16x32_bf16 v[20:23], v[44:47], v[252:255], v[20:23]\n"
"v_mfma_f32_16x16x32_bf16 v[28:31], v[244:247], v[44:47], v[28:31]\n"
"v_add_u32 v63, 152576, v0\n"
"ds_read_b64 v[50:51], v62 offset:16392\n"
"ds_read_b64 v[48:49], v63 offset:8\n"
"ds_read_b64_tr_b16 v[44:45], v61 offset:12288\n"
"ds_read_b64_tr_b16 v[46:47], v61 offset:12800\n"
"v_add_u32 v63, 153600, v0\n"
"v_mul_f32 v24, v24, v1\n"
"v_mul_f32 v25, v25, v1\n"
"v_mul_f32 v26, v26, v1\n"
"v_mul_f32 v27, v27, v1\n"
"v_mul_f32 v20, v20, v32\n"
"v_mul_f32 v21, v21, v33\n"
"v_mul_f32 v22, v22, v34\n"
"v_mul_f32 v23, v23, v35\n"
"v_cvt_pk_bf16_f32 v20, v20, v21\n"
"v_cvt_pk_bf16_f32 v21, v22, v23\n"
"v_cvt_pk_bf16_f32 v22, v24, v25\n"
"v_cvt_pk_bf16_f32 v23, v26, v27\n"
"ds_write_b128 v62, v[20:23] offset:4096\n"
"v_cvt_pk_bf16_f32 v28, v28, v29\n"
"v_cvt_pk_bf16_f32 v29, v30, v31\n"
"ds_write_b64 v62, v[28:29] offset:8200\n"
"ds_read_b64_tr_b16 v[32:33], v61 offset:0\n"
"ds_read_b64_tr_b16 v[34:35], v61 offset:512\n"
"ds_read_b32 v1, v63\n"
"s_waitcnt lgkmcnt(3)\n"
"s_barrier\n"
"s_setprio 3\n"
"v_mfma_f32_16x16x16_bf16 v[28:31], v[48:49], v[50:51], 0\n"
"v_xor_b32 v61, 96, v61\n"
"ds_read_b64_tr_b16 v[48:49], v61 offset:0\n"
"ds_read_b64_tr_b16 v[50:51], v61 offset:512\n"
"s_waitcnt vmcnt(0)\n"
"LB_116:\n"
"s_waitcnt lgkmcnt(3)\n"
"v_mfma_f32_16x16x32_bf16 v[20:23], v[32:35], v[4:7], 0\n"
"ds_read_b64_tr_b16 v[32:33], v61 offset:4096\n"
"ds_read_b64_tr_b16 v[34:35], v61 offset:4608\n"
"v_bitop3_b32 v63, 1, v4, v6 bitop3:0x80\n"
"v_bitop3_b32 v63, v63, v8, v10 bitop3:0x80\n"
"v_mfma_f32_16x16x32_bf16 v[20:23], v[36:39], v[8:11], v[20:23]\n"
"ds_read_b64_tr_b16 v[36:37], v61 offset:8192\n"
"ds_read_b64_tr_b16 v[38:39], v61 offset:8704\n"
"v_bitop3_b32 v63, v63, v12, v14 bitop3:0x80\n"
"v_bitop3_b32 v63, v63, v16, v18 bitop3:0x80\n"
"v_mfma_f32_16x16x32_bf16 v[20:23], v[40:43], v[12:15], v[20:23]\n"
"ds_read_b64_tr_b16 v[40:41], v61 offset:12288\n"
"ds_read_b64_tr_b16 v[42:43], v61 offset:12800\n"
"v_cmp_eq_u32 vcc, 0, v63\n"
"v_mfma_f32_16x16x32_bf16 v[20:23], v[44:47], v[16:19], v[20:23]\n"
"v_xor_b32 v61, 32, v61\n"
"ds_read_b64_tr_b16 v[44:45], v61 offset:0\n"
"ds_read_b64_tr_b16 v[46:47], v61 offset:512\n"
"s_cbranch_vccnz LC_117\n"
"s_waitcnt lgkmcnt(8)\n"
"v_mfma_f32_16x16x32_bf16 v[24:27], v[48:51], v[4:7], 0\n"
"ds_read_b64_tr_b16 v[48:49], v61 offset:4096\n"
"ds_read_b64_tr_b16 v[50:51], v61 offset:4608\n"
"s_waitcnt lgkmcnt(8)\n"
"v_mfma_f32_16x16x32_bf16 v[24:27], v[32:35], v[8:11], v[24:27]\n"
"ds_read_b64_tr_b16 v[32:33], v61 offset:8192\n"
"ds_read_b64_tr_b16 v[34:35], v61 offset:8704\n"
"s_waitcnt lgkmcnt(8)\n"
"v_mfma_f32_16x16x32_bf16 v[24:27], v[36:39], v[12:15], v[24:27]\n"
"ds_read_b64_tr_b16 v[36:37], v61 offset:12288\n"
"ds_read_b64_tr_b16 v[38:39], v61 offset:12800\n"
"ds_write_b128 v60, v[20:23] offset:32768\n"
"s_waitcnt lgkmcnt(9)\n"
"v_mfma_f32_16x16x32_bf16 v[24:27], v[40:43], v[16:19], v[24:27]\n"
"v_xor_b32 v61, 96, v61\n"
"ds_read_b64_tr_b16 v[40:41], v61 offset:0\n"
"ds_read_b64_tr_b16 v[42:43], v61 offset:512\n"
"s_waitcnt lgkmcnt(9)\n"
"v_mfma_f32_16x16x32_bf16 v[20:23], v[44:47], v[4:7], 0\n"
"ds_read_b64_tr_b16 v[44:45], v61 offset:4096\n"
"ds_read_b64_tr_b16 v[46:47], v61 offset:4608\n"
"s_waitcnt lgkmcnt(9)\n"
"v_mfma_f32_16x16x32_bf16 v[20:23], v[48:51], v[8:11], v[20:23]\n"
"ds_read_b64_tr_b16 v[48:49], v61 offset:8192\n"
"ds_read_b64_tr_b16 v[50:51], v61 offset:8704\n"
"s_add_u32 m0, s58, 16384\n"
"s_add_u32 s69, s51, 0xac8000\n"
"buffer_load_dwordx4 v0, s[12:15], s69 offen offset:0 sc1 lds\n"
"buffer_load_dwordx4 v0, s[12:15], s69 offen offset:1024 sc1 lds\n"
"buffer_load_dwordx4 v0, s[12:15], s69 offen offset:2048 sc1 lds\n"
"buffer_load_dwordx4 v0, s[12:15], s69 offen offset:3072 sc1 lds\n"
"s_waitcnt lgkmcnt(9)\n"
"v_mfma_f32_16x16x32_bf16 v[20:23], v[32:35], v[12:15], v[20:23]\n"
"ds_read_b64_tr_b16 v[32:33], v61 offset:12288\n"
"ds_read_b64_tr_b16 v[34:35], v61 offset:12800\n"
"v_xor_b32 v61, 32, v61\n"
"ds_write_b128 v60, v[24:27] offset:33792\n"
"s_waitcnt lgkmcnt(10)\n"
"v_mfma_f32_16x16x32_bf16 v[20:23], v[36:39], v[16:19], v[20:23]\n"
"s_waitcnt lgkmcnt(7)\n"
"v_mfma_f32_16x16x32_bf16 v[28:31], v[40:43], v[4:7], v[28:31]\n"
"s_waitcnt lgkmcnt(5)\n"
"v_mfma_f32_16x16x32_bf16 v[28:31], v[44:47], v[8:11], v[28:31]\n"
"s_waitcnt lgkmcnt(3)\n"
"v_mfma_f32_16x16x32_bf16 v[28:31], v[48:51], v[12:15], v[28:31]\n"
"s_nop 1\n"
"ds_write_b128 v60, v[20:23] offset:34816\n"
"s_waitcnt lgkmcnt(2)\n"
"v_mfma_f32_16x16x32_bf16 v[28:31], v[32:35], v[16:19], v[28:31]\n"
"s_waitcnt lgkmcnt(0)\n"
"s_barrier\n"
"v_xor_b32 v47, 0x3000, v60\n"
"v_xor_b32 v45, 0x1000, v60\n"
"v_xor_b32 v46, 0x2000, v60\n"
"ds_read_b128 v[40:43], v47 offset:34816\n"
"ds_read_b128 v[32:35], v45 offset:32768\n"
"ds_read_b128 v[36:39], v46 offset:33792\n"
"s_add_u32 s69, s50, 0xccc000\n"
"s_waitcnt lgkmcnt(2)\n"
"v_add_f32 v28, v28, v40\n"
"v_add_f32 v29, v29, v41\n"
"v_add_f32 v30, v30, v42\n"
"v_add_f32 v31, v31, v43\n"
"s_waitcnt lgkmcnt(0)\n"
"v_add_f32 v32, v32, v36\n"
"v_add_f32 v33, v33, v37\n"
"v_add_f32 v34, v34, v38\n"
"v_add_f32 v35, v35, v39\n"
"v_add_f32 v28, v28, v32\n"
"v_add_f32 v29, v29, v33\n"
"v_cvt_pk_bf16_f32 v46, v28, v29\n"
"v_add_f32 v30, v30, v34\n"
"v_add_f32 v31, v31, v35\n"
"v_or_b32 v44, 1, v46\n"
"v_cvt_pk_bf16_f32 v45, v30, v31\n"
"buffer_store_dwordx2 v[44:45], v1, s[12:15], s69 offen sc1\n"
"v_mov_b32 v47, v45\n"
"ds_write_b64 v62, v[46:47] offset:16392\n"
"v_add_u32 v63, 152576, v0\n"
"ds_read_b64 v[50:51], v62 offset:16384\n"
"ds_read_b64 v[48:49], v63 offset:8\n"
"ds_read_b64_tr_b16 v[32:33], v61 offset:0\n"
"ds_read_b64_tr_b16 v[34:35], v61 offset:512\n"
"ds_read_b64_tr_b16 v[36:37], v61 offset:4096\n"
"ds_read_b64_tr_b16 v[38:39], v61 offset:4608\n"
"ds_read_b64_tr_b16 v[40:41], v61 offset:8192\n"
"ds_read_b64_tr_b16 v[42:43], v61 offset:8704\n"
"ds_read_b64_tr_b16 v[44:45], v61 offset:12288\n"
"ds_read_b64_tr_b16 v[46:47], v61 offset:12800\n"
"s_waitcnt lgkmcnt(8)\n"
"v_mfma_f32_16x16x16_bf16 v[28:31], v[48:49], v[50:51], 0\n"
"v_xor_b32 v61, 96, v61\n"
"ds_read_b64_tr_b16 v[48:49], v61 offset:0\n"
"ds_read_b64_tr_b16 v[50:51], v61 offset:512\n"
"s_waitcnt vmcnt(1)\n"
"ds_read_b128 v[4:7], v60 offset:16384\n"
"ds_read_b128 v[8:11], v60 offset:17408\n"
"ds_read_b128 v[12:15], v60 offset:18432\n"
"ds_read_b128 v[16:19], v60 offset:19456\n"
"LB_122:\n"
"s_waitcnt lgkmcnt(3)\n"
"v_mfma_f32_16x16x32_bf16 v[20:23], v[32:35], v[4:7], 0\n"
"ds_read_b64_tr_b16 v[32:33], v61 offset:4096\n"
"ds_read_b64_tr_b16 v[34:35], v61 offset:4608\n"
"v_bitop3_b32 v63, 1, v4, v6 bitop3:0x80\n"
"s_waitcnt lgkmcnt(4)\n"
"v_mfma_f32_16x16x32_bf16 v[20:23], v[36:39], v[8:11], v[20:23]\n"
"ds_read_b64_tr_b16 v[36:37], v61 offset:8192\n"
"ds_read_b64_tr_b16 v[38:39], v61 offset:8704\n"
"v_bitop3_b32 v63, v63, v8, v10 bitop3:0x80\n"
"s_waitcnt lgkmcnt(5)\n"
"v_mfma_f32_16x16x32_bf16 v[20:23], v[40:43], v[12:15], v[20:23]\n"
"ds_read_b64_tr_b16 v[40:41], v61 offset:12288\n"
"ds_read_b64_tr_b16 v[42:43], v61 offset:12800\n"
"v_bitop3_b32 v63, v63, v12, v14 bitop3:0x80\n"
"s_waitcnt lgkmcnt(6)\n"
"v_mfma_f32_16x16x32_bf16 v[20:23], v[44:47], v[16:19], v[20:23]\n"
"v_xor_b32 v61, 32, v61\n"
"ds_read_b64_tr_b16 v[44:45], v61 offset:0\n"
"ds_read_b64_tr_b16 v[46:47], v61 offset:512\n"
"v_bitop3_b32 v63, v63, v16, v18 bitop3:0x80\n"
"v_cmp_eq_u32 vcc, 0, v63\n"
"v_mfma_f32_16x16x32_bf16 v[24:27], v[48:51], v[4:7], 0\n"
"ds_read_b64_tr_b16 v[48:49], v61 offset:4096\n"
"ds_read_b64_tr_b16 v[50:51], v61 offset:4608\n"
"s_cbranch_vccnz LC_123\n"
"s_waitcnt lgkmcnt(8)\n"
"v_mfma_f32_16x16x32_bf16 v[24:27], v[32:35], v[8:11], v[24:27]\n"
"ds_read_b64_tr_b16 v[32:33], v61 offset:8192\n"
"ds_read_b64_tr_b16 v[34:35], v61 offset:8704\n"
"s_waitcnt lgkmcnt(8)\n"
"v_mfma_f32_16x16x32_bf16 v[24:27], v[36:39], v[12:15], v[24:27]\n"
"ds_read_b64_tr_b16 v[36:37], v61 offset:12288\n"
"ds_read_b64_tr_b16 v[38:39], v61 offset:12800\n"
"ds_write_b128 v60, v[20:23] offset:16384\n"
"s_waitcnt lgkmcnt(9)\n"
"v_mfma_f32_16x16x32_bf16 v[24:27], v[40:43], v[16:19], v[24:27]\n"
"v_xor_b32 v61, 96, v61\n"
"ds_read_b64_tr_b16 v[40:41], v61 offset:0\n"
"ds_read_b64_tr_b16 v[42:43], v61 offset:512\n"
"s_waitcnt lgkmcnt(9)\n"
"v_mfma_f32_16x16x32_bf16 v[20:23], v[44:47], v[4:7], 0\n"
"ds_read_b64_tr_b16 v[44:45], v61 offset:4096\n"
"ds_read_b64_tr_b16 v[46:47], v61 offset:4608\n"
"s_waitcnt lgkmcnt(9)\n"
"v_mfma_f32_16x16x32_bf16 v[20:23], v[48:51], v[8:11], v[20:23]\n"
"ds_read_b64_tr_b16 v[48:49], v61 offset:8192\n"
"ds_read_b64_tr_b16 v[50:51], v61 offset:8704\n"
"s_waitcnt lgkmcnt(9)\n"
"v_mfma_f32_16x16x32_bf16 v[20:23], v[32:35], v[12:15], v[20:23]\n"
"ds_read_b64_tr_b16 v[32:33], v61 offset:12288\n"
"ds_read_b64_tr_b16 v[34:35], v61 offset:12800\n"
"v_xor_b32 v61, 32, v61\n"
"ds_write_b128 v60, v[24:27] offset:17408\n"
"s_add_u32 m0, s58, 0\n"
"s_add_u32 s69, s51, 0xac4000\n"
"buffer_load_dwordx4 v0, s[12:15], s69 offen offset:0 sc1 lds\n"
"buffer_load_dwordx4 v0, s[12:15], s69 offen offset:1024 sc1 lds\n"
"buffer_load_dwordx4 v0, s[12:15], s69 offen offset:2048 sc1 lds\n"
"buffer_load_dwordx4 v0, s[12:15], s69 offen offset:3072 sc1 lds\n"
"s_waitcnt lgkmcnt(10)\n"
"v_mfma_f32_16x16x32_bf16 v[20:23], v[36:39], v[16:19], v[20:23]\n"
"s_waitcnt lgkmcnt(7)\n"
"v_mfma_f32_16x16x32_bf16 v[28:31], v[40:43], v[4:7], v[28:31]\n"
"s_waitcnt lgkmcnt(5)\n"
"v_mfma_f32_16x16x32_bf16 v[28:31], v[44:47], v[8:11], v[28:31]\n"
"s_waitcnt lgkmcnt(3)\n"
"v_mfma_f32_16x16x32_bf16 v[28:31], v[48:51], v[12:15], v[28:31]\n"
"s_nop 1\n"
"ds_write_b128 v60, v[20:23] offset:18432\n"
"s_waitcnt lgkmcnt(2)\n"
"v_mfma_f32_16x16x32_bf16 v[28:31], v[32:35], v[16:19], v[28:31]\n"
"s_waitcnt lgkmcnt(0)\n"
"s_barrier\n"
"v_xor_b32 v47, 0x3000, v60\n"
"v_xor_b32 v45, 0x1000, v60\n"
"v_xor_b32 v46, 0x2000, v60\n"
"ds_read_b128 v[40:43], v47 offset:18432\n"
"ds_read_b128 v[32:35], v45 offset:16384\n"
"ds_read_b128 v[36:39], v46 offset:17408\n"
"s_add_u32 s69, s50, 0xcc8000\n"
"s_waitcnt lgkmcnt(2)\n"
"v_add_f32 v28, v28, v40\n"
"v_add_f32 v29, v29, v41\n"
"v_add_f32 v30, v30, v42\n"
"v_add_f32 v31, v31, v43\n"
"s_waitcnt lgkmcnt(0)\n"
"v_add_f32 v32, v32, v36\n"
"v_add_f32 v33, v33, v37\n"
"v_add_f32 v34, v34, v38\n"
"v_add_f32 v35, v35, v39\n"
"v_add_f32 v28, v28, v32\n"
"v_add_f32 v29, v29, v33\n"
"v_cvt_pk_bf16_f32 v46, v28, v29\n"
"v_add_f32 v30, v30, v34\n"
"v_add_f32 v31, v31, v35\n"
"v_or_b32 v44, 1, v46\n"
"v_cvt_pk_bf16_f32 v45, v30, v31\n"
"buffer_store_dwordx2 v[44:45], v1, s[12:15], s69 offen sc1\n"
"v_mov_b32 v47, v45\n"
"ds_write_b64 v62, v[46:47] offset:16384\n"
"v_add_u32 v63, 152576, v0\n"
"ds_read_b64 v[50:51], v62 offset:12296\n"
"ds_read_b64 v[48:49], v63 offset:8\n"
"ds_read_b64_tr_b16 v[32:33], v61 offset:0\n"
"ds_read_b64_tr_b16 v[34:35], v61 offset:512\n"
"ds_read_b64_tr_b16 v[36:37], v61 offset:4096\n"
"ds_read_b64_tr_b16 v[38:39], v61 offset:4608\n"
"ds_read_b64_tr_b16 v[40:41], v61 offset:8192\n"
"ds_read_b64_tr_b16 v[42:43], v61 offset:8704\n"
"ds_read_b64_tr_b16 v[44:45], v61 offset:12288\n"
"ds_read_b64_tr_b16 v[46:47], v61 offset:12800\n"
"s_waitcnt lgkmcnt(8)\n"
"v_mfma_f32_16x16x16_bf16 v[28:31], v[48:49], v[50:51], 0\n"
"v_xor_b32 v61, 96, v61\n"
"ds_read_b64_tr_b16 v[48:49], v61 offset:0\n"
"ds_read_b64_tr_b16 v[50:51], v61 offset:512\n"
"s_waitcnt vmcnt(1)\n"
"ds_read_b128 v[4:7], v60 offset:0\n"
"ds_read_b128 v[8:11], v60 offset:1024\n"
"ds_read_b128 v[12:15], v60 offset:2048\n"
"ds_read_b128 v[16:19], v60 offset:3072\n"
"LB_124:\n"
"s_waitcnt lgkmcnt(3)\n"
"v_mfma_f32_16x16x32_bf16 v[20:23], v[32:35], v[4:7], 0\n"
"ds_read_b64_tr_b16 v[32:33], v61 offset:4096\n"
"ds_read_b64_tr_b16 v[34:35], v61 offset:4608\n"
"v_bitop3_b32 v63, 1, v4, v6 bitop3:0x80\n"
"s_waitcnt lgkmcnt(4)\n"
"v_mfma_f32_16x16x32_bf16 v[20:23], v[36:39], v[8:11], v[20:23]\n"
"ds_read_b64_tr_b16 v[36:37], v61 offset:8192\n"
"ds_read_b64_tr_b16 v[38:39], v61 offset:8704\n"
"v_bitop3_b32 v63, v63, v8, v10 bitop3:0x80\n"
"s_waitcnt lgkmcnt(5)\n"
"v_mfma_f32_16x16x32_bf16 v[20:23], v[40:43], v[12:15], v[20:23]\n"
"ds_read_b64_tr_b16 v[40:41], v61 offset:12288\n"
"ds_read_b64_tr_b16 v[42:43], v61 offset:12800\n"
"v_bitop3_b32 v63, v63, v12, v14 bitop3:0x80\n"
"s_waitcnt lgkmcnt(6)\n"
"v_mfma_f32_16x16x32_bf16 v[20:23], v[44:47], v[16:19], v[20:23]\n"
"v_xor_b32 v61, 32, v61\n"
"ds_read_b64_tr_b16 v[44:45], v61 offset:0\n"
"ds_read_b64_tr_b16 v[46:47], v61 offset:512\n"
"v_bitop3_b32 v63, v63, v16, v18 bitop3:0x80\n"
"v_cmp_eq_u32 vcc, 0, v63\n"
"v_mfma_f32_16x16x32_bf16 v[24:27], v[48:51], v[4:7], 0\n"
"ds_read_b64_tr_b16 v[48:49], v61 offset:4096\n"
"ds_read_b64_tr_b16 v[50:51], v61 offset:4608\n"
"s_cbranch_vccnz LC_125\n"
"v_xor_b32 v63, 0x400, v62\n"
"s_waitcnt lgkmcnt(8)\n"
"v_mfma_f32_16x16x32_bf16 v[24:27], v[32:35], v[8:11], v[24:27]\n"
"ds_read_b64_tr_b16 v[32:33], v61 offset:8192\n"
"ds_read_b64_tr_b16 v[34:35], v61 offset:8704\n"
"s_waitcnt lgkmcnt(8)\n"
"v_mfma_f32_16x16x32_bf16 v[24:27], v[36:39], v[12:15], v[24:27]\n"
"ds_read_b64_tr_b16 v[36:37], v61 offset:12288\n"
"ds_read_b64_tr_b16 v[38:39], v61 offset:12800\n"
"ds_write_b128 v63, v[20:23] offset:0\n"
"s_waitcnt lgkmcnt(9)\n"
"v_mfma_f32_16x16x32_bf16 v[24:27], v[40:43], v[16:19], v[24:27]\n"
"v_xor_b32 v61, 96, v61\n"
"ds_read_b64_tr_b16 v[40:41], v61 offset:0\n"
"ds_read_b64_tr_b16 v[42:43], v61 offset:512\n"
"v_xor_b32 v63, 0x800, v62\n"
"s_waitcnt lgkmcnt(9)\n"
"v_mfma_f32_16x16x32_bf16 v[20:23], v[44:47], v[4:7], 0\n"
"ds_read_b64_tr_b16 v[44:45], v61 offset:4096\n"
"ds_read_b64_tr_b16 v[46:47], v61 offset:4608\n"
"s_add_u32 m0, s58, 0\n"
"s_add_u32 s69, s51, 0xac0000\n"
"buffer_load_dwordx4 v0, s[12:15], s69 offen offset:0 sc1 lds\n"
"buffer_load_dwordx4 v0, s[12:15], s69 offen offset:1024 sc1 lds\n"
"buffer_load_dwordx4 v0, s[12:15], s69 offen offset:2048 sc1 lds\n"
"buffer_load_dwordx4 v0, s[12:15], s69 offen offset:3072 sc1 lds\n"
"s_waitcnt lgkmcnt(9)\n"
"v_mfma_f32_16x16x32_bf16 v[20:23], v[48:51], v[8:11], v[20:23]\n"
"ds_read_b64_tr_b16 v[48:49], v61 offset:8192\n"
"ds_read_b64_tr_b16 v[50:51], v61 offset:8704\n"
"s_waitcnt lgkmcnt(9)\n"
"v_mfma_f32_16x16x32_bf16 v[20:23], v[32:35], v[12:15], v[20:23]\n"
"ds_read_b64_tr_b16 v[32:33], v61 offset:12288\n"
"ds_read_b64_tr_b16 v[34:35], v61 offset:12800\n"
"v_xor_b32 v61, 32, v61\n"
"ds_write_b128 v63, v[24:27] offset:4096\n"
"s_waitcnt lgkmcnt(10)\n"
"v_mfma_f32_16x16x32_bf16 v[20:23], v[36:39], v[16:19], v[20:23]\n"
"v_xor_b32 v63, 0xc00, v62\n"
"s_waitcnt lgkmcnt(7)\n"
"v_mfma_f32_16x16x32_bf16 v[28:31], v[40:43], v[4:7], v[28:31]\n"
"s_waitcnt lgkmcnt(5)\n"
"v_mfma_f32_16x16x32_bf16 v[28:31], v[44:47], v[8:11], v[28:31]\n"
"s_waitcnt lgkmcnt(3)\n"
"v_mfma_f32_16x16x32_bf16 v[28:31], v[48:51], v[12:15], v[28:31]\n"
"s_nop 0\n"
"ds_write_b128 v63, v[20:23] offset:8192\n"
"s_waitcnt lgkmcnt(2)\n"
"v_mfma_f32_16x16x32_bf16 v[28:31], v[32:35], v[16:19], v[28:31]\n"
"v_mov_b32 v63, 9\n"
"s_lshl_b64 exec, 1, s28\n"
"ds_write_b32 v3, v63 offset:4\n"
"s_mov_b64 exec, -1\n"
"ds_read_b32 v63, v3 offset:4\n"
"s_waitcnt lgkmcnt(0)\n"
"v_cmp_gt_u32 vcc, 9, v63\n"
"s_nop 1\n"
"s_and_b32 s68, vcc_lo, 15\n"
"s_cbranch_scc1 Lf_126\n"
"Lg_127:\n"
"ds_read_b128 v[40:43], v62 offset:8192\n"
"ds_read_b128 v[32:35], v62 offset:0\n"
"ds_read_b128 v[36:39], v62 offset:4096\n"
"s_add_u32 s69, s50, 0xcc4000\n"
"s_waitcnt lgkmcnt(2)\n"
"v_add_f32 v28, v28, v40\n"
"v_add_f32 v29, v29, v41\n"
"v_add_f32 v30, v30, v42\n"
"v_add_f32 v31, v31, v43\n"
"s_waitcnt lgkmcnt(0)\n"
"v_mov_b32 v63, 10\n"
"s_lshl_b64 exec, 1, s28\n"
"ds_write_b32 v3, v63 offset:4\n"
"s_mov_b64 exec, -1\n"
"v_add_f32 v32, v32, v36\n"
"v_add_f32 v33, v33, v37\n"
"v_add_f32 v34, v34, v38\n"
"v_add_f32 v35, v35, v39\n"
"v_add_f32 v28, v28, v32\n"
"v_add_f32 v29, v29, v33\n"
"v_cvt_pk_bf16_f32 v46, v28, v29\n"
"v_add_f32 v30, v30, v34\n"
"v_add_f32 v31, v31, v35\n"
"v_or_b32 v44, 1, v46\n"
"v_cvt_pk_bf16_f32 v45, v30, v31\n"
"buffer_store_dwordx2 v[44:45], v1, s[12:15], s69 offen sc1\n"
"v_mov_b32 v47, v45\n"
"ds_write_b64 v62, v[46:47] offset:12296\n"
"s_setprio 3\n"
"v_add_u32 v63, 152576, v0\n"
"ds_read_b64 v[50:51], v62 offset:12288\n"
"ds_read_b64 v[48:49], v63 offset:8\n"
"ds_read_b64_tr_b16 v[32:33], v61 offset:0\n"
"ds_read_b64_tr_b16 v[34:35], v61 offset:512\n"
"ds_read_b64_tr_b16 v[36:37], v61 offset:4096\n"
"ds_read_b64_tr_b16 v[38:39], v61 offset:4608\n"
"ds_read_b64_tr_b16 v[40:41], v61 offset:8192\n"
"ds_read_b64_tr_b16 v[42:43], v61 offset:8704\n"
"ds_read_b64_tr_b16 v[44:45], v61 offset:12288\n"
"ds_read_b64_tr_b16 v[46:47], v61 offset:12800\n"
"s_waitcnt lgkmcnt(8)\n"
"v_mfma_f32_16x16x16_bf16 v[28:31], v[48:49], v[50:51], 0\n"
"v_xor_b32 v61, 96, v61\n"
"ds_read_b64_tr_b16 v[48:49], v61 offset:0\n"
"ds_read_b64_tr_b16 v[50:51], v61 offset:512\n"
"s_waitcnt vmcnt(1)\n"
"ds_read_b128 v[4:7], v60 offset:0\n"
"ds_read_b128 v[8:11], v60 offset:1024\n"
"ds_read_b128 v[12:15], v60 offset:2048\n"
"ds_read_b128 v[16:19], v60 offset:3072\n"
"LB_128:\n"
"s_waitcnt lgkmcnt(3)\n"
"v_mfma_f32_16x16x32_bf16 v[20:23], v[32:35], v[4:7], 0\n"
"ds_read_b64_tr_b16 v[32:33], v61 offset:4096\n"
"ds_read_b64_tr_b16 v[34:35], v61 offset:4608\n"
"v_bitop3_b32 v63, 1, v4, v6 bitop3:0x80\n"
"s_waitcnt lgkmcnt(4)\n"
"v_mfma_f32_16x16x32_bf16 v[20:23], v[36:39], v[8:11], v[20:23]\n"
"ds_read_b64_tr_b16 v[36:37], v61 offset:8192\n"
"ds_read_b64_tr_b16 v[38:39], v61 offset:8704\n"
"v_bitop3_b32 v63, v63, v8, v10 bitop3:0x80\n"
"s_waitcnt lgkmcnt(5)\n"
"v_mfma_f32_16x16x32_bf16 v[20:23], v[40:43], v[12:15], v[20:23]\n"
"ds_read_b64_tr_b16 v[40:41], v61 offset:12288\n"
"ds_read_b64_tr_b16 v[42:43], v61 offset:12800\n"
"v_bitop3_b32 v63, v63, v12, v14 bitop3:0x80\n"
"s_waitcnt lgkmcnt(6)\n"
"v_mfma_f32_16x16x32_bf16 v[20:23], v[44:47], v[16:19], v[20:23]\n"
"v_xor_b32 v61, 32, v61\n"
"ds_read_b64_tr_b16 v[44:45], v61 offset:0\n"
"ds_read_b64_tr_b16 v[46:47], v61 offset:512\n"
"v_bitop3_b32 v63, v63, v16, v18 bitop3:0x80\n"
"v_cmp_eq_u32 vcc, 0, v63\n"
"v_mfma_f32_16x16x32_bf16 v[24:27], v[48:51], v[4:7], 0\n"
"ds_read_b64_tr_b16 v[48:49], v61 offset:4096\n"
"ds_read_b64_tr_b16 v[50:51], v61 offset:4608\n"
"s_cbranch_vccnz LC_129\n"
"s_waitcnt lgkmcnt(8)\n"
"v_mfma_f32_16x16x32_bf16 v[24:27], v[32:35], v[8:11], v[24:27]\n"
"ds_read_b64_tr_b16 v[32:33], v61 offset:8192\n"
"ds_read_b64_tr_b16 v[34:35], v61 offset:8704\n"
"s_waitcnt lgkmcnt(8)\n"
"v_mfma_f32_16x16x32_bf16 v[24:27], v[36:39], v[12:15], v[24:27]\n"
"ds_read_b64_tr_b16 v[36:37], v61 offset:12288\n"
"ds_read_b64_tr_b16 v[38:39], v61 offset:12800\n"
"ds_write_b128 v60, v[20:23] offset:0\n"
"s_waitcnt lgkmcnt(9)\n"
"v_mfma_f32_16x16x32_bf16 v[24:27], v[40:43], v[16:19], v[24:27]\n"
"v_xor_b32 v61, 96, v61\n"
"ds_read_b64_tr_b16 v[40:41], v61 offset:0\n"
"ds_read_b64_tr_b16 v[42:43], v61 offset:512\n"
"s_waitcnt lgkmcnt(9)\n"
"v_mfma_f32_16x16x32_bf16 v[20:23], v[44:47], v[4:7], 0\n"
"ds_read_b64_tr_b16 v[44:45], v61 offset:4096\n"
"ds_read_b64_tr_b16 v[46:47], v61 offset:4608\n"
"s_waitcnt lgkmcnt(9)\n"
"v_mfma_f32_16x16x32_bf16 v[20:23], v[48:51], v[8:11], v[20:23]\n"
"ds_read_b64_tr_b16 v[48:49], v61 offset:8192\n"
"ds_read_b64_tr_b16 v[50:51], v61 offset:8704\n"
"s_waitcnt lgkmcnt(9)\n"
"v_mfma_f32_16x16x32_bf16 v[20:23], v[32:35], v[12:15], v[20:23]\n"
"ds_read_b64_tr_b16 v[32:33], v61 offset:12288\n"
"ds_read_b64_tr_b16 v[34:35], v61 offset:12800\n"
"v_xor_b32 v61, 32, v61\n"
"ds_write_b128 v60, v[24:27] offset:1024\n"
"s_waitcnt lgkmcnt(10)\n"
"v_mfma_f32_16x16x32_bf16 v[20:23], v[36:39], v[16:19], v[20:23]\n"
"s_waitcnt lgkmcnt(7)\n"
"v_mfma_f32_16x16x32_bf16 v[28:31], v[40:43], v[4:7], v[28:31]\n"
"s_waitcnt lgkmcnt(5)\n"
"v_mfma_f32_16x16x32_bf16 v[28:31], v[44:47], v[8:11], v[28:31]\n"
"s_waitcnt lgkmcnt(3)\n"
"v_mfma_f32_16x16x32_bf16 v[28:31], v[48:51], v[12:15], v[28:31]\n"
"s_nop 1\n"
"ds_write_b128 v60, v[20:23] offset:2048\n"
"s_waitcnt lgkmcnt(2)\n"
"v_mfma_f32_16x16x32_bf16 v[28:31], v[32:35], v[16:19], v[28:31]\n"
"v_mov_b32 v63, 11\n"
"s_lshl_b64 exec, 1, s28\n"
"ds_write_b32 v3, v63 offset:4\n"
"s_mov_b64 exec, -1\n"
"ds_read_b32 v63, v3 offset:4\n"
"s_waitcnt lgkmcnt(0)\n"
"v_cmp_gt_u32 vcc, 11, v63\n"
"s_nop 1\n"
"s_and_b32 s68, vcc_lo, 15\n"
"s_cbranch_scc1 Lf_130\n"
"Lg_131:\n"
"v_xor_b32 v47, 0x3000, v60\n"
"v_xor_b32 v45, 0x1000, v60\n"
"v_xor_b32 v46, 0x2000, v60\n"
"ds_read_b128 v[40:43], v47 offset:2048\n"
"ds_read_b128 v[32:35], v45 offset:0\n"
"ds_read_b128 v[36:39], v46 offset:1024\n"
"s_add_u32 s69, s50, 0xcc0000\n"
"s_waitcnt lgkmcnt(2)\n"
"v_add_f32 v28, v28, v40\n"
"v_add_f32 v29, v29, v41\n"
"v_add_f32 v30, v30, v42\n"
"v_add_f32 v31, v31, v43\n"
"s_waitcnt lgkmcnt(0)\n"
"v_mov_b32 v63, 12\n"
"s_lshl_b64 exec, 1, s28\n"
"ds_write_b32 v3, v63 offset:4\n"
"s_mov_b64 exec, -1\n"
"v_add_f32 v32, v32, v36\n"
"v_add_f32 v33, v33, v37\n"
"v_add_f32 v34, v34, v38\n"
"v_add_f32 v35, v35, v39\n"
"v_add_f32 v28, v28, v32\n"
"v_add_f32 v29, v29, v33\n"
"v_cvt_pk_bf16_f32 v46, v28, v29\n"
"v_add_f32 v30, v30, v34\n"
"v_add_f32 v31, v31, v35\n"
"v_or_b32 v44, 1, v46\n"
"v_cvt_pk_bf16_f32 v45, v30, v31\n"
"buffer_store_dwordx2 v[44:45], v1, s[12:15], s69 offen sc1\n"
"v_mov_b32 v47, v45\n"
"ds_write_b64 v62, v[46:47] offset:12288\n"
"v_bfe_u32 v56, v0, 5, 2\n"
"v_bfe_u32 v57, v0, 8, 1\n"
"v_xor_b32 v56, v56, v57\n"
"v_and_b32 v57, 16, v0\n"
"v_lshl_add_u32 v56, v56, 5, v57\n"
"v_lshrrev_b32 v57, 7, v0\n"
"v_lshl_add_u32 v56, v57, 10, v56\n"
"v_xor_b32 v57, 64, v56\n"
"s_add_u32 m0, s57, 0\n"
"s_nop 0\n"
"buffer_load_dwordx4 v56, s[4:7], s56 offen lds\n"
"s_add_u32 m0, s57, 1024\n"
"s_add_u32 s71, s56, 0x2000\n"
"buffer_load_dwordx4 v57, s[4:7], s71 offen lds\n"
"v_add_u32 v63, 151552, v0\n"
"v_mov_b32 v40, 0x3f803f80\n"
"v_mov_b32 v41, 0x3f803f80\n"
"ds_read_b128 v[34:37], v63\n"
"ds_read_b64 v[38:39], v63 offset:1024\n"
"s_setprio 2\n"
"ds_read_b32 v63, v3 offset:0\n"
"s_waitcnt lgkmcnt(0)\n"
"v_cmp_gt_u32 vcc, 7, v63\n"
"s_nop 1\n"
"s_and_b32 s68, vcc_lo, 15\n"
"s_cbranch_scc1 Lf_132\n"
"Lg_133:\n"
"s_memrealtime s[70:71]\n"
"ds_read_b128 v[20:23], v62 offset:0\n"
"ds_read_b64 v[28:29], v62 offset:8192\n"
"ds_read_b32 v32, v3 offset:780\n"
"ds_read_b96 v[46:48], v62\n"
"ds_read_b32 v50, v62 offset:12\n"
"v_mov_b32 v49, s64\n"
"v_mov_b32 v51, s64\n"
"v_lshrrev_b32 v63, 1, v0\n"
"s_add_u32 s69, s54, 0x400000\n"
"s_waitcnt lgkmcnt(0)\n"
"buffer_store_dwordx4 v[46:49], v0, s[12:15], s69 offen sc1\n"
"buffer_store_dwordx2 v[50:51], v63, s[12:15], s69 offen offset:1024 sc1\n"
"s_lshl_b32 s68, s30, 10\n"
"s_add_u32 s68, s68, 0xee0000\n"
"s_lshl_b32 s69, s31, 7\n"
"s_add_u32 s68, s68, s69\n"
"s_cmp_eq_u32 s28, 0\n"
"s_cselect_b32 s69, 0, 0x80000000\n"
"v_mov_b32 v52, s64\n"
"v_mov_b32 v53, s65\n"
"v_mov_b32 v54, s70\n"
"v_mov_b32 v55, s69\n"
"s_mov_b64 exec, 1\n"
"buffer_store_dwordx3 v[52:54], v55, s[12:15], s68 offen sc1\n"
"s_mov_b64 exec, -1\n"
"v_mfma_f32_16x16x16_bf16 v[42:45], v[20:21], v[28:29], v[34:37]\n"
"s_add_u32 m0, s57, 2048\n"
"s_add_u32 s71, s56, 0x4000\n"
"buffer_load_dwordx4 v56, s[4:7], s71 offen lds\n"
"s_add_u32 m0, s57, 3072\n"
"s_add_u32 s71, s56, 0x6000\n"
"buffer_load_dwordx4 v57, s[4:7], s71 offen lds\n"
"s_add_u32 m0, s57, 4096\n"
"s_add_u32 s71, s56, 0x8000\n"
"buffer_load_dwordx4 v56, s[4:7], s71 offen lds\n"
"s_add_u32 m0, s57, 5120\n"
"s_add_u32 s71, s56, 0xa000\n"
"buffer_load_dwordx4 v57, s[4:7], s71 offen lds\n"
"s_add_u32 m0, s57, 6144\n"
"s_add_u32 s71, s56, 0xc000\n"
"buffer_load_dwordx4 v56, s[4:7], s71 offen lds\n"
"s_add_u32 m0, s57, 7168\n"
"s_add_u32 s71, s56, 0xe000\n"
"buffer_load_dwordx4 v57, s[4:7], s71 offen lds\n"
"s_add_u32 m0, s57, 8192\n"
"s_add_u32 s71, s56, 0x10000\n"
"buffer_load_dwordx4 v56, s[4:7], s71 offen lds\n"
"s_add_u32 m0, s57, 9216\n"
"s_add_u32 s71, s56, 0x12000\n"
"buffer_load_dwordx4 v57, s[4:7], s71 offen lds\n"
"s_barrier\n"
"ds_read_b128 v[24:27], v62 offset:4096\n"
"ds_read_b64 v[30:31], v62 offset:8200\n"
"ds_read_b32 v33, v3 offset:524\n"
"s_waitcnt lgkmcnt(2)\n"
"v_mfma_f32_16x16x16_bf16 v[4:7], v[26:27], v[28:29], v[34:37]\n"
"s_nop 7\n"
"v_cndmask_b32_e64 v42, v42, v4, s[36:37]\n"
"v_cndmask_b32_e64 v43, v43, v5, s[38:39]\n"
"v_cndmask_b32_e64 v44, v44, v6, s[40:41]\n"
"v_cndmask_b32_e64 v45, v45, v7, s[42:43]\n"
"v_max3_f32 v8, v42, v43, v44\n"
"v_max_f32 v8, v8, v45\n"
"v_mov_b32 v9, v8\n"
"s_nop 1\n"
"v_permlane32_swap_b32 v8, v9\n"
"v_max_f32 v8, v8, v9\n"
"v_mov_b32 v9, v8\n"
"s_nop 1\n"
"v_permlane16_swap_b32 v8, v9\n"
"v_max_f32 v8, v8, v9\n"
"v_mul_f32 v8, v8, v32\n"
"v_fma_f32 v42, v42, v32, -v8\n"
"v_fma_f32 v43, v43, v32, -v8\n"
"v_fma_f32 v44, v44, v32, -v8\n"
"v_fma_f32 v45, v45, v32, -v8\n"
"v_exp_f32 v42, v42\n"
"v_exp_f32 v43, v43\n"
"v_exp_f32 v44, v44\n"
"v_exp_f32 v45, v45\n"
"v_cvt_pk_bf16_f32 v10, v42, v43\n"
"v_cvt_pk_bf16_f32 v11, v44, v45\n"
"v_and_b32 v14, v10, v38\n"
"v_and_b32 v15, v11, v39\n"
"v_mfma_f32_16x16x16_bf16 v[4:7], v[40:41], v[10:11], 0\n"
"v_xor_b32 v12, v10, v14\n"
"v_xor_b32 v13, v11, v15\n"
"s_nop 1\n"
"v_mfma_f32_16x16x32_bf16 v[42:45], v[22:25], v[12:15], 0\n"
"s_lshl_b32 s68, s30, 10\n"
"s_add_u32 s68, s68, 0xee0000\n"
"v_bfe_u32 v52, v0, 4, 3\n"
"v_lshlrev_b32 v52, 7, v52\n"
"buffer_load_dwordx3 v[52:54], v52, s[12:15], s68 offen sc1\n"
"s_add_u32 s69, s50, 0xacc000\n"
"v_rcp_f32 v8, v4\n"
"s_nop 0\n"
"v_mul_f32 v42, v42, v8\n"
"v_mul_f32 v43, v43, v8\n"
"v_mul_f32 v44, v44, v8\n"
"v_mul_f32 v45, v45, v8\n"
"v_cvt_pk_bf16_f32 v10, v42, v43\n"
"v_cvt_pk_bf16_f32 v11, v44, v45\n"
"v_and_b32 v10, -2, v10\n"
"buffer_store_dwordx2 v[10:11], v1, s[12:15], s69 offen sc1\n"
"s_waitcnt lgkmcnt(0)\n"
"v_mfma_f32_16x16x16_bf16 v[42:45], v[26:27], v[30:31], v[34:37]\n"
"s_add_u32 m0, s57, 10240\n"
"s_add_u32 s71, s56, 0x14000\n"
"buffer_load_dwordx4 v56, s[4:7], s71 offen lds\n"
"s_add_u32 m0, s57, 11264\n"
"s_add_u32 s71, s56, 0x16000\n"
"buffer_load_dwordx4 v57, s[4:7], s71 offen lds\n"
"s_add_u32 m0, s57, 12288\n"
"s_add_u32 s71, s56, 0x18000\n"
"buffer_load_dwordx4 v56, s[4:7], s71 offen lds\n"
"s_add_u32 m0, s57, 13312\n"
"s_add_u32 s71, s56, 0x1a000\n"
"buffer_load_dwordx4 v57, s[4:7], s71 offen lds\n"
"s_add_u32 m0, s57, 14336\n"
"s_add_u32 s71, s56, 0x1c000\n"
"buffer_load_dwordx4 v56, s[4:7], s71 offen lds\n"
"s_add_u32 m0, s57, 15360\n"
"s_add_u32 s71, s56, 0x1e000\n"
"buffer_load_dwordx4 v57, s[4:7], s71 offen lds\n"
"s_waitcnt vmcnt(7)\n"
"v_xor_b32 v52, s64, v52\n"
"v_xor_b32 v53, s65, v53\n"
"v_or_b32 v52, v52, v53\n"
"v_cmp_ne_u32 vcc, 0, v52\n"
"s_nop 1\n"
"s_cbranch_vccnz Lr_134\n"
"Lk_135:\n"
"s_memrealtime s[70:71]\n"
"s_waitcnt lgkmcnt(0)\n"
"v_subrev_u32 v53, s70, v54\n"
"s_nop 1\n"
"v_max_i32_dpp v53, v53, v53 quad_perm:[1,0,3,2] row_mask:0xf bank_mask:0xf\n"
"s_nop 1\n"
"v_max_i32_dpp v53, v53, v53 quad_perm:[2,3,0,1] row_mask:0xf bank_mask:0xf\n"
"s_nop 1\n"
"v_max_i32_dpp v53, v53, v53 row_half_mirror row_mask:0xf bank_mask:0xf\n"
"s_nop 0\n"
"v_readfirstlane_b32 s68, v53\n"
"s_nop 1\n"
"s_add_i32 s68, s68, 150\n"
"s_min_i32 s68, s68, 150\n"
"s_cmp_le_i32 s68, 2\n"
"s_cbranch_scc1 La_137\n"
"s_add_u32 s62, s68, s70\n"
"s_mov_b32 s69, 0\n"
"La_136:\n"
"s_memrealtime s[70:71]\n"
"s_add_u32 s69, s69, 1\n"
"s_waitcnt lgkmcnt(0)\n"
"s_cmp_gt_u32 s69, 200\n"
"s_cbranch_scc1 La_137\n"
"s_sub_u32 s68, s70, s62\n"
"s_cmp_lt_i32 s68, 0\n"
"s_cbranch_scc1 La_136\n"
"La_137:\n"
"s_waitcnt vmcnt(0)\n"
"s_barrier\n"
"ds_read_b128 v[20:23], v62 offset:0\n"
"ds_read_b64 v[28:29], v62 offset:8192\n"
"ds_read_b32 v32, v3 offset:268\n"
"s_waitcnt lgkmcnt(2)\n"
"v_mfma_f32_16x16x16_bf16 v[4:7], v[20:21], v[30:31], v[34:37]\n"
"s_nop 7\n"
"v_cndmask_b32_e64 v42, v42, v4, s[36:37]\n"
"v_cndmask_b32_e64 v43, v43, v5, s[38:39]\n"
"v_cndmask_b32_e64 v44, v44, v6, s[40:41]\n"
"v_cndmask_b32_e64 v45, v45, v7, s[42:43]\n"
"v_max3_f32 v8, v42, v43, v44\n"
"v_max_f32 v8, v8, v45\n"
"v_mov_b32 v9, v8\n"
"s_nop 1\n"
"v_permlane32_swap_b32 v8, v9\n"
"v_max_f32 v8, v8, v9\n"
"v_mov_b32 v9, v8\n"
"s_nop 1\n"
"v_permlane16_swap_b32 v8, v9\n"
"v_max_f32 v8, v8, v9\n"
"v_mul_f32 v8, v8, v33\n"
"v_fma_f32 v42, v42, v33, -v8\n"
"v_fma_f32 v43, v43, v33, -v8\n"
"v_fma_f32 v44, v44, v33, -v8\n"
"v_fma_f32 v45, v45, v33, -v8\n"
"v_exp_f32 v42, v42\n"
"v_exp_f32 v43, v43\n"
"v_exp_f32 v44, v44\n"
"v_exp_f32 v45, v45\n"
"v_cvt_pk_bf16_f32 v10, v42, v43\n"
"v_cvt_pk_bf16_f32 v11, v44, v45\n"
"v_and_b32 v12, v10, v38\n"
"v_and_b32 v13, v11, v39\n"
"v_mfma_f32_16x16x16_bf16 v[4:7], v[40:41], v[10:11], 0\n"
"v_xor_b32 v14, v10, v12\n"
"v_xor_b32 v15, v11, v13\n"
"s_nop 1\n"
"v_mfma_f32_16x16x32_bf16 v[42:45], v[22:25], v[12:15], 0\n"
"s_add_u32 s69, s50, 0xac8000\n"
"s_nop 1\n"
"v_rcp_f32 v8, v4\n"
"s_nop 3\n"
"v_mul_f32 v42, v42, v8\n"
"v_mul_f32 v43, v43, v8\n"
"v_mul_f32 v44, v44, v8\n"
"v_mul_f32 v45, v45, v8\n"
"v_cvt_pk_bf16_f32 v10, v42, v43\n"
"v_cvt_pk_bf16_f32 v11, v44, v45\n"
"v_and_b32 v10, -2, v10\n"
"buffer_store_dwordx2 v[10:11], v1, s[12:15], s69 offen sc1\n"
"s_waitcnt lgkmcnt(0)\n"
"v_mfma_f32_16x16x16_bf16 v[42:45], v[20:21], v[28:29], v[34:37]\n"
"v_or_b32 v46, s60, v0\n"
"v_lshrrev_b32 v50, 1, v0\n"
"v_or_b32 v50, s60, v50\n"
"s_add_u32 s69, s54, 0x3f0000\n"
"buffer_load_dwordx4 v[46:49], v46, s[12:15], s69 offen sc1\n"
"buffer_load_dwordx2 v[50:51], v50, s[12:15], s69 offen offset:1024 sc1\n"
"s_barrier\n"
"ds_read_b128 v[24:27], v62 offset:4096\n"
"ds_read_b64 v[30:31], v62 offset:8200\n"
"ds_read_b32 v33, v3 offset:12\n"
"s_waitcnt lgkmcnt(2)\n"
"v_mfma_f32_16x16x16_bf16 v[4:7], v[26:27], v[28:29], v[34:37]\n"
"s_nop 7\n"
"v_cndmask_b32_e64 v42, v42, v4, s[36:37]\n"
"v_cndmask_b32_e64 v43, v43, v5, s[38:39]\n"
"v_cndmask_b32_e64 v44, v44, v6, s[40:41]\n"
"v_cndmask_b32_e64 v45, v45, v7, s[42:43]\n"
"v_max3_f32 v8, v42, v43, v44\n"
"v_max_f32 v8, v8, v45\n"
"v_mov_b32 v9, v8\n"
"s_nop 1\n"
"v_permlane32_swap_b32 v8, v9\n"
"v_max_f32 v8, v8, v9\n"
"v_mov_b32 v9, v8\n"
"s_nop 1\n"
"v_permlane16_swap_b32 v8, v9\n"
"v_max_f32 v8, v8, v9\n"
"v_mul_f32 v8, v8, v32\n"
"v_fma_f32 v42, v42, v32, -v8\n"
"v_fma_f32 v43, v43, v32, -v8\n"
"v_fma_f32 v44, v44, v32, -v8\n"
"v_fma_f32 v45, v45, v32, -v8\n"
"v_exp_f32 v42, v42\n"
"v_exp_f32 v43, v43\n"
"v_exp_f32 v44, v44\n"
"v_exp_f32 v45, v45\n"
"v_cvt_pk_bf16_f32 v10, v42, v43\n"
"v_cvt_pk_bf16_f32 v11, v44, v45\n"
"v_and_b32 v14, v10, v38\n"
"v_and_b32 v15, v11, v39\n"
"v_mfma_f32_16x16x16_bf16 v[4:7], v[40:41], v[10:11], 0\n"
"v_xor_b32 v12, v10, v14\n"
"v_xor_b32 v13, v11, v15\n"
"s_nop 1\n"
"v_mfma_f32_16x16x32_bf16 v[42:45], v[22:25], v[12:15], 0\n"
"s_add_u32 s69, s50, 0xac4000\n"
"s_nop 1\n"
"v_rcp_f32 v8, v4\n"
"s_nop 3\n"
"v_mul_f32 v42, v42, v8\n"
"v_mul_f32 v43, v43, v8\n"
"v_mul_f32 v44, v44, v8\n"
"v_mul_f32 v45, v45, v8\n"
"v_cvt_pk_bf16_f32 v10, v42, v43\n"
"v_cvt_pk_bf16_f32 v11, v44, v45\n"
"v_and_b32 v10, -2, v10\n"
"buffer_store_dwordx2 v[10:11], v1, s[12:15], s69 offen sc1\n"
"s_waitcnt lgkmcnt(0)\n"
"v_mfma_f32_16x16x16_bf16 v[42:45], v[26:27], v[30:31], v[34:37]\n"
"s_barrier\n"
"s_waitcnt vmcnt(1)\n"
"v_xor_b32 v4, s64, v49\n"
"v_xor_b32 v5, s64, v51\n"
"v_or_b32 v4, v4, v5\n"
"v_cmp_ne_u32 vcc, 0, v4\n"
"s_cmp_lg_u32 s60, 0\n"
"s_nop 0\n"
"s_cselect_b64 vcc, 0, vcc\n"
"s_cbranch_vccnz Lr_138\n"
"Lk_139:\n"
"v_min_f32 v4, s61, v34\n"
"v_min_f32 v5, s61, v35\n"
"v_min_f32 v6, s61, v36\n"
"v_min_f32 v7, s61, v37\n"
"v_mov_b32 v22, v48\n"
"v_mov_b32 v23, v50\n"
"v_mfma_f32_16x16x16_bf16 v[4:7], v[46:47], v[30:31], v[4:7]\n"
"s_nop 7\n"
"v_cndmask_b32_e64 v42, v42, v4, s[36:37]\n"
"v_cndmask_b32_e64 v43, v43, v5, s[38:39]\n"
"v_cndmask_b32_e64 v44, v44, v6, s[40:41]\n"
"v_cndmask_b32_e64 v45, v45, v7, s[42:43]\n"
"v_max3_f32 v8, v42, v43, v44\n"
"v_max_f32 v8, v8, v45\n"
"v_mov_b32 v9, v8\n"
"s_nop 1\n"
"v_permlane32_swap_b32 v8, v9\n"
"v_max_f32 v8, v8, v9\n"
"v_mov_b32 v9, v8\n"
"s_nop 1\n"
"v_permlane16_swap_b32 v8, v9\n"
"v_max_f32 v8, v8, v9\n"
"v_mul_f32 v8, v8, v33\n"
"v_fma_f32 v42, v42, v33, -v8\n"
"v_fma_f32 v43, v43, v33, -v8\n"
"v_fma_f32 v44, v44, v33, -v8\n"
"v_fma_f32 v45, v45, v33, -v8\n"
"v_exp_f32 v42, v42\n"
"v_exp_f32 v43, v43\n"
"v_exp_f32 v44, v44\n"
"v_exp_f32 v45, v45\n"
"v_cvt_pk_bf16_f32 v10, v42, v43\n"
"v_cvt_pk_bf16_f32 v11, v44, v45\n"
"v_and_b32 v12, v10, v38\n"
"v_and_b32 v13, v11, v39\n"
"v_mfma_f32_16x16x16_bf16 v[4:7], v[40:41], v[10:11], 0\n"
"v_xor_b32 v14, v10, v12\n"
"v_xor_b32 v15, v11, v13\n"
"s_nop 1\n"
"v_mfma_f32_16x16x32_bf16 v[42:45], v[22:25], v[12:15], 0\n"
"s_add_u32 s69, s50, 0xac0000\n"
"s_nop 1\n"
"v_rcp_f32 v8, v4\n"
"s_nop 3\n"
"v_mul_f32 v42, v42, v8\n"
"v_mul_f32 v43, v43, v8\n"
"v_mul_f32 v44, v44, v8\n"
"v_mul_f32 v45, v45, v8\n"
"v_cvt_pk_bf16_f32 v10, v42, v43\n"
"v_cvt_pk_bf16_f32 v11, v44, v45\n"
"v_and_b32 v10, -2, v10\n"
"buffer_store_dwordx2 v[10:11], v1, s[12:15], s69 offen sc1\n"
"v_mov_b32 v56, 0x3f803f80\n"
"v_mov_b32 v57, 0x3f803f80\n"
"v_mov_b32 v58, 0x3f803f80\n"
"v_mov_b32 v59, 0x3f803f80\n"
"v_lshrrev_b32 v61, 8, v0\n"
"v_lshlrev_b32 v61, 6, v61\n"
"v_add_u32 v61, 154624, v61\n"
"s_sleep 9\n"
"s_add_u32 m0, s58, 49152\n"
"s_add_u32 s69, s51, 0xccc000\n"
"buffer_load_dwordx4 v0, s[12:15], s69 offen offset:0 sc1 lds\n"
"buffer_load_dwordx4 v0, s[12:15], s69 offen offset:1024 sc1 lds\n"
"buffer_load_dwordx4 v0, s[12:15], s69 offen offset:2048 sc1 lds\n"
"buffer_load_dwordx4 v0, s[12:15], s69 offen offset:3072 sc1 lds\n"
"s_barrier\n"
"s_setprio 1\n"
"s_waitcnt vmcnt(0)\n"
"ds_read_b128 v[32:35], v60 offset:49152\n"
"ds_read_b128 v[36:39], v60 offset:50176\n"
"ds_read_b128 v[40:43], v60 offset:51200\n"
"ds_read_b128 v[44:47], v60 offset:52224\n"
"s_waitcnt lgkmcnt(2)\n"
"v_mfma_f32_16x16x32_bf16 v[48:51], v[56:59], v[32:35], 0\n"
"v_bitop3_b32 v63, 1, v32, v34 bitop3:0x10\n"
"v_bitop3_b32 v63, v63, v36, v38 bitop3:0x10\n"
"v_mfma_f32_16x16x32_bf16 v[52:55], v[32:35], v[32:35], 0\n"
"v_mfma_f32_16x16x32_bf16 v[48:51], v[56:59], v[36:39], v[48:51]\n"
"v_mfma_f32_16x16x32_bf16 v[52:55], v[36:39], v[36:39], v[52:55]\n"
"s_waitcnt lgkmcnt(1)\n"
"v_mfma_f32_16x16x32_bf16 v[48:51], v[56:59], v[40:43], v[48:51]\n"
"v_mfma_f32_16x16x32_bf16 v[52:55], v[40:43], v[40:43], v[52:55]\n"
"s_waitcnt lgkmcnt(0)\n"
"v_mfma_f32_16x16x32_bf16 v[48:51], v[56:59], v[44:47], v[48:51]\n"
"v_bitop3_b32 v63, v63, v40, v42 bitop3:0x10\n"
"v_bitop3_b32 v63, v63, v44, v46 bitop3:0x10\n"
"v_mfma_f32_16x16x32_bf16 v[52:55], v[44:47], v[44:47], v[52:55]\n"
"v_cmp_eq_u32 vcc, 0, v63\n"
"v_mfma_f32_16x16x32_bf16 v[4:7], v[68:71], v[32:35], 0\n"
"v_mfma_f32_16x16x32_bf16 v[8:11], v[32:35], v[72:75], 0\n"
"v_mov_b32 v16, 9\n"
"v_mfma_f32_16x16x32_bf16 v[12:15], v[64:67], v[32:35], 0\n"
"s_cbranch_vccnz LQ_140\n"
"LP_141:\n"
"s_lshl_b64 exec, 1, s28\n"
"ds_write_b32 v3, v16 offset:0\n"
"s_mov_b64 exec, -1\n"
"v_mfma_f32_16x16x32_bf16 v[4:7], v[80:83], v[36:39], v[4:7]\n"
"ds_read_b32 v17, v3 offset:0\n"
"v_cndmask_b32_e64 v52, v52, v53, s[44:45]\n"
"v_cndmask_b32_e64 v54, v54, v55, s[44:45]\n"
"v_mfma_f32_16x16x32_bf16 v[8:11], v[36:39], v[84:87], v[8:11]\n"
"v_cndmask_b32_e64 v49, v52, v54, s[46:47]\n"
"v_or_b32 v48, 1, v48\n"
"v_xor_b32 v2, 0x1000, v60\n"
"v_mfma_f32_16x16x32_bf16 v[12:15], v[76:79], v[36:39], v[12:15]\n"
"s_waitcnt lgkmcnt(0)\n"
"v_cmp_gt_u32 vcc, 9, v17\n"
"s_nop 1\n"
"s_and_b32 s68, vcc_lo, 15\n"
"s_cbranch_scc1 Lf_142\n"
"Lg_143:\n"
"ds_read_b128 v[32:35], v2 offset:49152\n"
"ds_read_b128 v[36:39], v2 offset:50176\n"
"v_add_u32 v63, s53, v3\n"
"v_mfma_f32_16x16x32_bf16 v[4:7], v[92:95], v[40:43], v[4:7]\n"
"s_mov_b64 exec, s[48:49]\n"
"ds_write_b64 v63, v[48:49] offset:6656\n"
"s_mov_b64 exec, -1\n"
"v_mfma_f32_16x16x32_bf16 v[8:11], v[40:43], v[96:99], v[8:11]\n"
"v_mfma_f32_16x16x32_bf16 v[12:15], v[88:91], v[40:43], v[12:15]\n"
"ds_read_b128 v[40:43], v2 offset:51200\n"
"v_mfma_f32_16x16x32_bf16 v[4:7], v[104:107], v[44:47], v[4:7]\n"
"v_mfma_f32_16x16x32_bf16 v[8:11], v[44:47], v[108:111], v[8:11]\n"
"v_mfma_f32_16x16x32_bf16 v[12:15], v[100:103], v[44:47], v[12:15]\n"
"ds_read_b128 v[44:47], v2 offset:52224\n"
"s_add_u32 m0, s58, 32768\n"
"s_add_u32 s69, s51, 0xcc8000\n"
"buffer_load_dwordx4 v0, s[12:15], s69 offen offset:0 sc1 lds\n"
"buffer_load_dwordx4 v0, s[12:15], s69 offen offset:1024 sc1 lds\n"
"buffer_load_dwordx4 v0, s[12:15], s69 offen offset:2048 sc1 lds\n"
"buffer_load_dwordx4 v0, s[12:15], s69 offen offset:3072 sc1 lds\n"
"s_waitcnt lgkmcnt(4)\n"
"v_mfma_f32_16x16x32_bf16 v[4:7], v[116:119], v[32:35], v[4:7]\n"
"ds_read_b128 v[48:51], v3 offset:6656\n"
"ds_read_b128 v[52:55], v3 offset:6912\n"
"v_mfma_f32_16x16x32_bf16 v[8:11], v[32:35], v[120:123], v[8:11]\n"
"v_xor_b32 v2, 0x2000, v60\n"
"v_mfma_f32_16x16x32_bf16 v[12:15], v[112:115], v[32:35], v[12:15]\n"
"ds_read_b128 v[32:35], v2 offset:49152\n"
"s_waitcnt lgkmcnt(6)\n"
"v_mfma_f32_16x16x32_bf16 v[4:7], v[128:131], v[36:39], v[4:7]\n"
"v_mfma_f32_16x16x32_bf16 v[8:11], v[36:39], v[132:135], v[8:11]\n"
"s_waitcnt lgkmcnt(1)\n"
"v_mfma_f32_16x16x32_bf16 v[12:15], v[124:127], v[36:39], v[12:15]\n"
"ds_read_b128 v[36:39], v2 offset:50176\n"
"v_bitop3_b32 v63, 1, v48, v50 bitop3:0x80\n"
"v_bitop3_b32 v63, v63, v52, v54 bitop3:0x80\n"
"v_mfma_f32_16x16x32_bf16 v[4:7], v[140:143], v[40:43], v[4:7]\n"
"v_cmp_eq_u32 vcc, 0, v63\n"
"s_nop 1\n"
"s_cbranch_vccnz LQ_144\n"
"LP_145:\n"
"v_mfma_f32_16x16x32_bf16 v[8:11], v[40:43], v[144:147], v[8:11]\n"
"v_add_f32 v48, v48, v50\n"
"v_add_f32 v52, v52, v54\n"
"v_mfma_f32_16x16x32_bf16 v[12:15], v[136:139], v[40:43], v[12:15]\n"
"ds_read_b128 v[40:43], v2 offset:51200\n"
"v_mfma_f32_16x16x32_bf16 v[4:7], v[152:155], v[44:47], v[4:7]\n"
"v_add_f32 v49, v49, v51\n"
"v_add_f32 v53, v53, v55\n"
"v_mfma_f32_16x16x32_bf16 v[8:11], v[44:47], v[156:159], v[8:11]\n"
"v_add_f32 v52, v52, v48\n"
"v_add_f32 v53, v53, v49\n"
"v_mfma_f32_16x16x32_bf16 v[12:15], v[148:151], v[44:47], v[12:15]\n"
"ds_read_b128 v[44:47], v2 offset:52224\n"
"s_waitcnt lgkmcnt(3)\n"
"v_mfma_f32_16x16x32_bf16 v[4:7], v[164:167], v[32:35], v[4:7]\n"
"v_mul_f32 v48, s72, v52\n"
"v_mul_f32 v52, 0xba000000, v52\n"
"v_mfma_f32_16x16x32_bf16 v[8:11], v[32:35], v[168:171], v[8:11]\n"
"v_xor_b32 v2, 0x3000, v60\n"
"v_mul_f32 v49, v48, v48\n"
"v_mfma_f32_16x16x32_bf16 v[12:15], v[160:163], v[32:35], v[12:15]\n"
"ds_read_b128 v[32:35], v2 offset:49152\n"
"s_waitcnt lgkmcnt(3)\n"
"v_mfma_f32_16x16x32_bf16 v[4:7], v[176:179], v[36:39], v[4:7]\n"
"v_fma_f32 v54, -v53, s72, -v49\n"
"v_cvt_pk_bf16_f32 v53, v52, v52\n"
"v_mfma_f32_16x16x32_bf16 v[8:11], v[36:39], v[180:183], v[8:11]\n"
"v_max_f32 v54, 0, v54\n"
"v_and_b32 v53, 0xffff0000, v53\n"
"v_mfma_f32_16x16x32_bf16 v[12:15], v[172:175], v[36:39], v[12:15]\n"
"ds_read_b128 v[36:39], v2 offset:50176\n"
"s_waitcnt lgkmcnt(3)\n"
"v_mfma_f32_16x16x32_bf16 v[4:7], v[188:191], v[40:43], v[4:7]\n"
"v_add_f32 v54, 0x3727c5ac, v54\n"
"v_sub_f32 v55, v52, v53\n"
"v_mfma_f32_16x16x32_bf16 v[8:11], v[40:43], v[192:195], v[8:11]\n"
"v_rsq_f32 v1, v54\n"
"v_cvt_pk_bf16_f32 v53, v52, v55\n"
"v_mfma_f32_16x16x32_bf16 v[12:15], v[184:187], v[40:43], v[12:15]\n"
"ds_read_b128 v[40:43], v2 offset:51200\n"
"s_waitcnt lgkmcnt(3)\n"
"v_mfma_f32_16x16x32_bf16 v[4:7], v[200:203], v[44:47], v[4:7]\n"
"v_cvt_pk_bf16_f32 v52, v52, v55\n"
"v_mul_f32 v55, s73, v1\n"
"v_mfma_f32_16x16x32_bf16 v[8:11], v[44:47], v[204:207], v[8:11]\n"
"s_mov_b64 exec, 0xffff\n"
"ds_write2_b32 v3, v1, v55 offset0:194 offset1:195\n"
"s_mov_b64 exec, -1\n"
"v_add_u32 v63, s52, v3\n"
"v_mfma_f32_16x16x32_bf16 v[12:15], v[196:199], v[44:47], v[12:15]\n"
"ds_read_b128 v[44:47], v2 offset:52224\n"
"ds_read_b128 v[48:51], v63\n"
"ds_read_b64 v[54:55], v63 offset:256\n"
"s_waitcnt lgkmcnt(6)\n"
"v_mfma_f32_16x16x32_bf16 v[4:7], v[212:215], v[32:35], v[4:7]\n"
"v_mfma_f32_16x16x32_bf16 v[8:11], v[32:35], v[216:219], v[8:11]\n"
"v_mfma_f32_16x16x32_bf16 v[12:15], v[208:211], v[32:35], v[12:15]\n"
"s_waitcnt lgkmcnt(5)\n"
"v_mfma_f32_16x16x32_bf16 v[4:7], v[224:227], v[36:39], v[4:7]\n"
"ds_read2_b32 v[16:17], v61 offset0:194 offset1:198\n"
"ds_read2_b32 v[18:19], v61 offset0:202 offset1:206\n"
"v_mfma_f32_16x16x32_bf16 v[8:11], v[36:39], v[228:231], v[8:11]\n"
"v_mfma_f32_16x16x32_bf16 v[12:15], v[220:223], v[36:39], v[12:15]\n"
"s_waitcnt lgkmcnt(6)\n"
"v_mfma_f32_16x16x32_bf16 v[4:7], v[236:239], v[40:43], v[4:7]\n"
"v_mfma_f32_16x16x32_bf16 v[8:11], v[40:43], v[240:243], v[8:11]\n"
"v_mfma_f32_16x16x32_bf16 v[12:15], v[232:235], v[40:43], v[12:15]\n"
"ds_read_b32 v20, v3 offset:4\n"
"s_waitcnt lgkmcnt(5)\n"
"s_waitcnt lgkmcnt(3)\n"
"v_mfma_f32_16x16x16_bf16 v[4:7], v[50:51], v[52:53], v[4:7]\n"
"v_mfma_f32_16x16x16_bf16 v[8:11], v[52:53], v[54:55], v[8:11]\n"
"v_mfma_f32_16x16x16_bf16 v[12:15], v[48:49], v[52:53], v[12:15]\n"
"v_mfma_f32_16x16x32_bf16 v[4:7], v[248:251], v[44:47], v[4:7]\n"
"v_mfma_f32_16x16x32_bf16 v[8:11], v[44:47], v[252:255], v[8:11]\n"
"v_mfma_f32_16x16x32_bf16 v[12:15], v[244:247], v[44:47], v[12:15]\n"
"s_waitcnt vmcnt(0)\n"
"ds_read_b128 v[32:35], v60 offset:32768\n"
"ds_read_b128 v[36:39], v60 offset:33792\n"
"ds_read_b128 v[40:43], v60 offset:34816\n"
"ds_read_b128 v[44:47], v60 offset:35840\n"
"s_waitcnt lgkmcnt(4)\n"
"v_cmp_gt_u32 vcc, 16, v20\n"
"s_nop 1\n"
"s_and_b32 s68, vcc_lo, 15\n"
"s_cbranch_scc1 Lf_146\n"
"Lg_147:\n"
"s_waitcnt lgkmcnt(2)\n"
"v_mfma_f32_16x16x32_bf16 v[48:51], v[56:59], v[32:35], 0\n"
"v_bitop3_b32 v63, 1, v32, v34 bitop3:0x10\n"
"v_bitop3_b32 v63, v63, v36, v38 bitop3:0x10\n"
"v_mfma_f32_16x16x32_bf16 v[52:55], v[32:35], v[32:35], 0\n"
"v_mul_f32 v4, v4, v1\n"
"v_mul_f32 v5, v5, v1\n"
"v_mfma_f32_16x16x32_bf16 v[48:51], v[56:59], v[36:39], v[48:51]\n"
"v_mul_f32 v6, v6, v1\n"
"v_mul_f32 v7, v7, v1\n"
"v_mfma_f32_16x16x32_bf16 v[52:55], v[36:39], v[36:39], v[52:55]\n"
"v_cvt_pk_bf16_f32 v4, v4, v5\n"
"v_cvt_pk_bf16_f32 v5, v6, v7\n"
"s_waitcnt lgkmcnt(1)\n"
"v_mfma_f32_16x16x32_bf16 v[48:51], v[56:59], v[40:43], v[48:51]\n"
"v_mul_f32 v8, v8, v16\n"
"v_mul_f32 v9, v9, v17\n"
"v_mfma_f32_16x16x32_bf16 v[52:55], v[40:43], v[40:43], v[52:55]\n"
"v_mul_f32 v10, v10, v18\n"
"v_mul_f32 v11, v11, v19\n"
"s_waitcnt lgkmcnt(0)\n"
"v_mfma_f32_16x16x32_bf16 v[48:51], v[56:59], v[44:47], v[48:51]\n"
"v_cvt_pk_bf16_f32 v6, v8, v9\n"
"v_cvt_pk_bf16_f32 v7, v10, v11\n"
"v_mfma_f32_16x16x32_bf16 v[52:55], v[44:47], v[44:47], v[52:55]\n"
"ds_write_b128 v62, v[4:7] offset:0\n"
"v_cvt_pk_bf16_f32 v12, v12, v13\n"
"v_cvt_pk_bf16_f32 v13, v14, v15\n"
"v_mfma_f32_16x16x32_bf16 v[24:27], v[68:71], v[32:35], 0\n"
"ds_write_b64 v62, v[12:13] offset:8192\n"
"v_bitop3_b32 v63, v63, v40, v42 bitop3:0x10\n"
"v_bitop3_b32 v63, v63, v44, v46 bitop3:0x10\n"
"v_mfma_f32_16x16x32_bf16 v[20:23], v[32:35], v[72:75], 0\n"
"v_cmp_eq_u32 vcc, 0, v63\n"
"v_mfma_f32_16x16x32_bf16 v[28:31], v[64:67], v[32:35], 0\n"
"s_nop 0\n"
"s_cbranch_vccnz LQ_148\n"
"LP_149:\n"
"s_add_u32 m0, s58, 16384\n"
"s_add_u32 s69, s51, 0xcc4000\n"
"buffer_load_dwordx4 v0, s[12:15], s69 offen offset:0 sc1 lds\n"
"buffer_load_dwordx4 v0, s[12:15], s69 offen offset:1024 sc1 lds\n"
"buffer_load_dwordx4 v0, s[12:15], s69 offen offset:2048 sc1 lds\n"
"buffer_load_dwordx4 v0, s[12:15], s69 offen offset:3072 sc1 lds\n"
"v_mov_b32 v18, 10\n"
"v_mfma_f32_16x16x32_bf16 v[24:27], v[80:83], v[36:39], v[24:27]\n"
"s_lshl_b64 exec, 1, s28\n"
"ds_write_b32 v3, v18 offset:0\n"
"s_mov_b64 exec, -1\n"
"v_cndmask_b32_e64 v52, v52, v53, s[44:45]\n"
"v_cndmask_b32_e64 v54, v54, v55, s[44:45]\n"
"v_mfma_f32_16x16x32_bf16 v[20:23], v[36:39], v[84:87], v[20:23]\n"
"ds_read_b32 v17, v3 offset:0\n"
"v_cndmask_b32_e64 v49, v52, v54, s[46:47]\n"
"v_or_b32 v48, 1, v48\n"
"v_xor_b32 v2, 0x1000, v60\n"
"v_mfma_f32_16x16x32_bf16 v[28:31], v[76:79], v[36:39], v[28:31]\n"
"s_waitcnt lgkmcnt(0)\n"
"v_cmp_gt_u32 vcc, 10, v17\n"
"s_nop 1\n"
"s_and_b32 s68, vcc_lo, 15\n"
"s_cbranch_scc1 Lf_150\n"
"Lg_151:\n"
"ds_read_b128 v[32:35], v2 offset:32768\n"
"ds_read_b128 v[36:39], v2 offset:33792\n"
"v_add_u32 v63, s53, v3\n"
"v_mfma_f32_16x16x32_bf16 v[24:27], v[92:95], v[40:43], v[24:27]\n"
"s_mov_b64 exec, s[48:49]\n"
"ds_write_b64 v63, v[48:49] offset:6144\n"
"s_mov_b64 exec, -1\n"
"v_mfma_f32_16x16x32_bf16 v[20:23], v[40:43], v[96:99], v[20:23]\n"
"v_mfma_f32_16x16x32_bf16 v[28:31], v[88:91], v[40:43], v[28:31]\n"
"ds_read_b128 v[40:43], v2 offset:34816\n"
"v_mfma_f32_16x16x32_bf16 v[24:27], v[104:107], v[44:47], v[24:27]\n"
"v_mfma_f32_16x16x32_bf16 v[20:23], v[44:47], v[108:111], v[20:23]\n"
"v_mfma_f32_16x16x32_bf16 v[28:31], v[100:103], v[44:47], v[28:31]\n"
"ds_read_b128 v[44:47], v2 offset:35840\n"
"s_waitcnt lgkmcnt(4)\n"
"v_mfma_f32_16x16x32_bf16 v[24:27], v[116:119], v[32:35], v[24:27]\n"
"ds_read_b128 v[48:51], v3 offset:6144\n"
"ds_read_b128 v[52:55], v3 offset:6400\n"
"v_mfma_f32_16x16x32_bf16 v[20:23], v[32:35], v[120:123], v[20:23]\n"
"v_xor_b32 v2, 0x2000, v60\n"
"v_mfma_f32_16x16x32_bf16 v[28:31], v[112:115], v[32:35], v[28:31]\n"
"ds_read_b128 v[32:35], v2 offset:32768\n"
"s_waitcnt lgkmcnt(6)\n"
"v_mfma_f32_16x16x32_bf16 v[24:27], v[128:131], v[36:39], v[24:27]\n"
"v_mfma_f32_16x16x32_bf16 v[20:23], v[36:39], v[132:135], v[20:23]\n"
"s_waitcnt lgkmcnt(1)\n"
"v_mfma_f32_16x16x32_bf16 v[28:31], v[124:127], v[36:39], v[28:31]\n"
"ds_read_b128 v[36:39], v2 offset:33792\n"
"v_bitop3_b32 v63, 1, v48, v50 bitop3:0x80\n"
"v_bitop3_b32 v63, v63, v52, v54 bitop3:0x80\n"
"v_mfma_f32_16x16x32_bf16 v[24:27], v[140:143], v[40:43], v[24:27]\n"
"v_cmp_eq_u32 vcc, 0, v63\n"
"s_nop 1\n"
"s_cbranch_vccnz LQ_152\n"
"LP_153:\n"
"v_mfma_f32_16x16x32_bf16 v[20:23], v[40:43], v[144:147], v[20:23]\n"
"v_add_f32 v48, v48, v50\n"
"v_add_f32 v52, v52, v54\n"
"v_mfma_f32_16x16x32_bf16 v[28:31], v[136:139], v[40:43], v[28:31]\n"
"ds_read_b128 v[40:43], v2 offset:34816\n"
"v_mfma_f32_16x16x32_bf16 v[24:27], v[152:155], v[44:47], v[24:27]\n"
"v_add_f32 v49, v49, v51\n"
"v_add_f32 v53, v53, v55\n"
"v_mfma_f32_16x16x32_bf16 v[20:23], v[44:47], v[156:159], v[20:23]\n"
"v_add_f32 v52, v52, v48\n"
"v_add_f32 v53, v53, v49\n"
"v_mfma_f32_16x16x32_bf16 v[28:31], v[148:151], v[44:47], v[28:31]\n"
"ds_read_b128 v[44:47], v2 offset:35840\n"
"s_waitcnt lgkmcnt(3)\n"
"v_mfma_f32_16x16x32_bf16 v[24:27], v[164:167], v[32:35], v[24:27]\n"
"v_mul_f32 v48, s72, v52\n"
"v_mul_f32 v52, 0xba000000, v52\n"
"v_mfma_f32_16x16x32_bf16 v[20:23], v[32:35], v[168:171], v[20:23]\n"
"v_xor_b32 v2, 0x3000, v60\n"
"v_mul_f32 v49, v48, v48\n"
"v_mfma_f32_16x16x32_bf16 v[28:31], v[160:163], v[32:35], v[28:31]\n"
"ds_read_b128 v[32:35], v2 offset:32768\n"
"s_waitcnt lgkmcnt(3)\n"
"v_mfma_f32_16x16x32_bf16 v[24:27], v[176:179], v[36:39], v[24:27]\n"
"v_fma_f32 v54, -v53, s72, -v49\n"
"v_cvt_pk_bf16_f32 v53, v52, v52\n"
"v_mfma_f32_16x16x32_bf16 v[20:23], v[36:39], v[180:183], v[20:23]\n"
"v_max_f32 v54, 0, v54\n"
"v_and_b32 v53, 0xffff0000, v53\n"
"v_mfma_f32_16x16x32_bf16 v[28:31], v[172:175], v[36:39], v[28:31]\n"
"ds_read_b128 v[36:39], v2 offset:33792\n"
"s_waitcnt lgkmcnt(3)\n"
"v_mfma_f32_16x16x32_bf16 v[24:27], v[188:191], v[40:43], v[24:27]\n"
"v_add_f32 v54, 0x3727c5ac, v54\n"
"v_sub_f32 v55, v52, v53\n"
"v_mfma_f32_16x16x32_bf16 v[20:23], v[40:43], v[192:195], v[20:23]\n"
"v_rsq_f32 v1, v54\n"
"v_cvt_pk_bf16_f32 v53, v52, v55\n"
"v_mfma_f32_16x16x32_bf16 v[28:31], v[184:187], v[40:43], v[28:31]\n"
"ds_read_b128 v[40:43], v2 offset:34816\n"
"s_waitcnt lgkmcnt(3)\n"
"v_mfma_f32_16x16x32_bf16 v[24:27], v[200:203], v[44:47], v[24:27]\n"
"v_cvt_pk_bf16_f32 v52, v52, v55\n"
"v_mul_f32 v55, s73, v1\n"
"v_mfma_f32_16x16x32_bf16 v[20:23], v[44:47], v[204:207], v[20:23]\n"
"s_mov_b64 exec, 0xffff\n"
"ds_write2_b32 v3, v1, v55 offset0:130 offset1:131\n"
"s_mov_b64 exec, -1\n"
"v_add_u32 v63, s52, v3\n"
"v_mfma_f32_16x16x32_bf16 v[28:31], v[196:199], v[44:47], v[28:31]\n"
"ds_read_b128 v[44:47], v2 offset:35840\n"
"ds_read_b128 v[48:51], v63\n"
"ds_read_b64 v[54:55], v63 offset:256\n"
"s_waitcnt lgkmcnt(6)\n"
"v_mfma_f32_16x16x32_bf16 v[24:27], v[212:215], v[32:35], v[24:27]\n"
"v_mfma_f32_16x16x32_bf16 v[20:23], v[32:35], v[216:219], v[20:23]\n"
"v_mfma_f32_16x16x32_bf16 v[28:31], v[208:211], v[32:35], v[28:31]\n"
"s_waitcnt lgkmcnt(5)\n"
"v_mfma_f32_16x16x32_bf16 v[24:27], v[224:227], v[36:39], v[24:27]\n"
"ds_read2_b32 v[16:17], v61 offset0:130 offset1:134\n"
"ds_read2_b32 v[18:19], v61 offset0:138 offset1:142\n"
"v_mfma_f32_16x16x32_bf16 v[20:23], v[36:39], v[228:231], v[20:23]\n"
"v_mfma_f32_16x16x32_bf16 v[28:31], v[220:223], v[36:39], v[28:31]\n"
"s_waitcnt lgkmcnt(6)\n"
"v_mfma_f32_16x16x32_bf16 v[24:27], v[236:239], v[40:43], v[24:27]\n"
"v_mfma_f32_16x16x32_bf16 v[20:23], v[40:43], v[240:243], v[20:23]\n"
"v_mfma_f32_16x16x32_bf16 v[28:31], v[232:235], v[40:43], v[28:31]\n"
"s_waitcnt lgkmcnt(4)\n"
"s_waitcnt lgkmcnt(2)\n"
"v_mfma_f32_16x16x16_bf16 v[24:27], v[50:51], v[52:53], v[24:27]\n"
"v_mfma_f32_16x16x16_bf16 v[20:23], v[52:53], v[54:55], v[20:23]\n"
"v_mfma_f32_16x16x16_bf16 v[28:31], v[48:49], v[52:53], v[28:31]\n"
"v_mfma_f32_16x16x32_bf16 v[24:27], v[248:251], v[44:47], v[24:27]\n"
"v_mfma_f32_16x16x32_bf16 v[20:23], v[44:47], v[252:255], v[20:23]\n"
"v_mfma_f32_16x16x32_bf16 v[28:31], v[244:247], v[44:47], v[28:31]\n"
"s_waitcnt vmcnt(0)\n"
"ds_read_b128 v[32:35], v60 offset:16384\n"
"ds_read_b128 v[36:39], v60 offset:17408\n"
"ds_read_b128 v[40:43], v60 offset:18432\n"
"ds_read_b128 v[44:47], v60 offset:19456\n"
"s_waitcnt lgkmcnt(4)\n"
"s_waitcnt lgkmcnt(2)\n"
"v_mfma_f32_16x16x32_bf16 v[48:51], v[56:59], v[32:35], 0\n"
"v_bitop3_b32 v63, 1, v32, v34 bitop3:0x10\n"
"v_bitop3_b32 v63, v63, v36, v38 bitop3:0x10\n"
"v_mfma_f32_16x16x32_bf16 v[52:55], v[32:35], v[32:35], 0\n"
"v_mul_f32 v24, v24, v1\n"
"v_mul_f32 v25, v25, v1\n"
"v_mfma_f32_16x16x32_bf16 v[48:51], v[56:59], v[36:39], v[48:51]\n"
"v_mul_f32 v26, v26, v1\n"
"v_mul_f32 v27, v27, v1\n"
"v_mfma_f32_16x16x32_bf16 v[52:55], v[36:39], v[36:39], v[52:55]\n"
"v_mul_f32 v20, v20, v16\n"
"v_mul_f32 v21, v21, v17\n"
"s_waitcnt lgkmcnt(1)\n"
"v_mfma_f32_16x16x32_bf16 v[48:51], v[56:59], v[40:43], v[48:51]\n"
"v_mul_f32 v22, v22, v18\n"
"v_mul_f32 v23, v23, v19\n"
"v_mfma_f32_16x16x32_bf16 v[52:55], v[40:43], v[40:43], v[52:55]\n"
"v_cvt_pk_bf16_f32 v20, v20, v21\n"
"v_cvt_pk_bf16_f32 v21, v22, v23\n"
"s_waitcnt lgkmcnt(0)\n"
"v_mfma_f32_16x16x32_bf16 v[48:51], v[56:59], v[44:47], v[48:51]\n"
"v_cvt_pk_bf16_f32 v22, v24, v25\n"
"v_cvt_pk_bf16_f32 v23, v26, v27\n"
"v_mfma_f32_16x16x32_bf16 v[52:55], v[44:47], v[44:47], v[52:55]\n"
"ds_write_b128 v62, v[20:23] offset:4096\n"
"v_cvt_pk_bf16_f32 v28, v28, v29\n"
"v_cvt_pk_bf16_f32 v29, v30, v31\n"
"v_mfma_f32_16x16x32_bf16 v[4:7], v[68:71], v[32:35], 0\n"
"ds_write_b64 v62, v[28:29] offset:8200\n"
"v_bitop3_b32 v63, v63, v40, v42 bitop3:0x10\n"
"v_bitop3_b32 v63, v63, v44, v46 bitop3:0x10\n"
"v_mfma_f32_16x16x32_bf16 v[8:11], v[32:35], v[72:75], 0\n"
"v_cmp_eq_u32 vcc, 0, v63\n"
"v_mfma_f32_16x16x32_bf16 v[12:15], v[64:67], v[32:35], 0\n"
"s_nop 0\n"
"s_cbranch_vccnz LQ_154\n"
"LP_155:\n"
"v_mfma_f32_16x16x32_bf16 v[4:7], v[80:83], v[36:39], v[4:7]\n"
"v_cndmask_b32_e64 v52, v52, v53, s[44:45]\n"
"v_cndmask_b32_e64 v54, v54, v55, s[44:45]\n"
"v_mfma_f32_16x16x32_bf16 v[8:11], v[36:39], v[84:87], v[8:11]\n"
"v_cndmask_b32_e64 v49, v52, v54, s[46:47]\n"
"v_or_b32 v48, 1, v48\n"
"v_xor_b32 v2, 0x1000, v60\n"
"v_mfma_f32_16x16x32_bf16 v[12:15], v[76:79], v[36:39], v[12:15]\n"
"s_waitcnt lgkmcnt(0)\n"
"s_barrier\n"
"ds_read_b128 v[32:35], v2 offset:16384\n"
"ds_read_b128 v[36:39], v2 offset:17408\n"
"v_add_u32 v63, s53, v3\n"
"v_mfma_f32_16x16x32_bf16 v[4:7], v[92:95], v[40:43], v[4:7]\n"
"s_mov_b64 exec, s[48:49]\n"
"ds_write_b64 v63, v[48:49] offset:5632\n"
"s_mov_b64 exec, -1\n"
"v_mfma_f32_16x16x32_bf16 v[8:11], v[40:43], v[96:99], v[8:11]\n"
"v_mfma_f32_16x16x32_bf16 v[12:15], v[88:91], v[40:43], v[12:15]\n"
"ds_read_b128 v[40:43], v2 offset:18432\n"
"s_add_u32 m0, s58, 0\n"
"s_add_u32 s69, s51, 0xcc0000\n"
"buffer_load_dwordx4 v0, s[12:15], s69 offen offset:0 sc1 lds\n"
"buffer_load_dwordx4 v0, s[12:15], s69 offen offset:1024 sc1 lds\n"
"buffer_load_dwordx4 v0, s[12:15], s69 offen offset:2048 sc1 lds\n"
"buffer_load_dwordx4 v0, s[12:15], s69 offen offset:3072 sc1 lds\n"
"v_mfma_f32_16x16x32_bf16 v[4:7], v[104:107], v[44:47], v[4:7]\n"
"v_mfma_f32_16x16x32_bf16 v[8:11], v[44:47], v[108:111], v[8:11]\n"
"v_mfma_f32_16x16x32_bf16 v[12:15], v[100:103], v[44:47], v[12:15]\n"
"ds_read_b128 v[44:47], v2 offset:19456\n"
"s_waitcnt lgkmcnt(4)\n"
"v_mfma_f32_16x16x32_bf16 v[4:7], v[116:119], v[32:35], v[4:7]\n"
"ds_read_b128 v[48:51], v3 offset:5632\n"
"ds_read_b128 v[52:55], v3 offset:5888\n"
"v_mfma_f32_16x16x32_bf16 v[8:11], v[32:35], v[120:123], v[8:11]\n"
"v_xor_b32 v2, 0x2000, v60\n"
"v_mfma_f32_16x16x32_bf16 v[12:15], v[112:115], v[32:35], v[12:15]\n"
"ds_read_b128 v[32:35], v2 offset:16384\n"
"s_waitcnt lgkmcnt(6)\n"
"v_mfma_f32_16x16x32_bf16 v[4:7], v[128:131], v[36:39], v[4:7]\n"
"v_mfma_f32_16x16x32_bf16 v[8:11], v[36:39], v[132:135], v[8:11]\n"
"s_waitcnt lgkmcnt(1)\n"
"v_mfma_f32_16x16x32_bf16 v[12:15], v[124:127], v[36:39], v[12:15]\n"
"ds_read_b128 v[36:39], v2 offset:17408\n"
"v_bitop3_b32 v63, 1, v48, v50 bitop3:0x80\n"
"v_bitop3_b32 v63, v63, v52, v54 bitop3:0x80\n"
"v_mfma_f32_16x16x32_bf16 v[4:7], v[140:143], v[40:43], v[4:7]\n"
"v_cmp_eq_u32 vcc, 0, v63\n"
"s_nop 1\n"
"s_cbranch_vccnz LQ_156\n"
"LP_157:\n"
"v_mfma_f32_16x16x32_bf16 v[8:11], v[40:43], v[144:147], v[8:11]\n"
"v_add_f32 v48, v48, v50\n"
"v_add_f32 v52, v52, v54\n"
"v_mfma_f32_16x16x32_bf16 v[12:15], v[136:139], v[40:43], v[12:15]\n"
"ds_read_b128 v[40:43], v2 offset:18432\n"
"v_mfma_f32_16x16x32_bf16 v[4:7], v[152:155], v[44:47], v[4:7]\n"
"v_add_f32 v49, v49, v51\n"
"v_add_f32 v53, v53, v55\n"
"v_mfma_f32_16x16x32_bf16 v[8:11], v[44:47], v[156:159], v[8:11]\n"
"v_add_f32 v52, v52, v48\n"
"v_add_f32 v53, v53, v49\n"
"v_mfma_f32_16x16x32_bf16 v[12:15], v[148:151], v[44:47], v[12:15]\n"
"ds_read_b128 v[44:47], v2 offset:19456\n"
"s_waitcnt lgkmcnt(3)\n"
"v_mfma_f32_16x16x32_bf16 v[4:7], v[164:167], v[32:35], v[4:7]\n"
"v_mul_f32 v48, s72, v52\n"
"v_mul_f32 v52, 0xba000000, v52\n"
"v_mfma_f32_16x16x32_bf16 v[8:11], v[32:35], v[168:171], v[8:11]\n"
"v_xor_b32 v2, 0x3000, v60\n"
"v_mul_f32 v49, v48, v48\n"
"v_mfma_f32_16x16x32_bf16 v[12:15], v[160:163], v[32:35], v[12:15]\n"
"ds_read_b128 v[32:35], v2 offset:16384\n"
"s_waitcnt lgkmcnt(3)\n"
"v_mfma_f32_16x16x32_bf16 v[4:7], v[176:179], v[36:39], v[4:7]\n"
"v_fma_f32 v54, -v53, s72, -v49\n"
"v_cvt_pk_bf16_f32 v53, v52, v52\n"
"v_mfma_f32_16x16x32_bf16 v[8:11], v[36:39], v[180:183], v[8:11]\n"
"v_max_f32 v54, 0, v54\n"
"v_and_b32 v53, 0xffff0000, v53\n"
"v_mfma_f32_16x16x32_bf16 v[12:15], v[172:175], v[36:39], v[12:15]\n"
"ds_read_b128 v[36:39], v2 offset:17408\n"
"s_waitcnt lgkmcnt(3)\n"
"v_mfma_f32_16x16x32_bf16 v[4:7], v[188:191], v[40:43], v[4:7]\n"
"v_add_f32 v54, 0x3727c5ac, v54\n"
"v_sub_f32 v55, v52, v53\n"
"v_mfma_f32_16x16x32_bf16 v[8:11], v[40:43], v[192:195], v[8:11]\n"
"v_rsq_f32 v1, v54\n"
"v_cvt_pk_bf16_f32 v53, v52, v55\n"
"v_mfma_f32_16x16x32_bf16 v[12:15], v[184:187], v[40:43], v[12:15]\n"
"ds_read_b128 v[40:43], v2 offset:18432\n"
"s_waitcnt lgkmcnt(3)\n"
"v_mfma_f32_16x16x32_bf16 v[4:7], v[200:203], v[44:47], v[4:7]\n"
"v_cvt_pk_bf16_f32 v52, v52, v55\n"
"v_mul_f32 v55, s73, v1\n"
"v_mfma_f32_16x16x32_bf16 v[8:11], v[44:47], v[204:207], v[8:11]\n"
"s_mov_b64 exec, 0xffff\n"
"ds_write2_b32 v3, v1, v55 offset0:66 offset1:67\n"
"s_mov_b64 exec, -1\n"
"v_add_u32 v63, s52, v3\n"
"v_mfma_f32_16x16x32_bf16 v[12:15], v[196:199], v[44:47], v[12:15]\n"
"ds_read_b128 v[44:47], v2 offset:19456\n"
"ds_read_b128 v[48:51], v63\n"
"ds_read_b64 v[54:55], v63 offset:256\n"
"s_waitcnt lgkmcnt(6)\n"
"v_mfma_f32_16x16x32_bf16 v[4:7], v[212:215], v[32:35], v[4:7]\n"
"v_mfma_f32_16x16x32_bf16 v[8:11], v[32:35], v[216:219], v[8:11]\n"
"v_mfma_f32_16x16x32_bf16 v[12:15], v[208:211], v[32:35], v[12:15]\n"
"s_waitcnt lgkmcnt(5)\n"
"v_mfma_f32_16x16x32_bf16 v[4:7], v[224:227], v[36:39], v[4:7]\n"
"ds_read2_b32 v[16:17], v61 offset0:66 offset1:70\n"
"ds_read2_b32 v[18:19], v61 offset0:74 offset1:78\n"
"v_mfma_f32_16x16x32_bf16 v[8:11], v[36:39], v[228:231], v[8:11]\n"
"v_mfma_f32_16x16x32_bf16 v[12:15], v[220:223], v[36:39], v[12:15]\n"
"s_waitcnt lgkmcnt(6)\n"
"v_mfma_f32_16x16x32_bf16 v[4:7], v[236:239], v[40:43], v[4:7]\n"
"v_mfma_f32_16x16x32_bf16 v[8:11], v[40:43], v[240:243], v[8:11]\n"
"v_mfma_f32_16x16x32_bf16 v[12:15], v[232:235], v[40:43], v[12:15]\n"
"s_waitcnt lgkmcnt(4)\n"
"s_waitcnt lgkmcnt(2)\n"
"v_mfma_f32_16x16x16_bf16 v[4:7], v[50:51], v[52:53], v[4:7]\n"
"v_mfma_f32_16x16x16_bf16 v[8:11], v[52:53], v[54:55], v[8:11]\n"
"v_mfma_f32_16x16x16_bf16 v[12:15], v[48:49], v[52:53], v[12:15]\n"
"v_mfma_f32_16x16x32_bf16 v[4:7], v[248:251], v[44:47], v[4:7]\n"
"v_mfma_f32_16x16x32_bf16 v[8:11], v[44:47], v[252:255], v[8:11]\n"
"v_mfma_f32_16x16x32_bf16 v[12:15], v[244:247], v[44:47], v[12:15]\n"
"s_waitcnt vmcnt(0)\n"
"ds_read_b128 v[32:35], v60 offset:0\n"
"ds_read_b128 v[36:39], v60 offset:1024\n"
"ds_read_b128 v[40:43], v60 offset:2048\n"
"ds_read_b128 v[44:47], v60 offset:3072\n"
"s_waitcnt lgkmcnt(4)\n"
"s_waitcnt lgkmcnt(2)\n"
"v_mfma_f32_16x16x32_bf16 v[48:51], v[56:59], v[32:35], 0\n"
"v_bitop3_b32 v63, 1, v32, v34 bitop3:0x10\n"
"v_bitop3_b32 v63, v63, v36, v38 bitop3:0x10\n"
"v_mfma_f32_16x16x32_bf16 v[52:55], v[32:35], v[32:35], 0\n"
"v_mul_f32 v4, v4, v1\n"
"v_mul_f32 v5, v5, v1\n"
"v_mfma_f32_16x16x32_bf16 v[48:51], v[56:59], v[36:39], v[48:51]\n"
"v_mul_f32 v6, v6, v1\n"
"v_mul_f32 v7, v7, v1\n"
"v_mfma_f32_16x16x32_bf16 v[52:55], v[36:39], v[36:39], v[52:55]\n"
"v_cvt_pk_bf16_f32 v4, v4, v5\n"
"v_cvt_pk_bf16_f32 v5, v6, v7\n"
"s_waitcnt lgkmcnt(1)\n"
"v_mfma_f32_16x16x32_bf16 v[48:51], v[56:59], v[40:43], v[48:51]\n"
"v_mul_f32 v8, v8, v16\n"
"v_mul_f32 v9, v9, v17\n"
"v_mfma_f32_16x16x32_bf16 v[52:55], v[40:43], v[40:43], v[52:55]\n"
"v_mul_f32 v10, v10, v18\n"
"v_mul_f32 v11, v11, v19\n"
"s_waitcnt lgkmcnt(0)\n"
"v_mfma_f32_16x16x32_bf16 v[48:51], v[56:59], v[44:47], v[48:51]\n"
"v_cvt_pk_bf16_f32 v6, v8, v9\n"
"v_cvt_pk_bf16_f32 v7, v10, v11\n"
"v_mfma_f32_16x16x32_bf16 v[52:55], v[44:47], v[44:47], v[52:55]\n"
"ds_write_b128 v62, v[4:7] offset:0\n"
"v_cvt_pk_bf16_f32 v12, v12, v13\n"
"v_cvt_pk_bf16_f32 v13, v14, v15\n"
"v_mfma_f32_16x16x32_bf16 v[24:27], v[68:71], v[32:35], 0\n"
"ds_write_b64 v62, v[12:13] offset:8192\n"
"v_bitop3_b32 v63, v63, v40, v42 bitop3:0x10\n"
"v_bitop3_b32 v63, v63, v44, v46 bitop3:0x10\n"
"v_mfma_f32_16x16x32_bf16 v[20:23], v[32:35], v[72:75], 0\n"
"v_cmp_eq_u32 vcc, 0, v63\n"
"v_mfma_f32_16x16x32_bf16 v[28:31], v[64:67], v[32:35], 0\n"
"s_nop 0\n"
"s_cbranch_vccnz LQ_158\n"
"LP_159:\n"
"v_mfma_f32_16x16x32_bf16 v[24:27], v[80:83], v[36:39], v[24:27]\n"
"v_cndmask_b32_e64 v52, v52, v53, s[44:45]\n"
"v_cndmask_b32_e64 v54, v54, v55, s[44:45]\n"
"v_mfma_f32_16x16x32_bf16 v[20:23], v[36:39], v[84:87], v[20:23]\n"
"v_cndmask_b32_e64 v49, v52, v54, s[46:47]\n"
"v_or_b32 v48, 1, v48\n"
"v_xor_b32 v2, 0x1000, v60\n"
"v_mfma_f32_16x16x32_bf16 v[28:31], v[76:79], v[36:39], v[28:31]\n"
"s_waitcnt lgkmcnt(0)\n"
"s_barrier\n"
"ds_read_b128 v[32:35], v2 offset:0\n"
"ds_read_b128 v[36:39], v2 offset:1024\n"
"s_add_u32 s69, s51, 0xacc000\n"
"buffer_load_dwordx4 v[4:7], v0, s[12:15], s69 offen offset:0 sc1\n"
"buffer_load_dwordx4 v[8:11], v0, s[12:15], s69 offen offset:1024 sc1\n"
"buffer_load_dwordx4 v[12:15], v0, s[12:15], s69 offen offset:2048 sc1\n"
"buffer_load_dwordx4 v[16:19], v0, s[12:15], s69 offen offset:3072 sc1\n"
"v_add_u32 v63, s53, v3\n"
"v_mfma_f32_16x16x32_bf16 v[24:27], v[92:95], v[40:43], v[24:27]\n"
"s_mov_b64 exec, s[48:49]\n"
"ds_write_b64 v63, v[48:49] offset:5120\n"
"s_mov_b64 exec, -1\n"
"v_mfma_f32_16x16x32_bf16 v[20:23], v[40:43], v[96:99], v[20:23]\n"
"v_mfma_f32_16x16x32_bf16 v[28:31], v[88:91], v[40:43], v[28:31]\n"
"ds_read_b128 v[40:43], v2 offset:2048\n"
"v_mfma_f32_16x16x32_bf16 v[24:27], v[104:107], v[44:47], v[24:27]\n"
"v_mfma_f32_16x16x32_bf16 v[20:23], v[44:47], v[108:111], v[20:23]\n"
"v_mfma_f32_16x16x32_bf16 v[28:31], v[100:103], v[44:47], v[28:31]\n"
"ds_read_b128 v[44:47], v2 offset:3072\n"
"s_waitcnt lgkmcnt(4)\n"
"v_mfma_f32_16x16x32_bf16 v[24:27], v[116:119], v[32:35], v[24:27]\n"
"ds_read_b128 v[48:51], v3 offset:5120\n"
"ds_read_b128 v[52:55], v3 offset:5376\n"
"v_mfma_f32_16x16x32_bf16 v[20:23], v[32:35], v[120:123], v[20:23]\n"
"v_xor_b32 v2, 0x2000, v60\n"
"v_mfma_f32_16x16x32_bf16 v[28:31], v[112:115], v[32:35], v[28:31]\n"
"ds_read_b128 v[32:35], v2 offset:0\n"
"s_waitcnt lgkmcnt(6)\n"
"v_mfma_f32_16x16x32_bf16 v[24:27], v[128:131], v[36:39], v[24:27]\n"
"v_mfma_f32_16x16x32_bf16 v[20:23], v[36:39], v[132:135], v[20:23]\n"
"s_waitcnt lgkmcnt(1)\n"
"v_mfma_f32_16x16x32_bf16 v[28:31], v[124:127], v[36:39], v[28:31]\n"
"ds_read_b128 v[36:39], v2 offset:1024\n"
"v_bitop3_b32 v63, 1, v48, v50 bitop3:0x80\n"
"v_bitop3_b32 v63, v63, v52, v54 bitop3:0x80\n"
"v_mfma_f32_16x16x32_bf16 v[24:27], v[140:143], v[40:43], v[24:27]\n"
"v_cmp_eq_u32 vcc, 0, v63\n"
"s_nop 1\n"
"s_cbranch_vccnz LQ_160\n"
"LP_161:\n"
"v_mfma_f32_16x16x32_bf16 v[20:23], v[40:43], v[144:147], v[20:23]\n"
"v_add_f32 v48, v48, v50\n"
"v_add_f32 v52, v52, v54\n"
"v_mfma_f32_16x16x32_bf16 v[28:31], v[136:139], v[40:43], v[28:31]\n"
"ds_read_b128 v[40:43], v2 offset:2048\n"
"v_mfma_f32_16x16x32_bf16 v[24:27], v[152:155], v[44:47], v[24:27]\n"
"v_add_f32 v49, v49, v51\n"
"v_add_f32 v53, v53, v55\n"
"v_mfma_f32_16x16x32_bf16 v[20:23], v[44:47], v[156:159], v[20:23]\n"
"v_add_f32 v52, v52, v48\n"
"v_add_f32 v53, v53, v49\n"
"v_mfma_f32_16x16x32_bf16 v[28:31], v[148:151], v[44:47], v[28:31]\n"
"ds_read_b128 v[44:47], v2 offset:3072\n"
"s_waitcnt lgkmcnt(3)\n"
"v_mfma_f32_16x16x32_bf16 v[24:27], v[164:167], v[32:35], v[24:27]\n"
"v_mul_f32 v48, s72, v52\n"
"v_mul_f32 v52, 0xba000000, v52\n"
"v_mfma_f32_16x16x32_bf16 v[20:23], v[32:35], v[168:171], v[20:23]\n"
"v_xor_b32 v2, 0x3000, v60\n"
"v_mul_f32 v49, v48, v48\n"
"v_mfma_f32_16x16x32_bf16 v[28:31], v[160:163], v[32:35], v[28:31]\n"
"ds_read_b128 v[32:35], v2 offset:0\n"
"s_waitcnt lgkmcnt(3)\n"
"v_mfma_f32_16x16x32_bf16 v[24:27], v[176:179], v[36:39], v[24:27]\n"
"v_fma_f32 v54, -v53, s72, -v49\n"
"v_cvt_pk_bf16_f32 v53, v52, v52\n"
"v_mfma_f32_16x16x32_bf16 v[20:23], v[36:39], v[180:183], v[20:23]\n"
"v_max_f32 v54, 0, v54\n"
"v_and_b32 v53, 0xffff0000, v53\n"
"v_mfma_f32_16x16x32_bf16 v[28:31], v[172:175], v[36:39], v[28:31]\n"
"ds_read_b128 v[36:39], v2 offset:1024\n"
"s_waitcnt lgkmcnt(3)\n"
"v_mfma_f32_16x16x32_bf16 v[24:27], v[188:191], v[40:43], v[24:27]\n"
"v_add_f32 v54, 0x3727c5ac, v54\n"
"v_sub_f32 v55, v52, v53\n"
"v_mfma_f32_16x16x32_bf16 v[20:23], v[40:43], v[192:195], v[20:23]\n"
"v_rsq_f32 v1, v54\n"
"v_cvt_pk_bf16_f32 v53, v52, v55\n"
"v_mfma_f32_16x16x32_bf16 v[28:31], v[184:187], v[40:43], v[28:31]\n"
"ds_read_b128 v[40:43], v2 offset:2048\n"
"s_waitcnt lgkmcnt(3)\n"
"v_mfma_f32_16x16x32_bf16 v[24:27], v[200:203], v[44:47], v[24:27]\n"
"v_cvt_pk_bf16_f32 v52, v52, v55\n"
"v_mul_f32 v55, s73, v1\n"
"v_mfma_f32_16x16x32_bf16 v[20:23], v[44:47], v[204:207], v[20:23]\n"
"s_mov_b64 exec, 0xffff\n"
"ds_write2_b32 v3, v1, v55 offset0:2 offset1:3\n"
"s_mov_b64 exec, -1\n"
"v_add_u32 v63, s52, v3\n"
"v_mfma_f32_16x16x32_bf16 v[28:31], v[196:199], v[44:47], v[28:31]\n"
"ds_read_b128 v[44:47], v2 offset:3072\n"
"ds_read_b128 v[48:51], v63\n"
"ds_read_b64 v[54:55], v63 offset:256\n"
"s_waitcnt lgkmcnt(6)\n"
"v_mfma_f32_16x16x32_bf16 v[24:27], v[212:215], v[32:35], v[24:27]\n"
"v_mfma_f32_16x16x32_bf16 v[20:23], v[32:35], v[216:219], v[20:23]\n"
"v_mfma_f32_16x16x32_bf16 v[28:31], v[208:211], v[32:35], v[28:31]\n"
"s_waitcnt lgkmcnt(5)\n"
"v_mfma_f32_16x16x32_bf16 v[24:27], v[224:227], v[36:39], v[24:27]\n"
"ds_read2_b32 v[32:33], v61 offset0:2 offset1:6\n"
"ds_read2_b32 v[34:35], v61 offset0:10 offset1:14\n"
"v_mfma_f32_16x16x32_bf16 v[20:23], v[36:39], v[228:231], v[20:23]\n"
"v_add_u32 v63, 153600, v0\n"
"ds_read_b32 v61, v63 offset:4\n"
"v_mfma_f32_16x16x32_bf16 v[28:31], v[220:223], v[36:39], v[28:31]\n"
"s_waitcnt lgkmcnt(7)\n"
"v_mfma_f32_16x16x32_bf16 v[24:27], v[236:239], v[40:43], v[24:27]\n"
"s_xor_b32 s68, s28, 1\n"
"s_lshl_b32 s68, s68, 5\n"
"v_mfma_f32_16x16x32_bf16 v[20:23], v[40:43], v[240:243], v[20:23]\n"
"s_waitcnt lgkmcnt(0)\n"
"v_xor_b32 v61, s68, v61\n"
"v_add_u32 v61, s57, v61\n"
"v_mfma_f32_16x16x32_bf16 v[28:31], v[232:235], v[40:43], v[28:31]\n"
"v_mfma_f32_16x16x16_bf16 v[24:27], v[50:51], v[52:53], v[24:27]\n"
"v_mfma_f32_16x16x16_bf16 v[20:23], v[52:53], v[54:55], v[20:23]\n"
"v_mfma_f32_16x16x16_bf16 v[28:31], v[48:49], v[52:53], v[28:31]\n"
"v_mfma_f32_16x16x32_bf16 v[24:27], v[248:251], v[44:47], v[24:27]\n"
"ds_read_b64_tr_b16 v[36:37], v61 offset:4096\n"
"ds_read_b64_tr_b16 v[38:39], v61 offset:4608\n"
"ds_read_b64_tr_b16 v[40:41], v61 offset:8192\n"
"ds_read_b64_tr_b16 v[42:43], v61 offset:8704\n"
"v_mfma_f32_16x16x32_bf16 v[20:23], v[44:47], v[252:255], v[20:23]\n"
"v_mfma_f32_16x16x32_bf16 v[28:31], v[244:247], v[44:47], v[28:31]\n"
"v_add_u32 v63, 152576, v0\n"
"ds_read_b64 v[50:51], v62 offset:16392\n"
"ds_read_b64 v[48:49], v63 offset:8\n"
"ds_read_b64_tr_b16 v[44:45], v61 offset:12288\n"
"ds_read_b64_tr_b16 v[46:47], v61 offset:12800\n"
"v_add_u32 v63, 153600, v0\n"
"v_mul_f32 v24, v24, v1\n"
"v_mul_f32 v25, v25, v1\n"
"v_mul_f32 v26, v26, v1\n"
"v_mul_f32 v27, v27, v1\n"
"v_mul_f32 v20, v20, v32\n"
"v_mul_f32 v21, v21, v33\n"
"v_mul_f32 v22, v22, v34\n"
"v_mul_f32 v23, v23, v35\n"
"v_cvt_pk_bf16_f32 v20, v20, v21\n"
"v_cvt_pk_bf16_f32 v21, v22, v23\n"
"v_cvt_pk_bf16_f32 v22, v24, v25\n"
"v_cvt_pk_bf16_f32 v23, v26, v27\n"
"ds_write_b128 v62, v[20:23] offset:4096\n"
"v_cvt_pk_bf16_f32 v28, v28, v29\n"
"v_cvt_pk_bf16_f32 v29, v30, v31\n"
"ds_write_b64 v62, v[28:29] offset:8200\n"
"ds_read_b64_tr_b16 v[32:33], v61 offset:0\n"
"ds_read_b64_tr_b16 v[34:35], v61 offset:512\n"
"ds_read_b32 v1, v63\n"
"s_waitcnt lgkmcnt(3)\n"
"s_barrier\n"
"s_setprio 3\n"
"v_mfma_f32_16x16x16_bf16 v[28:31], v[48:49], v[50:51], 0\n"
"v_xor_b32 v61, 96, v61\n"
"ds_read_b64_tr_b16 v[48:49], v61 offset:0\n"
"ds_read_b64_tr_b16 v[50:51], v61 offset:512\n"
"s_waitcnt vmcnt(0)\n"
"LB_162:\n"
"s_waitcnt lgkmcnt(3)\n"
"v_mfma_f32_16x16x32_bf16 v[20:23], v[32:35], v[4:7], 0\n"
"ds_read_b64_tr_b16 v[32:33], v61 offset:4096\n"
"ds_read_b64_tr_b16 v[34:35], v61 offset:4608\n"
"v_bitop3_b32 v63, 1, v4, v6 bitop3:0x80\n"
"v_bitop3_b32 v63, v63, v8, v10 bitop3:0x80\n"
"v_mfma_f32_16x16x32_bf16 v[20:23], v[36:39], v[8:11], v[20:23]\n"
"ds_read_b64_tr_b16 v[36:37], v61 offset:8192\n"
"ds_read_b64_tr_b16 v[38:39], v61 offset:8704\n"
"v_bitop3_b32 v63, v63, v12, v14 bitop3:0x80\n"
"v_bitop3_b32 v63, v63, v16, v18 bitop3:0x80\n"
"v_mfma_f32_16x16x32_bf16 v[20:23], v[40:43], v[12:15], v[20:23]\n"
"ds_read_b64_tr_b16 v[40:41], v61 offset:12288\n"
"ds_read_b64_tr_b16 v[42:43], v61 offset:12800\n"
"v_cmp_eq_u32 vcc, 0, v63\n"
"v_mfma_f32_16x16x32_bf16 v[20:23], v[44:47], v[16:19], v[20:23]\n"
"v_xor_b32 v61, 32, v61\n"
"ds_read_b64_tr_b16 v[44:45], v61 offset:0\n"
"ds_read_b64_tr_b16 v[46:47], v61 offset:512\n"
"s_cbranch_vccnz LC_163\n"
"s_waitcnt lgkmcnt(8)\n"
"v_mfma_f32_16x16x32_bf16 v[24:27], v[48:51], v[4:7], 0\n"
"ds_read_b64_tr_b16 v[48:49], v61 offset:4096\n"
"ds_read_b64_tr_b16 v[50:51], v61 offset:4608\n"
"s_waitcnt lgkmcnt(8)\n"
"v_mfma_f32_16x16x32_bf16 v[24:27], v[32:35], v[8:11], v[24:27]\n"
"ds_read_b64_tr_b16 v[32:33], v61 offset:8192\n"
"ds_read_b64_tr_b16 v[34:35], v61 offset:8704\n"
"s_waitcnt lgkmcnt(8)\n"
"v_mfma_f32_16x16x32_bf16 v[24:27], v[36:39], v[12:15], v[24:27]\n"
"ds_read_b64_tr_b16 v[36:37], v61 offset:12288\n"
"ds_read_b64_tr_b16 v[38:39], v61 offset:12800\n"
"ds_write_b128 v60, v[20:23] offset:32768\n"
"s_waitcnt lgkmcnt(9)\n"
"v_mfma_f32_16x16x32_bf16 v[24:27], v[40:43], v[16:19], v[24:27]\n"
"v_xor_b32 v61, 96, v61\n"
"ds_read_b64_tr_b16 v[40:41], v61 offset:0\n"
"ds_read_b64_tr_b16 v[42:43], v61 offset:512\n"
"s_waitcnt lgkmcnt(9)\n"
"v_mfma_f32_16x16x32_bf16 v[20:23], v[44:47], v[4:7], 0\n"
"ds_read_b64_tr_b16 v[44:45], v61 offset:4096\n"
"ds_read_b64_tr_b16 v[46:47], v61 offset:4608\n"
"s_waitcnt lgkmcnt(9)\n"
"v_mfma_f32_16x16x32_bf16 v[20:23], v[48:51], v[8:11], v[20:23]\n"
"ds_read_b64_tr_b16 v[48:49], v61 offset:8192\n"
"ds_read_b64_tr_b16 v[50:51], v61 offset:8704\n"
"s_add_u32 m0, s58, 16384\n"
"s_add_u32 s69, s51, 0xac8000\n"
"buffer_load_dwordx4 v0, s[12:15], s69 offen offset:0 sc1 lds\n"
"buffer_load_dwordx4 v0, s[12:15], s69 offen offset:1024 sc1 lds\n"
"buffer_load_dwordx4 v0, s[12:15], s69 offen offset:2048 sc1 lds\n"
"buffer_load_dwordx4 v0, s[12:15], s69 offen offset:3072 sc1 lds\n"
"s_waitcnt lgkmcnt(9)\n"
"v_mfma_f32_16x16x32_bf16 v[20:23], v[32:35], v[12:15], v[20:23]\n"
"ds_read_b64_tr_b16 v[32:33], v61 offset:12288\n"
"ds_read_b64_tr_b16 v[34:35], v61 offset:12800\n"
"v_xor_b32 v61, 32, v61\n"
"ds_write_b128 v60, v[24:27] offset:33792\n"
"s_waitcnt lgkmcnt(10)\n"
"v_mfma_f32_16x16x32_bf16 v[20:23], v[36:39], v[16:19], v[20:23]\n"
"s_waitcnt lgkmcnt(7)\n"
"v_mfma_f32_16x16x32_bf16 v[28:31], v[40:43], v[4:7], v[28:31]\n"
"s_waitcnt lgkmcnt(5)\n"
"v_mfma_f32_16x16x32_bf16 v[28:31], v[44:47], v[8:11], v[28:31]\n"
"s_waitcnt lgkmcnt(3)\n"
"v_mfma_f32_16x16x32_bf16 v[28:31], v[48:51], v[12:15], v[28:31]\n"
"s_nop 1\n"
"ds_write_b128 v60, v[20:23] offset:34816\n"
"s_waitcnt lgkmcnt(2)\n"
"v_mfma_f32_16x16x32_bf16 v[28:31], v[32:35], v[16:19], v[28:31]\n"
"s_waitcnt lgkmcnt(0)\n"
"s_barrier\n"
"v_xor_b32 v47, 0x3000, v60\n"
"v_xor_b32 v45, 0x1000, v60\n"
"v_xor_b32 v46, 0x2000, v60\n"
"ds_read_b128 v[40:43], v47 offset:34816\n"
"ds_read_b128 v[32:35], v45 offset:32768\n"
"ds_read_b128 v[36:39], v46 offset:33792\n"
"s_add_u32 s69, s55, 0x18000\n"
"v_bfe_u32 v48, v0, 4, 4\n"
"v_lshrrev_b32 v49, 8, v0\n"
"v_lshlrev_b32 v49, 3, v49\n"
"v_lshl_add_u32 v48, v48, 11, v49\n"
"s_waitcnt lgkmcnt(2)\n"
"v_add_f32 v28, v28, v40\n"
"v_add_f32 v29, v29, v41\n"
"v_add_f32 v30, v30, v42\n"
"v_add_f32 v31, v31, v43\n"
"s_waitcnt lgkmcnt(0)\n"
"v_add_f32 v32, v32, v36\n"
"v_add_f32 v33, v33, v37\n"
"v_add_f32 v34, v34, v38\n"
"v_add_f32 v35, v35, v39\n"
"v_add_f32 v28, v28, v32\n"
"v_add_f32 v29, v29, v33\n"
"v_add_f32 v30, v30, v34\n"
"v_add_f32 v31, v31, v35\n"
"v_cvt_pk_bf16_f32 v44, v28, v29\n"
"v_cvt_pk_bf16_f32 v45, v30, v31\n"
"buffer_store_dwordx2 v[44:45], v48, s[16:19], s69 offen\n"
"v_add_u32 v63, 152576, v0\n"
"ds_read_b64 v[50:51], v62 offset:16384\n"
"ds_read_b64 v[48:49], v63 offset:8\n"
"ds_read_b64_tr_b16 v[32:33], v61 offset:0\n"
"ds_read_b64_tr_b16 v[34:35], v61 offset:512\n"
"ds_read_b64_tr_b16 v[36:37], v61 offset:4096\n"
"ds_read_b64_tr_b16 v[38:39], v61 offset:4608\n"
"ds_read_b64_tr_b16 v[40:41], v61 offset:8192\n"
"ds_read_b64_tr_b16 v[42:43], v61 offset:8704\n"
"ds_read_b64_tr_b16 v[44:45], v61 offset:12288\n"
"ds_read_b64_tr_b16 v[46:47], v61 offset:12800\n"
"s_waitcnt lgkmcnt(8)\n"
"v_mfma_f32_16x16x16_bf16 v[28:31], v[48:49], v[50:51], 0\n"
"v_xor_b32 v61, 96, v61\n"
"ds_read_b64_tr_b16 v[48:49], v61 offset:0\n"
"ds_read_b64_tr_b16 v[50:51], v61 offset:512\n"
"s_waitcnt vmcnt(1)\n"
"ds_read_b128 v[4:7], v60 offset:16384\n"
"ds_read_b128 v[8:11], v60 offset:17408\n"
"ds_read_b128 v[12:15], v60 offset:18432\n"
"ds_read_b128 v[16:19], v60 offset:19456\n"
"LB_168:\n"
"s_waitcnt lgkmcnt(3)\n"
"v_mfma_f32_16x16x32_bf16 v[20:23], v[32:35], v[4:7], 0\n"
"ds_read_b64_tr_b16 v[32:33], v61 offset:4096\n"
"ds_read_b64_tr_b16 v[34:35], v61 offset:4608\n"
"v_bitop3_b32 v63, 1, v4, v6 bitop3:0x80\n"
"s_waitcnt lgkmcnt(4)\n"
"v_mfma_f32_16x16x32_bf16 v[20:23], v[36:39], v[8:11], v[20:23]\n"
"ds_read_b64_tr_b16 v[36:37], v61 offset:8192\n"
"ds_read_b64_tr_b16 v[38:39], v61 offset:8704\n"
"v_bitop3_b32 v63, v63, v8, v10 bitop3:0x80\n"
"s_waitcnt lgkmcnt(5)\n"
"v_mfma_f32_16x16x32_bf16 v[20:23], v[40:43], v[12:15], v[20:23]\n"
"ds_read_b64_tr_b16 v[40:41], v61 offset:12288\n"
"ds_read_b64_tr_b16 v[42:43], v61 offset:12800\n"
"v_bitop3_b32 v63, v63, v12, v14 bitop3:0x80\n"
"s_waitcnt lgkmcnt(6)\n"
"v_mfma_f32_16x16x32_bf16 v[20:23], v[44:47], v[16:19], v[20:23]\n"
"v_xor_b32 v61, 32, v61\n"
"ds_read_b64_tr_b16 v[44:45], v61 offset:0\n"
"ds_read_b64_tr_b16 v[46:47], v61 offset:512\n"
"v_bitop3_b32 v63, v63, v16, v18 bitop3:0x80\n"
"v_cmp_eq_u32 vcc, 0, v63\n"
"v_mfma_f32_16x16x32_bf16 v[24:27], v[48:51], v[4:7], 0\n"
"ds_read_b64_tr_b16 v[48:49], v61 offset:4096\n"
"ds_read_b64_tr_b16 v[50:51], v61 offset:4608\n"
"s_cbranch_vccnz LC_169\n"
"s_waitcnt lgkmcnt(8)\n"
"v_mfma_f32_16x16x32_bf16 v[24:27], v[32:35], v[8:11], v[24:27]\n"
"ds_read_b64_tr_b16 v[32:33], v61 offset:8192\n"
"ds_read_b64_tr_b16 v[34:35], v61 offset:8704\n"
"s_waitcnt lgkmcnt(8)\n"
"v_mfma_f32_16x16x32_bf16 v[24:27], v[36:39], v[12:15], v[24:27]\n"
"ds_read_b64_tr_b16 v[36:37], v61 offset:12288\n"
"ds_read_b64_tr_b16 v[38:39], v61 offset:12800\n"
"ds_write_b128 v60, v[20:23] offset:16384\n"
"s_waitcnt lgkmcnt(9)\n"
"v_mfma_f32_16x16x32_bf16 v[24:27], v[40:43], v[16:19], v[24:27]\n"
"v_xor_b32 v61, 96, v61\n"
"ds_read_b64_tr_b16 v[40:41], v61 offset:0\n"
"ds_read_b64_tr_b16 v[42:43], v61 offset:512\n"
"s_waitcnt lgkmcnt(9)\n"
"v_mfma_f32_16x16x32_bf16 v[20:23], v[44:47], v[4:7], 0\n"
"ds_read_b64_tr_b16 v[44:45], v61 offset:4096\n"
"ds_read_b64_tr_b16 v[46:47], v61 offset:4608\n"
"s_waitcnt lgkmcnt(9)\n"
"v_mfma_f32_16x16x32_bf16 v[20:23], v[48:51], v[8:11], v[20:23]\n"
"ds_read_b64_tr_b16 v[48:49], v61 offset:8192\n"
"ds_read_b64_tr_b16 v[50:51], v61 offset:8704\n"
"s_waitcnt lgkmcnt(9)\n"
"v_mfma_f32_16x16x32_bf16 v[20:23], v[32:35], v[12:15], v[20:23]\n"
"ds_read_b64_tr_b16 v[32:33], v61 offset:12288\n"
"ds_read_b64_tr_b16 v[34:35], v61 offset:12800\n"
"v_xor_b32 v61, 32, v61\n"
"ds_write_b128 v60, v[24:27] offset:17408\n"
"s_add_u32 m0, s58, 49152\n"
"s_add_u32 s69, s51, 0xac4000\n"
"buffer_load_dwordx4 v0, s[12:15], s69 offen offset:0 sc1 lds\n"
"buffer_load_dwordx4 v0, s[12:15], s69 offen offset:1024 sc1 lds\n"
"buffer_load_dwordx4 v0, s[12:15], s69 offen offset:2048 sc1 lds\n"
"buffer_load_dwordx4 v0, s[12:15], s69 offen offset:3072 sc1 lds\n"
"s_waitcnt lgkmcnt(10)\n"
"v_mfma_f32_16x16x32_bf16 v[20:23], v[36:39], v[16:19], v[20:23]\n"
"s_waitcnt lgkmcnt(7)\n"
"v_mfma_f32_16x16x32_bf16 v[28:31], v[40:43], v[4:7], v[28:31]\n"
"s_waitcnt lgkmcnt(5)\n"
"v_mfma_f32_16x16x32_bf16 v[28:31], v[44:47], v[8:11], v[28:31]\n"
"s_waitcnt lgkmcnt(3)\n"
"v_mfma_f32_16x16x32_bf16 v[28:31], v[48:51], v[12:15], v[28:31]\n"
"s_nop 1\n"
"ds_write_b128 v60, v[20:23] offset:18432\n"
"s_waitcnt lgkmcnt(2)\n"
"v_mfma_f32_16x16x32_bf16 v[28:31], v[32:35], v[16:19], v[28:31]\n"
"s_waitcnt lgkmcnt(0)\n"
"s_barrier\n"
"v_xor_b32 v47, 0x3000, v60\n"
"v_xor_b32 v45, 0x1000, v60\n"
"v_xor_b32 v46, 0x2000, v60\n"
"ds_read_b128 v[40:43], v47 offset:18432\n"
"ds_read_b128 v[32:35], v45 offset:16384\n"
"ds_read_b128 v[36:39], v46 offset:17408\n"
"s_add_u32 s69, s55, 0x10000\n"
"v_bfe_u32 v48, v0, 4, 4\n"
"v_lshrrev_b32 v49, 8, v0\n"
"v_lshlrev_b32 v49, 3, v49\n"
"v_lshl_add_u32 v48, v48, 11, v49\n"
"s_waitcnt lgkmcnt(2)\n"
"v_add_f32 v28, v28, v40\n"
"v_add_f32 v29, v29, v41\n"
"v_add_f32 v30, v30, v42\n"
"v_add_f32 v31, v31, v43\n"
"s_waitcnt lgkmcnt(0)\n"
"v_add_f32 v32, v32, v36\n"
"v_add_f32 v33, v33, v37\n"
"v_add_f32 v34, v34, v38\n"
"v_add_f32 v35, v35, v39\n"
"v_add_f32 v28, v28, v32\n"
"v_add_f32 v29, v29, v33\n"
"v_add_f32 v30, v30, v34\n"
"v_add_f32 v31, v31, v35\n"
"v_cvt_pk_bf16_f32 v44, v28, v29\n"
"v_cvt_pk_bf16_f32 v45, v30, v31\n"
"buffer_store_dwordx2 v[44:45], v48, s[16:19], s69 offen\n"
"v_add_u32 v63, 152576, v0\n"
"ds_read_b64 v[50:51], v62 offset:12296\n"
"ds_read_b64 v[48:49], v63 offset:8\n"
"ds_read_b64_tr_b16 v[32:33], v61 offset:0\n"
"ds_read_b64_tr_b16 v[34:35], v61 offset:512\n"
"ds_read_b64_tr_b16 v[36:37], v61 offset:4096\n"
"ds_read_b64_tr_b16 v[38:39], v61 offset:4608\n"
"ds_read_b64_tr_b16 v[40:41], v61 offset:8192\n"
"ds_read_b64_tr_b16 v[42:43], v61 offset:8704\n"
"ds_read_b64_tr_b16 v[44:45], v61 offset:12288\n"
"ds_read_b64_tr_b16 v[46:47], v61 offset:12800\n"
"s_waitcnt lgkmcnt(8)\n"
"v_mfma_f32_16x16x16_bf16 v[28:31], v[48:49], v[50:51], 0\n"
"v_xor_b32 v61, 96, v61\n"
"ds_read_b64_tr_b16 v[48:49], v61 offset:0\n"
"ds_read_b64_tr_b16 v[50:51], v61 offset:512\n"
"s_waitcnt vmcnt(1)\n"
"ds_read_b128 v[4:7], v60 offset:49152\n"
"ds_read_b128 v[8:11], v60 offset:50176\n"
"ds_read_b128 v[12:15], v60 offset:51200\n"
"ds_read_b128 v[16:19], v60 offset:52224\n"
"LB_170:\n"
"s_waitcnt lgkmcnt(3)\n"
"v_mfma_f32_16x16x32_bf16 v[20:23], v[32:35], v[4:7], 0\n"
"ds_read_b64_tr_b16 v[32:33], v61 offset:4096\n"
"ds_read_b64_tr_b16 v[34:35], v61 offset:4608\n"
"v_bitop3_b32 v63, 1, v4, v6 bitop3:0x80\n"
"s_waitcnt lgkmcnt(4)\n"
"v_mfma_f32_16x16x32_bf16 v[20:23], v[36:39], v[8:11], v[20:23]\n"
"ds_read_b64_tr_b16 v[36:37], v61 offset:8192\n"
"ds_read_b64_tr_b16 v[38:39], v61 offset:8704\n"
"v_bitop3_b32 v63, v63, v8, v10 bitop3:0x80\n"
"s_waitcnt lgkmcnt(5)\n"
"v_mfma_f32_16x16x32_bf16 v[20:23], v[40:43], v[12:15], v[20:23]\n"
"ds_read_b64_tr_b16 v[40:41], v61 offset:12288\n"
"ds_read_b64_tr_b16 v[42:43], v61 offset:12800\n"
"v_bitop3_b32 v63, v63, v12, v14 bitop3:0x80\n"
"s_waitcnt lgkmcnt(6)\n"
"v_mfma_f32_16x16x32_bf16 v[20:23], v[44:47], v[16:19], v[20:23]\n"
"v_xor_b32 v61, 32, v61\n"
"ds_read_b64_tr_b16 v[44:45], v61 offset:0\n"
"ds_read_b64_tr_b16 v[46:47], v61 offset:512\n"
"v_bitop3_b32 v63, v63, v16, v18 bitop3:0x80\n"
"v_cmp_eq_u32 vcc, 0, v63\n"
"v_mfma_f32_16x16x32_bf16 v[24:27], v[48:51], v[4:7], 0\n"
"ds_read_b64_tr_b16 v[48:49], v61 offset:4096\n"
"ds_read_b64_tr_b16 v[50:51], v61 offset:4608\n"
"s_cbranch_vccnz LC_171\n"
"v_xor_b32 v63, 0x400, v62\n"
"s_waitcnt lgkmcnt(8)\n"
"v_mfma_f32_16x16x32_bf16 v[24:27], v[32:35], v[8:11], v[24:27]\n"
"ds_read_b64_tr_b16 v[32:33], v61 offset:8192\n"
"ds_read_b64_tr_b16 v[34:35], v61 offset:8704\n"
"s_waitcnt lgkmcnt(8)\n"
"v_mfma_f32_16x16x32_bf16 v[24:27], v[36:39], v[12:15], v[24:27]\n"
"ds_read_b64_tr_b16 v[36:37], v61 offset:12288\n"
"ds_read_b64_tr_b16 v[38:39], v61 offset:12800\n"
"ds_write_b128 v63, v[20:23] offset:0\n"
"s_waitcnt lgkmcnt(9)\n"
"v_mfma_f32_16x16x32_bf16 v[24:27], v[40:43], v[16:19], v[24:27]\n"
"v_xor_b32 v61, 96, v61\n"
"ds_read_b64_tr_b16 v[40:41], v61 offset:0\n"
"ds_read_b64_tr_b16 v[42:43], v61 offset:512\n"
"v_xor_b32 v63, 0x800, v62\n"
"s_waitcnt lgkmcnt(9)\n"
"v_mfma_f32_16x16x32_bf16 v[20:23], v[44:47], v[4:7], 0\n"
"ds_read_b64_tr_b16 v[44:45], v61 offset:4096\n"
"ds_read_b64_tr_b16 v[46:47], v61 offset:4608\n"
"s_waitcnt lgkmcnt(9)\n"
"v_mfma_f32_16x16x32_bf16 v[20:23], v[48:51], v[8:11], v[20:23]\n"
"ds_read_b64_tr_b16 v[48:49], v61 offset:8192\n"
"ds_read_b64_tr_b16 v[50:51], v61 offset:8704\n"
"s_waitcnt lgkmcnt(9)\n"
"v_mfma_f32_16x16x32_bf16 v[20:23], v[32:35], v[12:15], v[20:23]\n"
"ds_read_b64_tr_b16 v[32:33], v61 offset:12288\n"
"ds_read_b64_tr_b16 v[34:35], v61 offset:12800\n"
"v_xor_b32 v61, 32, v61\n"
"ds_write_b128 v63, v[24:27] offset:4096\n"
"s_waitcnt lgkmcnt(10)\n"
"v_mfma_f32_16x16x32_bf16 v[20:23], v[36:39], v[16:19], v[20:23]\n"
"v_xor_b32 v63, 0xc00, v62\n"
"s_waitcnt lgkmcnt(7)\n"
"v_mfma_f32_16x16x32_bf16 v[28:31], v[40:43], v[4:7], v[28:31]\n"
"s_waitcnt lgkmcnt(5)\n"
"v_mfma_f32_16x16x32_bf16 v[28:31], v[44:47], v[8:11], v[28:31]\n"
"s_waitcnt lgkmcnt(3)\n"
"v_mfma_f32_16x16x32_bf16 v[28:31], v[48:51], v[12:15], v[28:31]\n"
"s_nop 0\n"
"ds_write_b128 v63, v[20:23] offset:8192\n"
"s_waitcnt lgkmcnt(2)\n"
"v_mfma_f32_16x16x32_bf16 v[28:31], v[32:35], v[16:19], v[28:31]\n"
"s_waitcnt lgkmcnt(0)\n"
"s_barrier\n"
"ds_read_b128 v[40:43], v62 offset:8192\n"
"ds_read_b128 v[32:35], v62 offset:0\n"
"ds_read_b128 v[36:39], v62 offset:4096\n"
"s_add_u32 s69, s55, 0x8000\n"
"v_bfe_u32 v48, v0, 4, 4\n"
"v_lshrrev_b32 v49, 8, v0\n"
"v_lshlrev_b32 v49, 3, v49\n"
"v_lshl_add_u32 v48, v48, 11, v49\n"
"s_waitcnt lgkmcnt(2)\n"
"v_add_f32 v28, v28, v40\n"
"v_add_f32 v29, v29, v41\n"
"v_add_f32 v30, v30, v42\n"
"v_add_f32 v31, v31, v43\n"
"s_waitcnt lgkmcnt(0)\n"
"v_add_f32 v32, v32, v36\n"
"v_add_f32 v33, v33, v37\n"
"v_add_f32 v34, v34, v38\n"
"v_add_f32 v35, v35, v39\n"
"v_add_f32 v28, v28, v32\n"
"v_add_f32 v29, v29, v33\n"
"v_add_f32 v30, v30, v34\n"
"v_add_f32 v31, v31, v35\n"
"v_cvt_pk_bf16_f32 v44, v28, v29\n"
"v_cvt_pk_bf16_f32 v45, v30, v31\n"
"buffer_store_dwordx2 v[44:45], v48, s[16:19], s69 offen\n"
"s_or_b32 s68, s35, s28\n"
"s_cmp_lg_u32 s68, 0\n"
"s_cbranch_scc1 Le_172\n"
"s_mov_b32 s70, 0\n"
"s_mov_b32 s69, 8192\n"
"Ld_173:\n"
"buffer_load_dwordx2 v[20:21], v0, s[12:15], s69 offen offset:0 sc1\n"
"buffer_load_dwordx2 v[22:23], v0, s[12:15], s69 offen offset:1024 sc1\n"
"buffer_load_dwordx2 v[24:25], v0, s[12:15], s69 offen offset:2048 sc1\n"
"buffer_load_dwordx2 v[26:27], v0, s[12:15], s69 offen offset:3072 sc1\n"
"s_waitcnt vmcnt(0)\n"
"v_cmp_ne_u32 vcc, s64, v20\n"
"v_cmp_ne_u32 s[36:37], s65, v21\n"
"s_nop 1\n"
"s_or_b64 vcc, vcc, s[36:37]\n"
"v_cmp_ne_u32 s[36:37], s64, v22\n"
"s_nop 1\n"
"s_or_b64 vcc, vcc, s[36:37]\n"
"v_cmp_ne_u32 s[36:37], s65, v23\n"
"s_nop 1\n"
"s_or_b64 vcc, vcc, s[36:37]\n"
"v_cmp_ne_u32 s[36:37], s64, v24\n"
"s_nop 1\n"
"s_or_b64 vcc, vcc, s[36:37]\n"
"v_cmp_ne_u32 s[36:37], s65, v25\n"
"s_nop 1\n"
"s_or_b64 vcc, vcc, s[36:37]\n"
"v_cmp_ne_u32 s[36:37], s64, v26\n"
"s_nop 1\n"
"s_or_b64 vcc, vcc, s[36:37]\n"
"v_cmp_ne_u32 s[36:37], s65, v27\n"
"s_nop 1\n"
"s_or_b64 vcc, vcc, s[36:37]\n"
"s_cbranch_scc0 Le_172b\n"
"s_add_u32 s70, s70, 1\n"
"s_cmp_lt_u32 s70, 32768\n"
"s_cbranch_scc1 Ld_173\n"
"s_branch L_END\n"
"Le_172b:\n"
"v_mov_b32 v20, s64\n"
"v_mov_b32 v21, s65\n"
"s_mov_b64 exec, 1\n"
"buffer_store_dwordx2 v[20:21], v0, s[12:15], 0 offen sc1\n"
"s_mov_b64 exec, -1\n"
"Le_172:\n"
"L_FIN:\n"
"s_branch L_END\n"
"Lv_4:\n"
"s_nop 4\n"
"s_waitcnt vmcnt(15)\n"
"s_waitcnt vmcnt(12)\n"
"v_mov_b32 v24, 0x3f803f80\n"
"v_mov_b32 v25, 0x3f803f80\n"
"v_mov_b32 v26, 0x3f803f80\n"
"v_mov_b32 v27, 0x3f803f80\n"
"s_waitcnt lgkmcnt(0)\n"
"s_barrier\n"
"v_mov_b32 v8, 153608\n"
"ds_read_b64 v[6:7], v8\n"
"ds_read_b64_tr_b16 v[208:209], v19 offset:0\n"
"ds_read_b64_tr_b16 v[210:211], v19 offset:512\n"
"ds_read_b64_tr_b16 v[212:213], v19 offset:8192\n"
"ds_read_b64_tr_b16 v[214:215], v19 offset:8704\n"
"ds_read_b64_tr_b16 v[216:217], v19 offset:16384\n"
"ds_read_b64_tr_b16 v[218:219], v19 offset:16896\n"
"ds_read_b64_tr_b16 v[220:221], v19 offset:4096\n"
"ds_read_b64_tr_b16 v[222:223], v19 offset:4608\n"
"ds_read_b64_tr_b16 v[224:225], v19 offset:12288\n"
"ds_read_b64_tr_b16 v[226:227], v19 offset:12800\n"
"ds_read_b64_tr_b16 v[228:229], v19 offset:20480\n"
"ds_read_b64_tr_b16 v[230:231], v19 offset:20992\n"
"ds_read_b64_tr_b16 v[40:41], v21 offset:0\n"
"ds_read_b64_tr_b16 v[42:43], v21 offset:512\n"
"ds_read_b64_tr_b16 v[44:45], v21 offset:4096\n"
"ds_read_b64_tr_b16 v[46:47], v21 offset:4608\n"
"s_waitcnt lgkmcnt(0)\n"
"buffer_store_dwordx4 v[40:43], v0, s[12:15], s25 offen sc1\n"
"buffer_store_dwordx4 v[44:47], v0, s[12:15], s26 offen sc1\n"
"s_waitcnt vmcnt(8)\n"
"s_barrier\n"
"ds_read_b64_tr_b16 v[232:233], v19 offset:24576\n"
"ds_read_b64_tr_b16 v[234:235], v19 offset:25088\n"
"ds_read_b64_tr_b16 v[236:237], v19 offset:32768\n"
"ds_read_b64_tr_b16 v[238:239], v19 offset:33280\n"
"ds_read_b64_tr_b16 v[240:241], v19 offset:40960\n"
"ds_read_b64_tr_b16 v[242:243], v19 offset:41472\n"
"ds_read_b64_tr_b16 v[244:245], v19 offset:28672\n"
"ds_read_b64_tr_b16 v[246:247], v19 offset:29184\n"
"ds_read_b64_tr_b16 v[248:249], v19 offset:36864\n"
"ds_read_b64_tr_b16 v[250:251], v19 offset:37376\n"
"ds_read_b64_tr_b16 v[252:253], v19 offset:45056\n"
"ds_read_b64_tr_b16 v[254:255], v19 offset:45568\n"
"v_readfirstlane_b32 s64, v6\n"
"v_readfirstlane_b32 s65, v7\n"
"s_nop 0\n"
"s_add_u32 s64, s64, 1\n"
"s_addc_u32 s65, s65, 0\n"
"v_mfma_f32_16x16x32_bf16 v[28:31], v[24:27], v[208:211], 0\n"
"v_mfma_f32_16x16x32_bf16 v[32:35], v[24:27], v[212:215], 0\n"
"v_mfma_f32_16x16x32_bf16 v[36:39], v[24:27], v[216:219], 0\n"
"v_mfma_f32_16x16x32_bf16 v[28:31], v[24:27], v[220:223], v[28:31]\n"
"v_mfma_f32_16x16x32_bf16 v[32:35], v[24:27], v[224:227], v[32:35]\n"
"v_mfma_f32_16x16x32_bf16 v[36:39], v[24:27], v[228:231], v[36:39]\n"
"s_waitcnt vmcnt(2)\n"
"s_waitcnt lgkmcnt(0)\n"
"s_barrier\n"
"ds_read_b64_tr_b16 v[160:161], v22 offset:0\n"
"ds_read_b64_tr_b16 v[162:163], v22 offset:512\n"
"ds_read_b64_tr_b16 v[164:165], v22 offset:8192\n"
"ds_read_b64_tr_b16 v[166:167], v22 offset:8704\n"
"ds_read_b64_tr_b16 v[168:169], v22 offset:16384\n"
"ds_read_b64_tr_b16 v[170:171], v22 offset:16896\n"
"ds_read_b64_tr_b16 v[172:173], v22 offset:4096\n"
"ds_read_b64_tr_b16 v[174:175], v22 offset:4608\n"
"ds_read_b64_tr_b16 v[176:177], v22 offset:12288\n"
"ds_read_b64_tr_b16 v[178:179], v22 offset:12800\n"
"ds_read_b64_tr_b16 v[180:181], v22 offset:20480\n"
"ds_read_b64_tr_b16 v[182:183], v22 offset:20992\n"
"v_mfma_f32_16x16x32_bf16 v[28:31], v[24:27], v[232:235], v[28:31]\n"
"v_mfma_f32_16x16x32_bf16 v[32:35], v[24:27], v[236:239], v[32:35]\n"
"v_mfma_f32_16x16x32_bf16 v[36:39], v[24:27], v[240:243], v[36:39]\n"
"v_mfma_f32_16x16x32_bf16 v[28:31], v[24:27], v[244:247], v[28:31]\n"
"v_mfma_f32_16x16x32_bf16 v[32:35], v[24:27], v[248:251], v[32:35]\n"
"v_mfma_f32_16x16x32_bf16 v[36:39], v[24:27], v[252:255], v[36:39]\n"
"s_waitcnt vmcnt(0)\n"
"s_waitcnt lgkmcnt(0)\n"
"s_barrier\n"
"ds_read_b64_tr_b16 v[184:185], v19 offset:0\n"
"ds_read_b64_tr_b16 v[186:187], v19 offset:512\n"
"ds_read_b64_tr_b16 v[188:189], v19 offset:8192\n"
"ds_read_b64_tr_b16 v[190:191], v19 offset:8704\n"
"ds_read_b64_tr_b16 v[192:193], v19 offset:16384\n"
"ds_read_b64_tr_b16 v[194:195], v19 offset:16896\n"
"ds_read_b64_tr_b16 v[196:197], v19 offset:4096\n"
"ds_read_b64_tr_b16 v[198:199], v19 offset:4608\n"
"ds_read_b64_tr_b16 v[200:201], v19 offset:12288\n"
"ds_read_b64_tr_b16 v[202:203], v19 offset:12800\n"
"ds_read_b64_tr_b16 v[204:205], v19 offset:20480\n"
"ds_read_b64_tr_b16 v[206:207], v19 offset:20992\n"
"s_lshl_b32 s68, s31, 9\n"
"s_lshl_b32 s69, s30, 4\n"
"s_add_u32 s68, s68, s69\n"
"s_add_u32 s68, s68, 4096\n"
"s_cmp_eq_u32 s28, 0\n"
"s_cselect_b32 s69, 0, 0x80000000\n"
"v_mov_b32 v40, s64\n"
"v_mov_b32 v41, s65\n"
"v_mov_b32 v42, s69\n"
"s_mov_b64 exec, 1\n"
"buffer_store_dwordx2 v[40:41], v42, s[12:15], s68 offen sc1\n"
"s_mov_b64 exec, -1\n"
"v_mfma_f32_16x16x32_bf16 v[28:31], v[24:27], v[160:163], v[28:31]\n"
"v_mfma_f32_16x16x32_bf16 v[32:35], v[24:27], v[164:167], v[32:35]\n"
"v_mfma_f32_16x16x32_bf16 v[36:39], v[24:27], v[168:171], v[36:39]\n"
"v_mfma_f32_16x16x32_bf16 v[28:31], v[24:27], v[172:175], v[28:31]\n"
"v_mfma_f32_16x16x32_bf16 v[32:35], v[24:27], v[176:179], v[32:35]\n"
"v_mfma_f32_16x16x32_bf16 v[36:39], v[24:27], v[180:183], v[36:39]\n"
"s_waitcnt lgkmcnt(0)\n"
"s_barrier\n"
"ds_read_b64_tr_b16 v[112:113], v19 offset:24576\n"
"ds_read_b64_tr_b16 v[114:115], v19 offset:25088\n"
"ds_read_b64_tr_b16 v[116:117], v19 offset:32768\n"
"ds_read_b64_tr_b16 v[118:119], v19 offset:33280\n"
"ds_read_b64_tr_b16 v[120:121], v19 offset:40960\n"
"ds_read_b64_tr_b16 v[122:123], v19 offset:41472\n"
"ds_read_b64_tr_b16 v[124:125], v19 offset:28672\n"
"ds_read_b64_tr_b16 v[126:127], v19 offset:29184\n"
"ds_read_b64_tr_b16 v[128:129], v19 offset:36864\n"
"ds_read_b64_tr_b16 v[130:131], v19 offset:37376\n"
"ds_read_b64_tr_b16 v[132:133], v19 offset:45056\n"
"ds_read_b64_tr_b16 v[134:135], v19 offset:45568\n"
"v_mfma_f32_16x16x32_bf16 v[28:31], v[24:27], v[184:187], v[28:31]\n"
"v_mfma_f32_16x16x32_bf16 v[32:35], v[24:27], v[188:191], v[32:35]\n"
"v_mfma_f32_16x16x32_bf16 v[36:39], v[24:27], v[192:195], v[36:39]\n"
"v_mfma_f32_16x16x32_bf16 v[28:31], v[24:27], v[196:199], v[28:31]\n"
"v_mfma_f32_16x16x32_bf16 v[32:35], v[24:27], v[200:203], v[32:35]\n"
"v_mfma_f32_16x16x32_bf16 v[36:39], v[24:27], v[204:207], v[36:39]\n"
"s_waitcnt lgkmcnt(0)\n"
"s_barrier\n"
"ds_read_b64_tr_b16 v[136:137], v22 offset:0\n"
"ds_read_b64_tr_b16 v[138:139], v22 offset:512\n"
"ds_read_b64_tr_b16 v[140:141], v22 offset:8192\n"
"ds_read_b64_tr_b16 v[142:143], v22 offset:8704\n"
"ds_read_b64_tr_b16 v[144:145], v22 offset:16384\n"
"ds_read_b64_tr_b16 v[146:147], v22 offset:16896\n"
"ds_read_b64_tr_b16 v[148:149], v22 offset:4096\n"
"ds_read_b64_tr_b16 v[150:151], v22 offset:4608\n"
"ds_read_b64_tr_b16 v[152:153], v22 offset:12288\n"
"ds_read_b64_tr_b16 v[154:155], v22 offset:12800\n"
"ds_read_b64_tr_b16 v[156:157], v22 offset:20480\n"
"ds_read_b64_tr_b16 v[158:159], v22 offset:20992\n"
"v_mfma_f32_16x16x32_bf16 v[28:31], v[24:27], v[112:115], v[28:31]\n"
"v_mfma_f32_16x16x32_bf16 v[32:35], v[24:27], v[116:119], v[32:35]\n"
"v_mfma_f32_16x16x32_bf16 v[36:39], v[24:27], v[120:123], v[36:39]\n"
"v_mfma_f32_16x16x32_bf16 v[28:31], v[24:27], v[124:127], v[28:31]\n"
"v_mfma_f32_16x16x32_bf16 v[32:35], v[24:27], v[128:131], v[32:35]\n"
"v_mfma_f32_16x16x32_bf16 v[36:39], v[24:27], v[132:135], v[36:39]\n"
"s_waitcnt lgkmcnt(0)\n"
"s_barrier\n"
"ds_read_b64_tr_b16 v[64:65], v19 offset:0\n"
"ds_read_b64_tr_b16 v[66:67], v19 offset:512\n"
"ds_read_b64_tr_b16 v[68:69], v19 offset:8192\n"
"ds_read_b64_tr_b16 v[70:71], v19 offset:8704\n"
"ds_read_b64_tr_b16 v[72:73], v19 offset:16384\n"
"ds_read_b64_tr_b16 v[74:75], v19 offset:16896\n"
"ds_read_b64_tr_b16 v[76:77], v19 offset:4096\n"
"ds_read_b64_tr_b16 v[78:79], v19 offset:4608\n"
"ds_read_b64_tr_b16 v[80:81], v19 offset:12288\n"
"ds_read_b64_tr_b16 v[82:83], v19 offset:12800\n"
"ds_read_b64_tr_b16 v[84:85], v19 offset:20480\n"
"ds_read_b64_tr_b16 v[86:87], v19 offset:20992\n"
"v_mfma_f32_16x16x32_bf16 v[28:31], v[24:27], v[136:139], v[28:31]\n"
"v_mfma_f32_16x16x32_bf16 v[32:35], v[24:27], v[140:143], v[32:35]\n"
"v_mfma_f32_16x16x32_bf16 v[36:39], v[24:27], v[144:147], v[36:39]\n"
"v_mfma_f32_16x16x32_bf16 v[28:31], v[24:27], v[148:151], v[28:31]\n"
"v_mfma_f32_16x16x32_bf16 v[32:35], v[24:27], v[152:155], v[32:35]\n"
"v_mfma_f32_16x16x32_bf16 v[36:39], v[24:27], v[156:159], v[36:39]\n"
"s_waitcnt lgkmcnt(0)\n"
"s_barrier\n"
"ds_read_b64_tr_b16 v[88:89], v19 offset:24576\n"
"ds_read_b64_tr_b16 v[90:91], v19 offset:25088\n"
"ds_read_b64_tr_b16 v[92:93], v19 offset:32768\n"
"ds_read_b64_tr_b16 v[94:95], v19 offset:33280\n"
"ds_read_b64_tr_b16 v[96:97], v19 offset:40960\n"
"ds_read_b64_tr_b16 v[98:99], v19 offset:41472\n"
"ds_read_b64_tr_b16 v[100:101], v19 offset:28672\n"
"ds_read_b64_tr_b16 v[102:103], v19 offset:29184\n"
"ds_read_b64_tr_b16 v[104:105], v19 offset:36864\n"
"ds_read_b64_tr_b16 v[106:107], v19 offset:37376\n"
"ds_read_b64_tr_b16 v[108:109], v19 offset:45056\n"
"ds_read_b64_tr_b16 v[110:111], v19 offset:45568\n"
"v_mfma_f32_16x16x32_bf16 v[28:31], v[24:27], v[64:67], v[28:31]\n"
"v_mfma_f32_16x16x32_bf16 v[32:35], v[24:27], v[68:71], v[32:35]\n"
"v_mfma_f32_16x16x32_bf16 v[36:39], v[24:27], v[72:75], v[36:39]\n"
"v_mfma_f32_16x16x32_bf16 v[28:31], v[24:27], v[76:79], v[28:31]\n"
"v_mfma_f32_16x16x32_bf16 v[32:35], v[24:27], v[80:83], v[32:35]\n"
"v_mfma_f32_16x16x32_bf16 v[36:39], v[24:27], v[84:87], v[36:39]\n"
"s_waitcnt lgkmcnt(0)\n"
"v_mfma_f32_16x16x32_bf16 v[28:31], v[24:27], v[88:91], v[28:31]\n"
"v_mfma_f32_16x16x32_bf16 v[32:35], v[24:27], v[92:95], v[32:35]\n"
"v_mfma_f32_16x16x32_bf16 v[36:39], v[24:27], v[96:99], v[36:39]\n"
"v_mfma_f32_16x16x32_bf16 v[28:31], v[24:27], v[100:103], v[28:31]\n"
"v_mfma_f32_16x16x32_bf16 v[32:35], v[24:27], v[104:107], v[32:35]\n"
"v_mfma_f32_16x16x32_bf16 v[36:39], v[24:27], v[108:111], v[36:39]\n"
"s_branch Lj_5\n"
"Lv_3:\n"
"s_nop 4\n"
"s_waitcnt vmcnt(15)\n"
"s_waitcnt vmcnt(12)\n"
"v_mov_b32 v24, 0x3f803f80\n"
"v_mov_b32 v25, 0x3f803f80\n"
"v_mov_b32 v26, 0x3f803f80\n"
"v_mov_b32 v27, 0x3f803f80\n"
"s_waitcnt lgkmcnt(0)\n"
"s_barrier\n"
"v_mov_b32 v8, 153608\n"
"ds_read_b64 v[6:7], v8\n"
"ds_read_b64_tr_b16 v[160:161], v19 offset:0\n"
"ds_read_b64_tr_b16 v[162:163], v19 offset:512\n"
"ds_read_b64_tr_b16 v[164:165], v19 offset:8192\n"
"ds_read_b64_tr_b16 v[166:167], v19 offset:8704\n"
"ds_read_b64_tr_b16 v[168:169], v19 offset:16384\n"
"ds_read_b64_tr_b16 v[170:171], v19 offset:16896\n"
"ds_read_b64_tr_b16 v[172:173], v19 offset:4096\n"
"ds_read_b64_tr_b16 v[174:175], v19 offset:4608\n"
"ds_read_b64_tr_b16 v[176:177], v19 offset:12288\n"
"ds_read_b64_tr_b16 v[178:179], v19 offset:12800\n"
"ds_read_b64_tr_b16 v[180:181], v19 offset:20480\n"
"ds_read_b64_tr_b16 v[182:183], v19 offset:20992\n"
"ds_read_b64_tr_b16 v[40:41], v21 offset:0\n"
"ds_read_b64_tr_b16 v[42:43], v21 offset:512\n"
"ds_read_b64_tr_b16 v[44:45], v21 offset:4096\n"
"ds_read_b64_tr_b16 v[46:47], v21 offset:4608\n"
"s_waitcnt lgkmcnt(0)\n"
"buffer_store_dwordx4 v[40:43], v0, s[12:15], s25 offen sc1\n"
"buffer_store_dwordx4 v[44:47], v0, s[12:15], s26 offen sc1\n"
"s_waitcnt vmcnt(8)\n"
"s_barrier\n"
"ds_read_b64_tr_b16 v[184:185], v19 offset:24576\n"
"ds_read_b64_tr_b16 v[186:187], v19 offset:25088\n"
"ds_read_b64_tr_b16 v[188:189], v19 offset:32768\n"
"ds_read_b64_tr_b16 v[190:191], v19 offset:33280\n"
"ds_read_b64_tr_b16 v[192:193], v19 offset:40960\n"
"ds_read_b64_tr_b16 v[194:195], v19 offset:41472\n"
"ds_read_b64_tr_b16 v[196:197], v19 offset:28672\n"
"ds_read_b64_tr_b16 v[198:199], v19 offset:29184\n"
"ds_read_b64_tr_b16 v[200:201], v19 offset:36864\n"
"ds_read_b64_tr_b16 v[202:203], v19 offset:37376\n"
"ds_read_b64_tr_b16 v[204:205], v19 offset:45056\n"
"ds_read_b64_tr_b16 v[206:207], v19 offset:45568\n"
"v_readfirstlane_b32 s64, v6\n"
"v_readfirstlane_b32 s65, v7\n"
"s_nop 0\n"
"s_add_u32 s64, s64, 1\n"
"s_addc_u32 s65, s65, 0\n"
"v_mfma_f32_16x16x32_bf16 v[28:31], v[24:27], v[160:163], 0\n"
"v_mfma_f32_16x16x32_bf16 v[32:35], v[24:27], v[164:167], 0\n"
"v_mfma_f32_16x16x32_bf16 v[36:39], v[24:27], v[168:171], 0\n"
"v_mfma_f32_16x16x32_bf16 v[28:31], v[24:27], v[172:175], v[28:31]\n"
"v_mfma_f32_16x16x32_bf16 v[32:35], v[24:27], v[176:179], v[32:35]\n"
"v_mfma_f32_16x16x32_bf16 v[36:39], v[24:27], v[180:183], v[36:39]\n"
"s_waitcnt vmcnt(2)\n"
"s_waitcnt lgkmcnt(0)\n"
"s_barrier\n"
"ds_read_b64_tr_b16 v[208:209], v22 offset:0\n"
"ds_read_b64_tr_b16 v[210:211], v22 offset:512\n"
"ds_read_b64_tr_b16 v[212:213], v22 offset:8192\n"
"ds_read_b64_tr_b16 v[214:215], v22 offset:8704\n"
"ds_read_b64_tr_b16 v[216:217], v22 offset:16384\n"
"ds_read_b64_tr_b16 v[218:219], v22 offset:16896\n"
"ds_read_b64_tr_b16 v[220:221], v22 offset:4096\n"
"ds_read_b64_tr_b16 v[222:223], v22 offset:4608\n"
"ds_read_b64_tr_b16 v[224:225], v22 offset:12288\n"
"ds_read_b64_tr_b16 v[226:227], v22 offset:12800\n"
"ds_read_b64_tr_b16 v[228:229], v22 offset:20480\n"
"ds_read_b64_tr_b16 v[230:231], v22 offset:20992\n"
"v_mfma_f32_16x16x32_bf16 v[28:31], v[24:27], v[184:187], v[28:31]\n"
"v_mfma_f32_16x16x32_bf16 v[32:35], v[24:27], v[188:191], v[32:35]\n"
"v_mfma_f32_16x16x32_bf16 v[36:39], v[24:27], v[192:195], v[36:39]\n"
"v_mfma_f32_16x16x32_bf16 v[28:31], v[24:27], v[196:199], v[28:31]\n"
"v_mfma_f32_16x16x32_bf16 v[32:35], v[24:27], v[200:203], v[32:35]\n"
"v_mfma_f32_16x16x32_bf16 v[36:39], v[24:27], v[204:207], v[36:39]\n"
"s_waitcnt vmcnt(0)\n"
"s_waitcnt lgkmcnt(0)\n"
"s_barrier\n"
"ds_read_b64_tr_b16 v[232:233], v19 offset:0\n"
"ds_read_b64_tr_b16 v[234:235], v19 offset:512\n"
"ds_read_b64_tr_b16 v[236:237], v19 offset:8192\n"
"ds_read_b64_tr_b16 v[238:239], v19 offset:8704\n"
"ds_read_b64_tr_b16 v[240:241], v19 offset:16384\n"
"ds_read_b64_tr_b16 v[242:243], v19 offset:16896\n"
"ds_read_b64_tr_b16 v[244:245], v19 offset:4096\n"
"ds_read_b64_tr_b16 v[246:247], v19 offset:4608\n"
"ds_read_b64_tr_b16 v[248:249], v19 offset:12288\n"
"ds_read_b64_tr_b16 v[250:251], v19 offset:12800\n"
"ds_read_b64_tr_b16 v[252:253], v19 offset:20480\n"
"ds_read_b64_tr_b16 v[254:255], v19 offset:20992\n"
"s_lshl_b32 s68, s31, 9\n"
"s_lshl_b32 s69, s30, 4\n"
"s_add_u32 s68, s68, s69\n"
"s_add_u32 s68, s68, 4096\n"
"s_cmp_eq_u32 s28, 0\n"
"s_cselect_b32 s69, 0, 0x80000000\n"
"v_mov_b32 v40, s64\n"
"v_mov_b32 v41, s65\n"
"v_mov_b32 v42, s69\n"
"s_mov_b64 exec, 1\n"
"buffer_store_dwordx2 v[40:41], v42, s[12:15], s68 offen sc1\n"
"s_mov_b64 exec, -1\n"
"v_mfma_f32_16x16x32_bf16 v[28:31], v[24:27], v[208:211], v[28:31]\n"
"v_mfma_f32_16x16x32_bf16 v[32:35], v[24:27], v[212:215], v[32:35]\n"
"v_mfma_f32_16x16x32_bf16 v[36:39], v[24:27], v[216:219], v[36:39]\n"
"v_mfma_f32_16x16x32_bf16 v[28:31], v[24:27], v[220:223], v[28:31]\n"
"v_mfma_f32_16x16x32_bf16 v[32:35], v[24:27], v[224:227], v[32:35]\n"
"v_mfma_f32_16x16x32_bf16 v[36:39], v[24:27], v[228:231], v[36:39]\n"
"s_waitcnt lgkmcnt(0)\n"
"s_barrier\n"
"ds_read_b64_tr_b16 v[64:65], v19 offset:24576\n"
"ds_read_b64_tr_b16 v[66:67], v19 offset:25088\n"
"ds_read_b64_tr_b16 v[68:69], v19 offset:32768\n"
"ds_read_b64_tr_b16 v[70:71], v19 offset:33280\n"
"ds_read_b64_tr_b16 v[72:73], v19 offset:40960\n"
"ds_read_b64_tr_b16 v[74:75], v19 offset:41472\n"
"ds_read_b64_tr_b16 v[76:77], v19 offset:28672\n"
"ds_read_b64_tr_b16 v[78:79], v19 offset:29184\n"
"ds_read_b64_tr_b16 v[80:81], v19 offset:36864\n"
"ds_read_b64_tr_b16 v[82:83], v19 offset:37376\n"
"ds_read_b64_tr_b16 v[84:85], v19 offset:45056\n"
"ds_read_b64_tr_b16 v[86:87], v19 offset:45568\n"
"v_mfma_f32_16x16x32_bf16 v[28:31], v[24:27], v[232:235], v[28:31]\n"
"v_mfma_f32_16x16x32_bf16 v[32:35], v[24:27], v[236:239], v[32:35]\n"
"v_mfma_f32_16x16x32_bf16 v[36:39], v[24:27], v[240:243], v[36:39]\n"
"v_mfma_f32_16x16x32_bf16 v[28:31], v[24:27], v[244:247], v[28:31]\n"
"v_mfma_f32_16x16x32_bf16 v[32:35], v[24:27], v[248:251], v[32:35]\n"
"v_mfma_f32_16x16x32_bf16 v[36:39], v[24:27], v[252:255], v[36:39]\n"
"s_waitcnt lgkmcnt(0)\n"
"s_barrier\n"
"ds_read_b64_tr_b16 v[88:89], v22 offset:0\n"
"ds_read_b64_tr_b16 v[90:91], v22 offset:512\n"
"ds_read_b64_tr_b16 v[92:93], v22 offset:8192\n"
"ds_read_b64_tr_b16 v[94:95], v22 offset:8704\n"
"ds_read_b64_tr_b16 v[96:97], v22 offset:16384\n"
"ds_read_b64_tr_b16 v[98:99], v22 offset:16896\n"
"ds_read_b64_tr_b16 v[100:101], v22 offset:4096\n"
"ds_read_b64_tr_b16 v[102:103], v22 offset:4608\n"
"ds_read_b64_tr_b16 v[104:105], v22 offset:12288\n"
"ds_read_b64_tr_b16 v[106:107], v22 offset:12800\n"
"ds_read_b64_tr_b16 v[108:109], v22 offset:20480\n"
"ds_read_b64_tr_b16 v[110:111], v22 offset:20992\n"
"v_mfma_f32_16x16x32_bf16 v[28:31], v[24:27], v[64:67], v[28:31]\n"
"v_mfma_f32_16x16x32_bf16 v[32:35], v[24:27], v[68:71], v[32:35]\n"
"v_mfma_f32_16x16x32_bf16 v[36:39], v[24:27], v[72:75], v[36:39]\n"
"v_mfma_f32_16x16x32_bf16 v[28:31], v[24:27], v[76:79], v[28:31]\n"
"v_mfma_f32_16x16x32_bf16 v[32:35], v[24:27], v[80:83], v[32:35]\n"
"v_mfma_f32_16x16x32_bf16 v[36:39], v[24:27], v[84:87], v[36:39]\n"
"s_waitcnt lgkmcnt(0)\n"
"s_barrier\n"
"ds_read_b64_tr_b16 v[112:113], v19 offset:0\n"
"ds_read_b64_tr_b16 v[114:115], v19 offset:512\n"
"ds_read_b64_tr_b16 v[116:117], v19 offset:8192\n"
"ds_read_b64_tr_b16 v[118:119], v19 offset:8704\n"
"ds_read_b64_tr_b16 v[120:121], v19 offset:16384\n"
"ds_read_b64_tr_b16 v[122:123], v19 offset:16896\n"
"ds_read_b64_tr_b16 v[124:125], v19 offset:4096\n"
"ds_read_b64_tr_b16 v[126:127], v19 offset:4608\n"
"ds_read_b64_tr_b16 v[128:129], v19 offset:12288\n"
"ds_read_b64_tr_b16 v[130:131], v19 offset:12800\n"
"ds_read_b64_tr_b16 v[132:133], v19 offset:20480\n"
"ds_read_b64_tr_b16 v[134:135], v19 offset:20992\n"
"v_mfma_f32_16x16x32_bf16 v[28:31], v[24:27], v[88:91], v[28:31]\n"
"v_mfma_f32_16x16x32_bf16 v[32:35], v[24:27], v[92:95], v[32:35]\n"
"v_mfma_f32_16x16x32_bf16 v[36:39], v[24:27], v[96:99], v[36:39]\n"
"v_mfma_f32_16x16x32_bf16 v[28:31], v[24:27], v[100:103], v[28:31]\n"
"v_mfma_f32_16x16x32_bf16 v[32:35], v[24:27], v[104:107], v[32:35]\n"
"v_mfma_f32_16x16x32_bf16 v[36:39], v[24:27], v[108:111], v[36:39]\n"
"s_waitcnt lgkmcnt(0)\n"
"s_barrier\n"
"ds_read_b64_tr_b16 v[136:137], v19 offset:24576\n"
"ds_read_b64_tr_b16 v[138:139], v19 offset:25088\n"
"ds_read_b64_tr_b16 v[140:141], v19 offset:32768\n"
"ds_read_b64_tr_b16 v[142:143], v19 offset:33280\n"
"ds_read_b64_tr_b16 v[144:145], v19 offset:40960\n"
"ds_read_b64_tr_b16 v[146:147], v19 offset:41472\n"
"ds_read_b64_tr_b16 v[148:149], v19 offset:28672\n"
"ds_read_b64_tr_b16 v[150:151], v19 offset:29184\n"
"ds_read_b64_tr_b16 v[152:153], v19 offset:36864\n"
"ds_read_b64_tr_b16 v[154:155], v19 offset:37376\n"
"ds_read_b64_tr_b16 v[156:157], v19 offset:45056\n"
"ds_read_b64_tr_b16 v[158:159], v19 offset:45568\n"
"v_mfma_f32_16x16x32_bf16 v[28:31], v[24:27], v[112:115], v[28:31]\n"
"v_mfma_f32_16x16x32_bf16 v[32:35], v[24:27], v[116:119], v[32:35]\n"
"v_mfma_f32_16x16x32_bf16 v[36:39], v[24:27], v[120:123], v[36:39]\n"
"v_mfma_f32_16x16x32_bf16 v[28:31], v[24:27], v[124:127], v[28:31]\n"
"v_mfma_f32_16x16x32_bf16 v[32:35], v[24:27], v[128:131], v[32:35]\n"
"v_mfma_f32_16x16x32_bf16 v[36:39], v[24:27], v[132:135], v[36:39]\n"
"s_waitcnt lgkmcnt(0)\n"
"v_mfma_f32_16x16x32_bf16 v[28:31], v[24:27], v[136:139], v[28:31]\n"
"v_mfma_f32_16x16x32_bf16 v[32:35], v[24:27], v[140:143], v[32:35]\n"
"v_mfma_f32_16x16x32_bf16 v[36:39], v[24:27], v[144:147], v[36:39]\n"
"v_mfma_f32_16x16x32_bf16 v[28:31], v[24:27], v[148:151], v[28:31]\n"
"v_mfma_f32_16x16x32_bf16 v[32:35], v[24:27], v[152:155], v[32:35]\n"
"v_mfma_f32_16x16x32_bf16 v[36:39], v[24:27], v[156:159], v[36:39]\n"
"s_branch Lj_5\n"
"Lv_2:\n"
"s_nop 4\n"
"s_waitcnt vmcnt(15)\n"
"s_waitcnt vmcnt(12)\n"
"v_mov_b32 v24, 0x3f803f80\n"
"v_mov_b32 v25, 0x3f803f80\n"
"v_mov_b32 v26, 0x3f803f80\n"
"v_mov_b32 v27, 0x3f803f80\n"
"s_waitcnt lgkmcnt(0)\n"
"s_barrier\n"
"v_mov_b32 v8, 153608\n"
"ds_read_b64 v[6:7], v8\n"
"ds_read_b64_tr_b16 v[112:113], v19 offset:0\n"
"ds_read_b64_tr_b16 v[114:115], v19 offset:512\n"
"ds_read_b64_tr_b16 v[116:117], v19 offset:8192\n"
"ds_read_b64_tr_b16 v[118:119], v19 offset:8704\n"
"ds_read_b64_tr_b16 v[120:121], v19 offset:16384\n"
"ds_read_b64_tr_b16 v[122:123], v19 offset:16896\n"
"ds_read_b64_tr_b16 v[124:125], v19 offset:4096\n"
"ds_read_b64_tr_b16 v[126:127], v19 offset:4608\n"
"ds_read_b64_tr_b16 v[128:129], v19 offset:12288\n"
"ds_read_b64_tr_b16 v[130:131], v19 offset:12800\n"
"ds_read_b64_tr_b16 v[132:133], v19 offset:20480\n"
"ds_read_b64_tr_b16 v[134:135], v19 offset:20992\n"
"ds_read_b64_tr_b16 v[40:41], v21 offset:0\n"
"ds_read_b64_tr_b16 v[42:43], v21 offset:512\n"
"ds_read_b64_tr_b16 v[44:45], v21 offset:4096\n"
"ds_read_b64_tr_b16 v[46:47], v21 offset:4608\n"
"s_waitcnt lgkmcnt(0)\n"
"buffer_store_dwordx4 v[40:43], v0, s[12:15], s25 offen sc1\n"
"buffer_store_dwordx4 v[44:47], v0, s[12:15], s26 offen sc1\n"
"s_waitcnt vmcnt(8)\n"
"s_barrier\n"
"ds_read_b64_tr_b16 v[136:137], v19 offset:24576\n"
"ds_read_b64_tr_b16 v[138:139], v19 offset:25088\n"
"ds_read_b64_tr_b16 v[140:141], v19 offset:32768\n"
"ds_read_b64_tr_b16 v[142:143], v19 offset:33280\n"
"ds_read_b64_tr_b16 v[144:145], v19 offset:40960\n"
"ds_read_b64_tr_b16 v[146:147], v19 offset:41472\n"
"ds_read_b64_tr_b16 v[148:149], v19 offset:28672\n"
"ds_read_b64_tr_b16 v[150:151], v19 offset:29184\n"
"ds_read_b64_tr_b16 v[152:153], v19 offset:36864\n"
"ds_read_b64_tr_b16 v[154:155], v19 offset:37376\n"
"ds_read_b64_tr_b16 v[156:157], v19 offset:45056\n"
"ds_read_b64_tr_b16 v[158:159], v19 offset:45568\n"
"v_readfirstlane_b32 s64, v6\n"
"v_readfirstlane_b32 s65, v7\n"
"s_nop 0\n"
"s_add_u32 s64, s64, 1\n"
"s_addc_u32 s65, s65, 0\n"
"v_mfma_f32_16x16x32_bf16 v[28:31], v[24:27], v[112:115], 0\n"
"v_mfma_f32_16x16x32_bf16 v[32:35], v[24:27], v[116:119], 0\n"
"v_mfma_f32_16x16x32_bf16 v[36:39], v[24:27], v[120:123], 0\n"
"v_mfma_f32_16x16x32_bf16 v[28:31], v[24:27], v[124:127], v[28:31]\n"
"v_mfma_f32_16x16x32_bf16 v[32:35], v[24:27], v[128:131], v[32:35]\n"
"v_mfma_f32_16x16x32_bf16 v[36:39], v[24:27], v[132:135], v[36:39]\n"
"s_waitcnt vmcnt(2)\n"
"s_waitcnt lgkmcnt(0)\n"
"s_barrier\n"
"ds_read_b64_tr_b16 v[64:65], v22 offset:0\n"
"ds_read_b64_tr_b16 v[66:67], v22 offset:512\n"
"ds_read_b64_tr_b16 v[68:69], v22 offset:8192\n"
"ds_read_b64_tr_b16 v[70:71], v22 offset:8704\n"
"ds_read_b64_tr_b16 v[72:73], v22 offset:16384\n"
"ds_read_b64_tr_b16 v[74:75], v22 offset:16896\n"
"ds_read_b64_tr_b16 v[76:77], v22 offset:4096\n"
"ds_read_b64_tr_b16 v[78:79], v22 offset:4608\n"
"ds_read_b64_tr_b16 v[80:81], v22 offset:12288\n"
"ds_read_b64_tr_b16 v[82:83], v22 offset:12800\n"
"ds_read_b64_tr_b16 v[84:85], v22 offset:20480\n"
"ds_read_b64_tr_b16 v[86:87], v22 offset:20992\n"
"v_mfma_f32_16x16x32_bf16 v[28:31], v[24:27], v[136:139], v[28:31]\n"
"v_mfma_f32_16x16x32_bf16 v[32:35], v[24:27], v[140:143], v[32:35]\n"
"v_mfma_f32_16x16x32_bf16 v[36:39], v[24:27], v[144:147], v[36:39]\n"
"v_mfma_f32_16x16x32_bf16 v[28:31], v[24:27], v[148:151], v[28:31]\n"
"v_mfma_f32_16x16x32_bf16 v[32:35], v[24:27], v[152:155], v[32:35]\n"
"v_mfma_f32_16x16x32_bf16 v[36:39], v[24:27], v[156:159], v[36:39]\n"
"s_waitcnt vmcnt(0)\n"
"s_waitcnt lgkmcnt(0)\n"
"s_barrier\n"
"ds_read_b64_tr_b16 v[88:89], v19 offset:0\n"
"ds_read_b64_tr_b16 v[90:91], v19 offset:512\n"
"ds_read_b64_tr_b16 v[92:93], v19 offset:8192\n"
"ds_read_b64_tr_b16 v[94:95], v19 offset:8704\n"
"ds_read_b64_tr_b16 v[96:97], v19 offset:16384\n"
"ds_read_b64_tr_b16 v[98:99], v19 offset:16896\n"
"ds_read_b64_tr_b16 v[100:101], v19 offset:4096\n"
"ds_read_b64_tr_b16 v[102:103], v19 offset:4608\n"
"ds_read_b64_tr_b16 v[104:105], v19 offset:12288\n"
"ds_read_b64_tr_b16 v[106:107], v19 offset:12800\n"
"ds_read_b64_tr_b16 v[108:109], v19 offset:20480\n"
"ds_read_b64_tr_b16 v[110:111], v19 offset:20992\n"
"s_lshl_b32 s68, s31, 9\n"
"s_lshl_b32 s69, s30, 4\n"
"s_add_u32 s68, s68, s69\n"
"s_add_u32 s68, s68, 4096\n"
"s_cmp_eq_u32 s28, 0\n"
"s_cselect_b32 s69, 0, 0x80000000\n"
"v_mov_b32 v40, s64\n"
"v_mov_b32 v41, s65\n"
"v_mov_b32 v42, s69\n"
"s_mov_b64 exec, 1\n"
"buffer_store_dwordx2 v[40:41], v42, s[12:15], s68 offen sc1\n"
"s_mov_b64 exec, -1\n"
"v_mfma_f32_16x16x32_bf16 v[28:31], v[24:27], v[64:67], v[28:31]\n"
"v_mfma_f32_16x16x32_bf16 v[32:35], v[24:27], v[68:71], v[32:35]\n"
"v_mfma_f32_16x16x32_bf16 v[36:39], v[24:27], v[72:75], v[36:39]\n"
"v_mfma_f32_16x16x32_bf16 v[28:31], v[24:27], v[76:79], v[28:31]\n"
"v_mfma_f32_16x16x32_bf16 v[32:35], v[24:27], v[80:83], v[32:35]\n"
"v_mfma_f32_16x16x32_bf16 v[36:39], v[24:27], v[84:87], v[36:39]\n"
"s_waitcnt lgkmcnt(0)\n"
"s_barrier\n"
"ds_read_b64_tr_b16 v[208:209], v19 offset:24576\n"
"ds_read_b64_tr_b16 v[210:211], v19 offset:25088\n"
"ds_read_b64_tr_b16 v[212:213], v19 offset:32768\n"
"ds_read_b64_tr_b16 v[214:215], v19 offset:33280\n"
"ds_read_b64_tr_b16 v[216:217], v19 offset:40960\n"
"ds_read_b64_tr_b16 v[218:219], v19 offset:41472\n"
"ds_read_b64_tr_b16 v[220:221], v19 offset:28672\n"
"ds_read_b64_tr_b16 v[222:223], v19 offset:29184\n"
"ds_read_b64_tr_b16 v[224:225], v19 offset:36864\n"
"ds_read_b64_tr_b16 v[226:227], v19 offset:37376\n"
"ds_read_b64_tr_b16 v[228:229], v19 offset:45056\n"
"ds_read_b64_tr_b16 v[230:231], v19 offset:45568\n"
"v_mfma_f32_16x16x32_bf16 v[28:31], v[24:27], v[88:91], v[28:31]\n"
"v_mfma_f32_16x16x32_bf16 v[32:35], v[24:27], v[92:95], v[32:35]\n"
"v_mfma_f32_16x16x32_bf16 v[36:39], v[24:27], v[96:99], v[36:39]\n"
"v_mfma_f32_16x16x32_bf16 v[28:31], v[24:27], v[100:103], v[28:31]\n"
"v_mfma_f32_16x16x32_bf16 v[32:35], v[24:27], v[104:107], v[32:35]\n"
"v_mfma_f32_16x16x32_bf16 v[36:39], v[24:27], v[108:111], v[36:39]\n"
"s_waitcnt lgkmcnt(0)\n"
"s_barrier\n"
"ds_read_b64_tr_b16 v[232:233], v22 offset:0\n"
"ds_read_b64_tr_b16 v[234:235], v22 offset:512\n"
"ds_read_b64_tr_b16 v[236:237], v22 offset:8192\n"
"ds_read_b64_tr_b16 v[238:239], v22 offset:8704\n"
"ds_read_b64_tr_b16 v[240:241], v22 offset:16384\n"
"ds_read_b64_tr_b16 v[242:243], v22 offset:16896\n"
"ds_read_b64_tr_b16 v[244:245], v22 offset:4096\n"
"ds_read_b64_tr_b16 v[246:247], v22 offset:4608\n"
"ds_read_b64_tr_b16 v[248:249], v22 offset:12288\n"
"ds_read_b64_tr_b16 v[250:251], v22 offset:12800\n"
"ds_read_b64_tr_b16 v[252:253], v22 offset:20480\n"
"ds_read_b64_tr_b16 v[254:255], v22 offset:20992\n"
"v_mfma_f32_16x16x32_bf16 v[28:31], v[24:27], v[208:211], v[28:31]\n"
"v_mfma_f32_16x16x32_bf16 v[32:35], v[24:27], v[212:215], v[32:35]\n"
"v_mfma_f32_16x16x32_bf16 v[36:39], v[24:27], v[216:219], v[36:39]\n"
"v_mfma_f32_16x16x32_bf16 v[28:31], v[24:27], v[220:223], v[28:31]\n"
"v_mfma_f32_16x16x32_bf16 v[32:35], v[24:27], v[224:227], v[32:35]\n"
"v_mfma_f32_16x16x32_bf16 v[36:39], v[24:27], v[228:231], v[36:39]\n"
"s_waitcnt lgkmcnt(0)\n"
"s_barrier\n"
"ds_read_b64_tr_b16 v[160:161], v19 offset:0\n"
"ds_read_b64_tr_b16 v[162:163], v19 offset:512\n"
"ds_read_b64_tr_b16 v[164:165], v19 offset:8192\n"
"ds_read_b64_tr_b16 v[166:167], v19 offset:8704\n"
"ds_read_b64_tr_b16 v[168:169], v19 offset:16384\n"
"ds_read_b64_tr_b16 v[170:171], v19 offset:16896\n"
"ds_read_b64_tr_b16 v[172:173], v19 offset:4096\n"
"ds_read_b64_tr_b16 v[174:175], v19 offset:4608\n"
"ds_read_b64_tr_b16 v[176:177], v19 offset:12288\n"
"ds_read_b64_tr_b16 v[178:179], v19 offset:12800\n"
"ds_read_b64_tr_b16 v[180:181], v19 offset:20480\n"
"ds_read_b64_tr_b16 v[182:183], v19 offset:20992\n"
"v_mfma_f32_16x16x32_bf16 v[28:31], v[24:27], v[232:235], v[28:31]\n"
"v_mfma_f32_16x16x32_bf16 v[32:35], v[24:27], v[236:239], v[32:35]\n"
"v_mfma_f32_16x16x32_bf16 v[36:39], v[24:27], v[240:243], v[36:39]\n"
"v_mfma_f32_16x16x32_bf16 v[28:31], v[24:27], v[244:247], v[28:31]\n"
"v_mfma_f32_16x16x32_bf16 v[32:35], v[24:27], v[248:251], v[32:35]\n"
"v_mfma_f32_16x16x32_bf16 v[36:39], v[24:27], v[252:255], v[36:39]\n"
"s_waitcnt lgkmcnt(0)\n"
"s_barrier\n"
"ds_read_b64_tr_b16 v[184:185], v19 offset:24576\n"
"ds_read_b64_tr_b16 v[186:187], v19 offset:25088\n"
"ds_read_b64_tr_b16 v[188:189], v19 offset:32768\n"
"ds_read_b64_tr_b16 v[190:191], v19 offset:33280\n"
"ds_read_b64_tr_b16 v[192:193], v19 offset:40960\n"
"ds_read_b64_tr_b16 v[194:195], v19 offset:41472\n"
"ds_read_b64_tr_b16 v[196:197], v19 offset:28672\n"
"ds_read_b64_tr_b16 v[198:199], v19 offset:29184\n"
"ds_read_b64_tr_b16 v[200:201], v19 offset:36864\n"
"ds_read_b64_tr_b16 v[202:203], v19 offset:37376\n"
"ds_read_b64_tr_b16 v[204:205], v19 offset:45056\n"
"ds_read_b64_tr_b16 v[206:207], v19 offset:45568\n"
"v_mfma_f32_16x16x32_bf16 v[28:31], v[24:27], v[160:163], v[28:31]\n"
"v_mfma_f32_16x16x32_bf16 v[32:35], v[24:27], v[164:167], v[32:35]\n"
"v_mfma_f32_16x16x32_bf16 v[36:39], v[24:27], v[168:171], v[36:39]\n"
"v_mfma_f32_16x16x32_bf16 v[28:31], v[24:27], v[172:175], v[28:31]\n"
"v_mfma_f32_16x16x32_bf16 v[32:35], v[24:27], v[176:179], v[32:35]\n"
"v_mfma_f32_16x16x32_bf16 v[36:39], v[24:27], v[180:183], v[36:39]\n"
"s_waitcnt lgkmcnt(0)\n"
"v_mfma_f32_16x16x32_bf16 v[28:31], v[24:27], v[184:187], v[28:31]\n"
"v_mfma_f32_16x16x32_bf16 v[32:35], v[24:27], v[188:191], v[32:35]\n"
"v_mfma_f32_16x16x32_bf16 v[36:39], v[24:27], v[192:195], v[36:39]\n"
"v_mfma_f32_16x16x32_bf16 v[28:31], v[24:27], v[196:199], v[28:31]\n"
"v_mfma_f32_16x16x32_bf16 v[32:35], v[24:27], v[200:203], v[32:35]\n"
"v_mfma_f32_16x16x32_bf16 v[36:39], v[24:27], v[204:207], v[36:39]\n"
"s_branch Lj_5\n"
"LQ_6:\n"
"s_add_u32 s63, s63, 1\n"
"s_cmp_gt_u32 s63, 32768\n"
"s_cbranch_scc1 L_END\n"
"ds_read_b128 v[48:51], v3 offset:6656\n"
"ds_read_b128 v[52:55], v3 offset:6912\n"
"s_waitcnt lgkmcnt(0)\n"
"v_bitop3_b32 v63, 1, v48, v50 bitop3:0x10\n"
"v_bitop3_b32 v63, v63, v52, v54 bitop3:0x10\n"
"v_cmp_eq_u32 vcc, 0, v63\n"
"s_nop 1\n"
"s_cbranch_vccnz LQ_6\n"
"s_nop 7\n"
"s_branch LP_7\n"
"LQ_8:\n"
"s_add_u32 s63, s63, 1\n"
"s_cmp_gt_u32 s63, 32768\n"
"s_cbranch_scc1 L_END\n"
"ds_read_b128 v[48:51], v3 offset:6144\n"
"ds_read_b128 v[52:55], v3 offset:6400\n"
"s_waitcnt lgkmcnt(0)\n"
"v_bitop3_b32 v63, 1, v48, v50 bitop3:0x10\n"
"v_bitop3_b32 v63, v63, v52, v54 bitop3:0x10\n"
"v_cmp_eq_u32 vcc, 0, v63\n"
"s_nop 1\n"
"s_cbranch_vccnz LQ_8\n"
"s_nop 7\n"
"s_branch LP_9\n"
"LQ_10:\n"
"s_add_u32 s63, s63, 1\n"
"s_cmp_gt_u32 s63, 32768\n"
"s_cbranch_scc1 L_END\n"
"ds_read_b128 v[48:51], v3 offset:5632\n"
"ds_read_b128 v[52:55], v3 offset:5888\n"
"s_waitcnt lgkmcnt(0)\n"
"v_bitop3_b32 v63, 1, v48, v50 bitop3:0x10\n"
"v_bitop3_b32 v63, v63, v52, v54 bitop3:0x10\n"
"v_cmp_eq_u32 vcc, 0, v63\n"
"s_nop 1\n"
"s_cbranch_vccnz LQ_10\n"
"s_nop 7\n"
"s_branch LP_11\n"
"LQ_12:\n"
"s_add_u32 s63, s63, 1\n"
"s_cmp_gt_u32 s63, 32768\n"
"s_cbranch_scc1 L_END\n"
"ds_read_b128 v[48:51], v3 offset:5120\n"
"ds_read_b128 v[52:55], v3 offset:5376\n"
"s_waitcnt lgkmcnt(0)\n"
"v_bitop3_b32 v63, 1, v48, v50 bitop3:0x10\n"
"v_bitop3_b32 v63, v63, v52, v54 bitop3:0x10\n"
"v_cmp_eq_u32 vcc, 0, v63\n"
"s_nop 1\n"
"s_cbranch_vccnz LQ_12\n"
"s_nop 7\n"
"s_branch LP_13\n"
"LC_15:\n"
"s_add_u32 s63, s63, 1\n"
"s_cmp_gt_u32 s63, 32768\n"
"s_cbranch_scc1 L_END\n"
"s_add_u32 s69, s51, 0xacc000\n"
"v_bitop3_b32 v63, 1, v4, v6 bitop3:0x10\n"
"v_cmp_eq_u32 vcc, 0, v63\n"
"s_nop 1\n"
"s_cbranch_vccz Lz_16\n"
"buffer_load_dwordx4 v[4:7], v0, s[12:15], s69 offen offset:0 sc1\n"
"Lz_16:\n"
"v_bitop3_b32 v63, 1, v8, v10 bitop3:0x10\n"
"v_cmp_eq_u32 vcc, 0, v63\n"
"s_nop 1\n"
"s_cbranch_vccz Lz_17\n"
"buffer_load_dwordx4 v[8:11], v0, s[12:15], s69 offen offset:1024 sc1\n"
"Lz_17:\n"
"v_bitop3_b32 v63, 1, v12, v14 bitop3:0x10\n"
"v_cmp_eq_u32 vcc, 0, v63\n"
"s_nop 1\n"
"s_cbranch_vccz Lz_18\n"
"buffer_load_dwordx4 v[12:15], v0, s[12:15], s69 offen offset:2048 sc1\n"
"Lz_18:\n"
"v_bitop3_b32 v63, 1, v16, v18 bitop3:0x10\n"
"v_cmp_eq_u32 vcc, 0, v63\n"
"s_nop 1\n"
"s_cbranch_vccz Lz_19\n"
"buffer_load_dwordx4 v[16:19], v0, s[12:15], s69 offen offset:3072 sc1\n"
"Lz_19:\n"
"s_waitcnt vmcnt(0) lgkmcnt(0)\n"
"v_bitop3_b32 v63, 1, v4, v6 bitop3:0x10\n"
"v_bitop3_b32 v63, v63, v8, v10 bitop3:0x10\n"
"v_bitop3_b32 v63, v63, v12, v14 bitop3:0x10\n"
"v_bitop3_b32 v63, v63, v16, v18 bitop3:0x10\n"
"v_cmp_eq_u32 vcc, 0, v63\n"
"s_nop 1\n"
"s_cbranch_vccnz LC_15\n"
"s_nop 7\n"
"v_xor_b32 v61, 64, v61\n"
"ds_read_b64_tr_b16 v[32:33], v61 offset:0\n"
"ds_read_b64_tr_b16 v[34:35], v61 offset:512\n"
"ds_read_b64_tr_b16 v[36:37], v61 offset:4096\n"
"ds_read_b64_tr_b16 v[38:39], v61 offset:4608\n"
"ds_read_b64_tr_b16 v[40:41], v61 offset:8192\n"
"ds_read_b64_tr_b16 v[42:43], v61 offset:8704\n"
"ds_read_b64_tr_b16 v[44:45], v61 offset:12288\n"
"ds_read_b64_tr_b16 v[46:47], v61 offset:12800\n"
"v_xor_b32 v61, 96, v61\n"
"ds_read_b64_tr_b16 v[48:49], v61 offset:0\n"
"ds_read_b64_tr_b16 v[50:51], v61 offset:512\n"
"s_waitcnt vmcnt(0) lgkmcnt(0)\n"
"s_branch LB_14\n"
"LC_21:\n"
"s_add_u32 s63, s63, 1\n"
"s_cmp_gt_u32 s63, 32768\n"
"s_cbranch_scc1 L_END\n"
"s_add_u32 m0, s58, 16384\n"
"s_add_u32 s69, s51, 0xac8000\n"
"buffer_load_dwordx4 v0, s[12:15], s69 offen offset:0 sc1 lds\n"
"buffer_load_dwordx4 v0, s[12:15], s69 offen offset:1024 sc1 lds\n"
"buffer_load_dwordx4 v0, s[12:15], s69 offen offset:2048 sc1 lds\n"
"buffer_load_dwordx4 v0, s[12:15], s69 offen offset:3072 sc1 lds\n"
"s_waitcnt vmcnt(0) lgkmcnt(0)\n"
"ds_read_b128 v[4:7], v60 offset:16384\n"
"ds_read_b128 v[8:11], v60 offset:17408\n"
"ds_read_b128 v[12:15], v60 offset:18432\n"
"ds_read_b128 v[16:19], v60 offset:19456\n"
"s_waitcnt vmcnt(0) lgkmcnt(0)\n"
"v_bitop3_b32 v63, 1, v4, v6 bitop3:0x10\n"
"v_bitop3_b32 v63, v63, v8, v10 bitop3:0x10\n"
"v_bitop3_b32 v63, v63, v12, v14 bitop3:0x10\n"
"v_bitop3_b32 v63, v63, v16, v18 bitop3:0x10\n"
"v_cmp_eq_u32 vcc, 0, v63\n"
"s_nop 1\n"
"s_cbranch_vccnz LC_21\n"
"s_nop 7\n"
"v_xor_b32 v61, 64, v61\n"
"ds_read_b64_tr_b16 v[32:33], v61 offset:0\n"
"ds_read_b64_tr_b16 v[34:35], v61 offset:512\n"
"ds_read_b64_tr_b16 v[36:37], v61 offset:4096\n"
"ds_read_b64_tr_b16 v[38:39], v61 offset:4608\n"
"ds_read_b64_tr_b16 v[40:41], v61 offset:8192\n"
"ds_read_b64_tr_b16 v[42:43], v61 offset:8704\n"
"ds_read_b64_tr_b16 v[44:45], v61 offset:12288\n"
"ds_read_b64_tr_b16 v[46:47], v61 offset:12800\n"
"v_xor_b32 v61, 96, v61\n"
"ds_read_b64_tr_b16 v[48:49], v61 offset:0\n"
"ds_read_b64_tr_b16 v[50:51], v61 offset:512\n"
"s_waitcnt vmcnt(0) lgkmcnt(0)\n"
"s_branch LB_20\n"
"LC_23:\n"
"s_add_u32 s63, s63, 1\n"
"s_cmp_gt_u32 s63, 32768\n"
"s_cbranch_scc1 L_END\n"
"s_add_u32 m0, s58, 0\n"
"s_add_u32 s69, s51, 0xac4000\n"
"buffer_load_dwordx4 v0, s[12:15], s69 offen offset:0 sc1 lds\n"
"buffer_load_dwordx4 v0, s[12:15], s69 offen offset:1024 sc1 lds\n"
"buffer_load_dwordx4 v0, s[12:15], s69 offen offset:2048 sc1 lds\n"
"buffer_load_dwordx4 v0, s[12:15], s69 offen offset:3072 sc1 lds\n"
"s_waitcnt vmcnt(0) lgkmcnt(0)\n"
"ds_read_b128 v[4:7], v60 offset:0\n"
"ds_read_b128 v[8:11], v60 offset:1024\n"
"ds_read_b128 v[12:15], v60 offset:2048\n"
"ds_read_b128 v[16:19], v60 offset:3072\n"
"s_waitcnt vmcnt(0) lgkmcnt(0)\n"
"v_bitop3_b32 v63, 1, v4, v6 bitop3:0x10\n"
"v_bitop3_b32 v63, v63, v8, v10 bitop3:0x10\n"
"v_bitop3_b32 v63, v63, v12, v14 bitop3:0x10\n"
"v_bitop3_b32 v63, v63, v16, v18 bitop3:0x10\n"
"v_cmp_eq_u32 vcc, 0, v63\n"
"s_nop 1\n"
"s_cbranch_vccnz LC_23\n"
"s_nop 7\n"
"v_xor_b32 v61, 64, v61\n"
"ds_read_b64_tr_b16 v[32:33], v61 offset:0\n"
"ds_read_b64_tr_b16 v[34:35], v61 offset:512\n"
"ds_read_b64_tr_b16 v[36:37], v61 offset:4096\n"
"ds_read_b64_tr_b16 v[38:39], v61 offset:4608\n"
"ds_read_b64_tr_b16 v[40:41], v61 offset:8192\n"
"ds_read_b64_tr_b16 v[42:43], v61 offset:8704\n"
"ds_read_b64_tr_b16 v[44:45], v61 offset:12288\n"
"ds_read_b64_tr_b16 v[46:47], v61 offset:12800\n"
"v_xor_b32 v61, 96, v61\n"
"ds_read_b64_tr_b16 v[48:49], v61 offset:0\n"
"ds_read_b64_tr_b16 v[50:51], v61 offset:512\n"
"s_waitcnt vmcnt(0) lgkmcnt(0)\n"
"s_branch LB_22\n"
"Lf_24:\n"
"s_add_u32 s27, s27, 1\n"
"s_cmp_gt_u32 s27, 1048576\n"
"s_cbranch_scc1 L_END\n"
"ds_read_b32 v63, v3 offset:4\n"
"s_waitcnt lgkmcnt(0)\n"
"v_cmp_gt_u32 vcc, 3, v63\n"
"s_nop 1\n"
"s_and_b32 s68, vcc_lo, 15\n"
"s_cbranch_scc1 Lf_24\n"
"s_nop 4\n"
"s_branch Lg_25\n"
"LC_27:\n"
"s_add_u32 s63, s63, 1\n"
"s_cmp_gt_u32 s63, 32768\n"
"s_cbranch_scc1 L_END\n"
"s_add_u32 m0, s58, 0\n"
"s_add_u32 s69, s51, 0xac0000\n"
"buffer_load_dwordx4 v0, s[12:15], s69 offen offset:0 sc1 lds\n"
"buffer_load_dwordx4 v0, s[12:15], s69 offen offset:1024 sc1 lds\n"
"buffer_load_dwordx4 v0, s[12:15], s69 offen offset:2048 sc1 lds\n"
"buffer_load_dwordx4 v0, s[12:15], s69 offen offset:3072 sc1 lds\n"
"s_waitcnt vmcnt(0) lgkmcnt(0)\n"
"ds_read_b128 v[4:7], v60 offset:0\n"
"ds_read_b128 v[8:11], v60 offset:1024\n"
"ds_read_b128 v[12:15], v60 offset:2048\n"
"ds_read_b128 v[16:19], v60 offset:3072\n"
"s_waitcnt vmcnt(0) lgkmcnt(0)\n"
"v_bitop3_b32 v63, 1, v4, v6 bitop3:0x10\n"
"v_bitop3_b32 v63, v63, v8, v10 bitop3:0x10\n"
"v_bitop3_b32 v63, v63, v12, v14 bitop3:0x10\n"
"v_bitop3_b32 v63, v63, v16, v18 bitop3:0x10\n"
"v_cmp_eq_u32 vcc, 0, v63\n"
"s_nop 1\n"
"s_cbranch_vccnz LC_27\n"
"s_nop 7\n"
"v_xor_b32 v61, 64, v61\n"
"ds_read_b64_tr_b16 v[32:33], v61 offset:0\n"
"ds_read_b64_tr_b16 v[34:35], v61 offset:512\n"
"ds_read_b64_tr_b16 v[36:37], v61 offset:4096\n"
"ds_read_b64_tr_b16 v[38:39], v61 offset:4608\n"
"ds_read_b64_tr_b16 v[40:41], v61 offset:8192\n"
"ds_read_b64_tr_b16 v[42:43], v61 offset:8704\n"
"ds_read_b64_tr_b16 v[44:45], v61 offset:12288\n"
"ds_read_b64_tr_b16 v[46:47], v61 offset:12800\n"
"v_xor_b32 v61, 96, v61\n"
"ds_read_b64_tr_b16 v[48:49], v61 offset:0\n"
"ds_read_b64_tr_b16 v[50:51], v61 offset:512\n"
"s_waitcnt vmcnt(0) lgkmcnt(0)\n"
"s_branch LB_26\n"
"Lf_28:\n"
"s_add_u32 s27, s27, 1\n"
"s_cmp_gt_u32 s27, 1048576\n"
"s_cbranch_scc1 L_END\n"
"ds_read_b32 v63, v3 offset:4\n"
"s_waitcnt lgkmcnt(0)\n"
"v_cmp_gt_u32 vcc, 5, v63\n"
"s_nop 1\n"
"s_and_b32 s68, vcc_lo, 15\n"
"s_cbranch_scc1 Lf_28\n"
"s_nop 4\n"
"s_branch Lg_29\n"
"Lf_30:\n"
"s_add_u32 s27, s27, 1\n"
"s_cmp_gt_u32 s27, 1048576\n"
"s_cbranch_scc1 L_END\n"
"ds_read_b32 v63, v3 offset:0\n"
"s_waitcnt lgkmcnt(0)\n"
"v_cmp_gt_u32 vcc, 4, v63\n"
"s_nop 1\n"
"s_and_b32 s68, vcc_lo, 15\n"
"s_cbranch_scc1 Lf_30\n"
"s_nop 4\n"
"s_branch Lg_31\n"
"Lr_32:\n"
"s_add_u32 s63, s63, 1\n"
"s_cmp_gt_u32 s63, 32768\n"
"s_cbranch_scc1 L_END\n"
"s_sleep 4\n"
"s_lshl_b32 s68, s30, 10\n"
"s_add_u32 s68, s68, 0xed0000\n"
"v_bfe_u32 v52, v0, 4, 3\n"
"v_lshlrev_b32 v52, 7, v52\n"
"buffer_load_dwordx3 v[52:54], v52, s[12:15], s68 offen sc1\n"
"s_waitcnt vmcnt(0) lgkmcnt(0)\n"
"v_xor_b32 v52, s64, v52\n"
"v_xor_b32 v53, s65, v53\n"
"v_or_b32 v52, v52, v53\n"
"v_cmp_ne_u32 vcc, 0, v52\n"
"s_nop 1\n"
"s_cbranch_vccnz Lr_32\n"
"s_nop 4\n"
"s_branch Lk_33\n"
"Lr_36:\n"
"s_add_u32 s63, s63, 1\n"
"s_cmp_gt_u32 s63, 32768\n"
"s_cbranch_scc1 L_END\n"
"v_or_b32 v46, s60, v0\n"
"v_lshrrev_b32 v50, 1, v0\n"
"v_or_b32 v50, s60, v50\n"
"s_add_u32 s69, s54, 0x1f0000\n"
"buffer_load_dwordx4 v[46:49], v46, s[12:15], s69 offen sc1\n"
"buffer_load_dwordx2 v[50:51], v50, s[12:15], s69 offen offset:1024 sc1\n"
"s_waitcnt vmcnt(0) lgkmcnt(0)\n"
"v_xor_b32 v4, s64, v49\n"
"v_xor_b32 v5, s64, v51\n"
"v_or_b32 v4, v4, v5\n"
"v_cmp_ne_u32 vcc, 0, v4\n"
"s_cmp_lg_u32 s60, 0\n"
"s_nop 0\n"
"s_cselect_b64 vcc, 0, vcc\n"
"s_cbranch_vccnz Lr_36\n"
"s_nop 4\n"
"s_branch Lk_37\n"
"LQ_38:\n"
"s_add_u32 s63, s63, 1\n"
"s_cmp_gt_u32 s63, 32768\n"
"s_cbranch_scc1 L_END\n"
"s_add_u32 m0, s58, 49152\n"
"s_add_u32 s69, s51, 0xccc000\n"
"buffer_load_dwordx4 v0, s[12:15], s69 offen offset:0 sc1 lds\n"
"buffer_load_dwordx4 v0, s[12:15], s69 offen offset:1024 sc1 lds\n"
"buffer_load_dwordx4 v0, s[12:15], s69 offen offset:2048 sc1 lds\n"
"buffer_load_dwordx4 v0, s[12:15], s69 offen offset:3072 sc1 lds\n"
"s_waitcnt vmcnt(0) lgkmcnt(0)\n"
"ds_read_b128 v[32:35], v60 offset:49152\n"
"ds_read_b128 v[36:39], v60 offset:50176\n"
"ds_read_b128 v[40:43], v60 offset:51200\n"
"ds_read_b128 v[44:47], v60 offset:52224\n"
"s_waitcnt vmcnt(0) lgkmcnt(0)\n"
"v_bitop3_b32 v63, 1, v32, v34 bitop3:0x80\n"
"v_bitop3_b32 v63, v63, v36, v38 bitop3:0x80\n"
"v_bitop3_b32 v63, v63, v40, v42 bitop3:0x80\n"
"v_bitop3_b32 v63, v63, v44, v46 bitop3:0x80\n"
"v_cmp_eq_u32 vcc, 0, v63\n"
"s_nop 1\n"
"s_cbranch_vccnz LQ_38\n"
"v_mfma_f32_16x16x32_bf16 v[48:51], v[56:59], v[32:35], 0\n"
"v_mfma_f32_16x16x32_bf16 v[52:55], v[32:35], v[32:35], 0\n"
"v_mfma_f32_16x16x32_bf16 v[48:51], v[56:59], v[36:39], v[48:51]\n"
"v_mfma_f32_16x16x32_bf16 v[52:55], v[36:39], v[36:39], v[52:55]\n"
"v_mfma_f32_16x16x32_bf16 v[48:51], v[56:59], v[40:43], v[48:51]\n"
"v_mfma_f32_16x16x32_bf16 v[52:55], v[40:43], v[40:43], v[52:55]\n"
"v_mfma_f32_16x16x32_bf16 v[48:51], v[56:59], v[44:47], v[48:51]\n"
"v_mfma_f32_16x16x32_bf16 v[52:55], v[44:47], v[44:47], v[52:55]\n"
"v_mfma_f32_16x16x32_bf16 v[4:7], v[68:71], v[32:35], 0\n"
"v_mfma_f32_16x16x32_bf16 v[8:11], v[32:35], v[72:75], 0\n"
"v_mfma_f32_16x16x32_bf16 v[12:15], v[64:67], v[32:35], 0\n"
"s_nop 7\n"
"s_branch LP_39\n"
"Lf_40:\n"
"s_add_u32 s27, s27, 1\n"
"s_cmp_gt_u32 s27, 1048576\n"
"s_cbranch_scc1 L_END\n"
"ds_read_b32 v17, v3 offset:0\n"
"s_waitcnt lgkmcnt(0)\n"
"v_cmp_gt_u32 vcc, 6, v17\n"
"s_nop 1\n"
"s_and_b32 s68, vcc_lo, 15\n"
"s_cbranch_scc1 Lf_40\n"
"s_nop 4\n"
"s_branch Lg_41\n"
"LQ_42:\n"
"s_add_u32 s63, s63, 1\n"
"s_cmp_gt_u32 s63, 32768\n"
"s_cbranch_scc1 L_END\n"
"ds_read_b128 v[48:51], v3 offset:6656\n"
"ds_read_b128 v[52:55], v3 offset:6912\n"
"s_waitcnt lgkmcnt(0)\n"
"v_bitop3_b32 v63, 1, v48, v50 bitop3:0x10\n"
"v_bitop3_b32 v63, v63, v52, v54 bitop3:0x10\n"
"v_cmp_eq_u32 vcc, 0, v63\n"
"s_nop 1\n"
"s_cbranch_vccnz LQ_42\n"
"s_nop 7\n"
"s_branch LP_43\n"
"Lf_44:\n"
"s_add_u32 s27, s27, 1\n"
"s_cmp_gt_u32 s27, 1048576\n"
"s_cbranch_scc1 L_END\n"
"ds_read_b32 v20, v3 offset:4\n"
"s_waitcnt lgkmcnt(0)\n"
"v_cmp_gt_u32 vcc, 10, v20\n"
"s_nop 1\n"
"s_and_b32 s68, vcc_lo, 15\n"
"s_cbranch_scc1 Lf_44\n"
"s_nop 4\n"
"s_branch Lg_45\n"
"LQ_46:\n"
"s_add_u32 s63, s63, 1\n"
"s_cmp_gt_u32 s63, 32768\n"
"s_cbranch_scc1 L_END\n"
"s_add_u32 m0, s58, 32768\n"
"s_add_u32 s69, s51, 0xcc8000\n"
"buffer_load_dwordx4 v0, s[12:15], s69 offen offset:0 sc1 lds\n"
"buffer_load_dwordx4 v0, s[12:15], s69 offen offset:1024 sc1 lds\n"
"buffer_load_dwordx4 v0, s[12:15], s69 offen offset:2048 sc1 lds\n"
"buffer_load_dwordx4 v0, s[12:15], s69 offen offset:3072 sc1 lds\n"
"s_waitcnt vmcnt(0) lgkmcnt(0)\n"
"ds_read_b128 v[32:35], v60 offset:32768\n"
"ds_read_b128 v[36:39], v60 offset:33792\n"
"ds_read_b128 v[40:43], v60 offset:34816\n"
"ds_read_b128 v[44:47], v60 offset:35840\n"
"s_waitcnt vmcnt(0) lgkmcnt(0)\n"
"v_bitop3_b32 v63, 1, v32, v34 bitop3:0x80\n"
"v_bitop3_b32 v63, v63, v36, v38 bitop3:0x80\n"
"v_bitop3_b32 v63, v63, v40, v42 bitop3:0x80\n"
"v_bitop3_b32 v63, v63, v44, v46 bitop3:0x80\n"
"v_cmp_eq_u32 vcc, 0, v63\n"
"s_nop 1\n"
"s_cbranch_vccnz LQ_46\n"
"v_mfma_f32_16x16x32_bf16 v[48:51], v[56:59], v[32:35], 0\n"
"v_mfma_f32_16x16x32_bf16 v[52:55], v[32:35], v[32:35], 0\n"
"v_mfma_f32_16x16x32_bf16 v[48:51], v[56:59], v[36:39], v[48:51]\n"
"v_mfma_f32_16x16x32_bf16 v[52:55], v[36:39], v[36:39], v[52:55]\n"
"v_mfma_f32_16x16x32_bf16 v[48:51], v[56:59], v[40:43], v[48:51]\n"
"v_mfma_f32_16x16x32_bf16 v[52:55], v[40:43], v[40:43], v[52:55]\n"
"v_mfma_f32_16x16x32_bf16 v[48:51], v[56:59], v[44:47], v[48:51]\n"
"v_mfma_f32_16x16x32_bf16 v[52:55], v[44:47], v[44:47], v[52:55]\n"
"v_mfma_f32_16x16x32_bf16 v[24:27], v[68:71], v[32:35], 0\n"
"v_mfma_f32_16x16x32_bf16 v[20:23], v[32:35], v[72:75], 0\n"
"v_mfma_f32_16x16x32_bf16 v[28:31], v[64:67], v[32:35], 0\n"
"s_nop 7\n"
"s_branch LP_47\n"
"Lf_48:\n"
"s_add_u32 s27, s27, 1\n"
"s_cmp_gt_u32 s27, 1048576\n"
"s_cbranch_scc1 L_END\n"
"ds_read_b32 v17, v3 offset:0\n"
"s_waitcnt lgkmcnt(0)\n"
"v_cmp_gt_u32 vcc, 7, v17\n"
"s_nop 1\n"
"s_and_b32 s68, vcc_lo, 15\n"
"s_cbranch_scc1 Lf_48\n"
"s_nop 4\n"
"s_branch Lg_49\n"
"LQ_50:\n"
"s_add_u32 s63, s63, 1\n"
"s_cmp_gt_u32 s63, 32768\n"
"s_cbranch_scc1 L_END\n"
"ds_read_b128 v[48:51], v3 offset:6144\n"
"ds_read_b128 v[52:55], v3 offset:6400\n"
"s_waitcnt lgkmcnt(0)\n"
"v_bitop3_b32 v63, 1, v48, v50 bitop3:0x10\n"
"v_bitop3_b32 v63, v63, v52, v54 bitop3:0x10\n"
"v_cmp_eq_u32 vcc, 0, v63\n"
"s_nop 1\n"
"s_cbranch_vccnz LQ_50\n"
"s_nop 7\n"
"s_branch LP_51\n"
"LQ_52:\n"
"s_add_u32 s63, s63, 1\n"
"s_cmp_gt_u32 s63, 32768\n"
"s_cbranch_scc1 L_END\n"
"s_add_u32 m0, s58, 16384\n"
"s_add_u32 s69, s51, 0xcc4000\n"
"buffer_load_dwordx4 v0, s[12:15], s69 offen offset:0 sc1 lds\n"
"buffer_load_dwordx4 v0, s[12:15], s69 offen offset:1024 sc1 lds\n"
"buffer_load_dwordx4 v0, s[12:15], s69 offen offset:2048 sc1 lds\n"
"buffer_load_dwordx4 v0, s[12:15], s69 offen offset:3072 sc1 lds\n"
"s_waitcnt vmcnt(0) lgkmcnt(0)\n"
"ds_read_b128 v[32:35], v60 offset:16384\n"
"ds_read_b128 v[36:39], v60 offset:17408\n"
"ds_read_b128 v[40:43], v60 offset:18432\n"
"ds_read_b128 v[44:47], v60 offset:19456\n"
"s_waitcnt vmcnt(0) lgkmcnt(0)\n"
"v_bitop3_b32 v63, 1, v32, v34 bitop3:0x80\n"
"v_bitop3_b32 v63, v63, v36, v38 bitop3:0x80\n"
"v_bitop3_b32 v63, v63, v40, v42 bitop3:0x80\n"
"v_bitop3_b32 v63, v63, v44, v46 bitop3:0x80\n"
"v_cmp_eq_u32 vcc, 0, v63\n"
"s_nop 1\n"
"s_cbranch_vccnz LQ_52\n"
"v_mfma_f32_16x16x32_bf16 v[48:51], v[56:59], v[32:35], 0\n"
"v_mfma_f32_16x16x32_bf16 v[52:55], v[32:35], v[32:35], 0\n"
"v_mfma_f32_16x16x32_bf16 v[48:51], v[56:59], v[36:39], v[48:51]\n"
"v_mfma_f32_16x16x32_bf16 v[52:55], v[36:39], v[36:39], v[52:55]\n"
"v_mfma_f32_16x16x32_bf16 v[48:51], v[56:59], v[40:43], v[48:51]\n"
"v_mfma_f32_16x16x32_bf16 v[52:55], v[40:43], v[40:43], v[52:55]\n"
"v_mfma_f32_16x16x32_bf16 v[48:51], v[56:59], v[44:47], v[48:51]\n"
"v_mfma_f32_16x16x32_bf16 v[52:55], v[44:47], v[44:47], v[52:55]\n"
"v_mfma_f32_16x16x32_bf16 v[4:7], v[68:71], v[32:35], 0\n"
"v_mfma_f32_16x16x32_bf16 v[8:11], v[32:35], v[72:75], 0\n"
"v_mfma_f32_16x16x32_bf16 v[12:15], v[64:67], v[32:35], 0\n"
"s_nop 7\n"
"s_branch LP_53\n"
"LQ_54:\n"
"s_add_u32 s63, s63, 1\n"
"s_cmp_gt_u32 s63, 32768\n"
"s_cbranch_scc1 L_END\n"
"ds_read_b128 v[48:51], v3 offset:5632\n"
"ds_read_b128 v[52:55], v3 offset:5888\n"
"s_waitcnt lgkmcnt(0)\n"
"v_bitop3_b32 v63, 1, v48, v50 bitop3:0x10\n"
"v_bitop3_b32 v63, v63, v52, v54 bitop3:0x10\n"
"v_cmp_eq_u32 vcc, 0, v63\n"
"s_nop 1\n"
"s_cbranch_vccnz LQ_54\n"
"s_nop 7\n"
"s_branch LP_55\n"
"LQ_56:\n"
"s_add_u32 s63, s63, 1\n"
"s_cmp_gt_u32 s63, 32768\n"
"s_cbranch_scc1 L_END\n"
"s_add_u32 m0, s58, 0\n"
"s_add_u32 s69, s51, 0xcc0000\n"
"buffer_load_dwordx4 v0, s[12:15], s69 offen offset:0 sc1 lds\n"
"buffer_load_dwordx4 v0, s[12:15], s69 offen offset:1024 sc1 lds\n"
"buffer_load_dwordx4 v0, s[12:15], s69 offen offset:2048 sc1 lds\n"
"buffer_load_dwordx4 v0, s[12:15], s69 offen offset:3072 sc1 lds\n"
"s_waitcnt vmcnt(0) lgkmcnt(0)\n"
"ds_read_b128 v[32:35], v60 offset:0\n"
"ds_read_b128 v[36:39], v60 offset:1024\n"
"ds_read_b128 v[40:43], v60 offset:2048\n"
"ds_read_b128 v[44:47], v60 offset:3072\n"
"s_waitcnt vmcnt(0) lgkmcnt(0)\n"
"v_bitop3_b32 v63, 1, v32, v34 bitop3:0x80\n"
"v_bitop3_b32 v63, v63, v36, v38 bitop3:0x80\n"
"v_bitop3_b32 v63, v63, v40, v42 bitop3:0x80\n"
"v_bitop3_b32 v63, v63, v44, v46 bitop3:0x80\n"
"v_cmp_eq_u32 vcc, 0, v63\n"
"s_nop 1\n"
"s_cbranch_vccnz LQ_56\n"
"v_mfma_f32_16x16x32_bf16 v[48:51], v[56:59], v[32:35], 0\n"
"v_mfma_f32_16x16x32_bf16 v[52:55], v[32:35], v[32:35], 0\n"
"v_mfma_f32_16x16x32_bf16 v[48:51], v[56:59], v[36:39], v[48:51]\n"
"v_mfma_f32_16x16x32_bf16 v[52:55], v[36:39], v[36:39], v[52:55]\n"
"v_mfma_f32_16x16x32_bf16 v[48:51], v[56:59], v[40:43], v[48:51]\n"
"v_mfma_f32_16x16x32_bf16 v[52:55], v[40:43], v[40:43], v[52:55]\n"
"v_mfma_f32_16x16x32_bf16 v[48:51], v[56:59], v[44:47], v[48:51]\n"
"v_mfma_f32_16x16x32_bf16 v[52:55], v[44:47], v[44:47], v[52:55]\n"
"v_mfma_f32_16x16x32_bf16 v[24:27], v[68:71], v[32:35], 0\n"
"v_mfma_f32_16x16x32_bf16 v[20:23], v[32:35], v[72:75], 0\n"
"v_mfma_f32_16x16x32_bf16 v[28:31], v[64:67], v[32:35], 0\n"
"s_nop 7\n"
"s_branch LP_57\n"
"LQ_58:\n"
"s_add_u32 s63, s63, 1\n"
"s_cmp_gt_u32 s63, 32768\n"
"s_cbranch_scc1 L_END\n"
"ds_read_b128 v[48:51], v3 offset:5120\n"
"ds_read_b128 v[52:55], v3 offset:5376\n"
"s_waitcnt lgkmcnt(0)\n"
"v_bitop3_b32 v63, 1, v48, v50 bitop3:0x10\n"
"v_bitop3_b32 v63, v63, v52, v54 bitop3:0x10\n"
"v_cmp_eq_u32 vcc, 0, v63\n"
"s_nop 1\n"
"s_cbranch_vccnz LQ_58\n"
"s_nop 7\n"
"s_branch LP_59\n"
"LC_61:\n"
"s_add_u32 s63, s63, 1\n"
"s_cmp_gt_u32 s63, 32768\n"
"s_cbranch_scc1 L_END\n"
"s_add_u32 s69, s51, 0xacc000\n"
"v_bitop3_b32 v63, 1, v4, v6 bitop3:0x10\n"
"v_cmp_eq_u32 vcc, 0, v63\n"
"s_nop 1\n"
"s_cbranch_vccz Lz_62\n"
"buffer_load_dwordx4 v[4:7], v0, s[12:15], s69 offen offset:0 sc1\n"
"Lz_62:\n"
"v_bitop3_b32 v63, 1, v8, v10 bitop3:0x10\n"
"v_cmp_eq_u32 vcc, 0, v63\n"
"s_nop 1\n"
"s_cbranch_vccz Lz_63\n"
"buffer_load_dwordx4 v[8:11], v0, s[12:15], s69 offen offset:1024 sc1\n"
"Lz_63:\n"
"v_bitop3_b32 v63, 1, v12, v14 bitop3:0x10\n"
"v_cmp_eq_u32 vcc, 0, v63\n"
"s_nop 1\n"
"s_cbranch_vccz Lz_64\n"
"buffer_load_dwordx4 v[12:15], v0, s[12:15], s69 offen offset:2048 sc1\n"
"Lz_64:\n"
"v_bitop3_b32 v63, 1, v16, v18 bitop3:0x10\n"
"v_cmp_eq_u32 vcc, 0, v63\n"
"s_nop 1\n"
"s_cbranch_vccz Lz_65\n"
"buffer_load_dwordx4 v[16:19], v0, s[12:15], s69 offen offset:3072 sc1\n"
"Lz_65:\n"
"s_waitcnt vmcnt(0) lgkmcnt(0)\n"
"v_bitop3_b32 v63, 1, v4, v6 bitop3:0x10\n"
"v_bitop3_b32 v63, v63, v8, v10 bitop3:0x10\n"
"v_bitop3_b32 v63, v63, v12, v14 bitop3:0x10\n"
"v_bitop3_b32 v63, v63, v16, v18 bitop3:0x10\n"
"v_cmp_eq_u32 vcc, 0, v63\n"
"s_nop 1\n"
"s_cbranch_vccnz LC_61\n"
"s_nop 7\n"
"v_xor_b32 v61, 64, v61\n"
"ds_read_b64_tr_b16 v[32:33], v61 offset:0\n"
"ds_read_b64_tr_b16 v[34:35], v61 offset:512\n"
"ds_read_b64_tr_b16 v[36:37], v61 offset:4096\n"
"ds_read_b64_tr_b16 v[38:39], v61 offset:4608\n"
"ds_read_b64_tr_b16 v[40:41], v61 offset:8192\n"
"ds_read_b64_tr_b16 v[42:43], v61 offset:8704\n"
"ds_read_b64_tr_b16 v[44:45], v61 offset:12288\n"
"ds_read_b64_tr_b16 v[46:47], v61 offset:12800\n"
"v_xor_b32 v61, 96, v61\n"
"ds_read_b64_tr_b16 v[48:49], v61 offset:0\n"
"ds_read_b64_tr_b16 v[50:51], v61 offset:512\n"
"s_waitcnt vmcnt(0) lgkmcnt(0)\n"
"s_branch LB_60\n"
"LC_67:\n"
"s_add_u32 s63, s63, 1\n"
"s_cmp_gt_u32 s63, 32768\n"
"s_cbranch_scc1 L_END\n"
"s_add_u32 m0, s58, 16384\n"
"s_add_u32 s69, s51, 0xac8000\n"
"buffer_load_dwordx4 v0, s[12:15], s69 offen offset:0 sc1 lds\n"
"buffer_load_dwordx4 v0, s[12:15], s69 offen offset:1024 sc1 lds\n"
"buffer_load_dwordx4 v0, s[12:15], s69 offen offset:2048 sc1 lds\n"
"buffer_load_dwordx4 v0, s[12:15], s69 offen offset:3072 sc1 lds\n"
"s_waitcnt vmcnt(0) lgkmcnt(0)\n"
"ds_read_b128 v[4:7], v60 offset:16384\n"
"ds_read_b128 v[8:11], v60 offset:17408\n"
"ds_read_b128 v[12:15], v60 offset:18432\n"
"ds_read_b128 v[16:19], v60 offset:19456\n"
"s_waitcnt vmcnt(0) lgkmcnt(0)\n"
"v_bitop3_b32 v63, 1, v4, v6 bitop3:0x10\n"
"v_bitop3_b32 v63, v63, v8, v10 bitop3:0x10\n"
"v_bitop3_b32 v63, v63, v12, v14 bitop3:0x10\n"
"v_bitop3_b32 v63, v63, v16, v18 bitop3:0x10\n"
"v_cmp_eq_u32 vcc, 0, v63\n"
"s_nop 1\n"
"s_cbranch_vccnz LC_67\n"
"s_nop 7\n"
"v_xor_b32 v61, 64, v61\n"
"ds_read_b64_tr_b16 v[32:33], v61 offset:0\n"
"ds_read_b64_tr_b16 v[34:35], v61 offset:512\n"
"ds_read_b64_tr_b16 v[36:37], v61 offset:4096\n"
"ds_read_b64_tr_b16 v[38:39], v61 offset:4608\n"
"ds_read_b64_tr_b16 v[40:41], v61 offset:8192\n"
"ds_read_b64_tr_b16 v[42:43], v61 offset:8704\n"
"ds_read_b64_tr_b16 v[44:45], v61 offset:12288\n"
"ds_read_b64_tr_b16 v[46:47], v61 offset:12800\n"
"v_xor_b32 v61, 96, v61\n"
"ds_read_b64_tr_b16 v[48:49], v61 offset:0\n"
"ds_read_b64_tr_b16 v[50:51], v61 offset:512\n"
"s_waitcnt vmcnt(0) lgkmcnt(0)\n"
"s_branch LB_66\n"
"LC_69:\n"
"s_add_u32 s63, s63, 1\n"
"s_cmp_gt_u32 s63, 32768\n"
"s_cbranch_scc1 L_END\n"
"s_add_u32 m0, s58, 0\n"
"s_add_u32 s69, s51, 0xac4000\n"
"buffer_load_dwordx4 v0, s[12:15], s69 offen offset:0 sc1 lds\n"
"buffer_load_dwordx4 v0, s[12:15], s69 offen offset:1024 sc1 lds\n"
"buffer_load_dwordx4 v0, s[12:15], s69 offen offset:2048 sc1 lds\n"
"buffer_load_dwordx4 v0, s[12:15], s69 offen offset:3072 sc1 lds\n"
"s_waitcnt vmcnt(0) lgkmcnt(0)\n"
"ds_read_b128 v[4:7], v60 offset:0\n"
"ds_read_b128 v[8:11], v60 offset:1024\n"
"ds_read_b128 v[12:15], v60 offset:2048\n"
"ds_read_b128 v[16:19], v60 offset:3072\n"
"s_waitcnt vmcnt(0) lgkmcnt(0)\n"
"v_bitop3_b32 v63, 1, v4, v6 bitop3:0x10\n"
"v_bitop3_b32 v63, v63, v8, v10 bitop3:0x10\n"
"v_bitop3_b32 v63, v63, v12, v14 bitop3:0x10\n"
"v_bitop3_b32 v63, v63, v16, v18 bitop3:0x10\n"
"v_cmp_eq_u32 vcc, 0, v63\n"
"s_nop 1\n"
"s_cbranch_vccnz LC_69\n"
"s_nop 7\n"
"v_xor_b32 v61, 64, v61\n"
"ds_read_b64_tr_b16 v[32:33], v61 offset:0\n"
"ds_read_b64_tr_b16 v[34:35], v61 offset:512\n"
"ds_read_b64_tr_b16 v[36:37], v61 offset:4096\n"
"ds_read_b64_tr_b16 v[38:39], v61 offset:4608\n"
"ds_read_b64_tr_b16 v[40:41], v61 offset:8192\n"
"ds_read_b64_tr_b16 v[42:43], v61 offset:8704\n"
"ds_read_b64_tr_b16 v[44:45], v61 offset:12288\n"
"ds_read_b64_tr_b16 v[46:47], v61 offset:12800\n"
"v_xor_b32 v61, 96, v61\n"
"ds_read_b64_tr_b16 v[48:49], v61 offset:0\n"
"ds_read_b64_tr_b16 v[50:51], v61 offset:512\n"
"s_waitcnt vmcnt(0) lgkmcnt(0)\n"
"s_branch LB_68\n"
"Lf_70:\n"
"s_add_u32 s27, s27, 1\n"
"s_cmp_gt_u32 s27, 1048576\n"
"s_cbranch_scc1 L_END\n"
"ds_read_b32 v63, v3 offset:4\n"
"s_waitcnt lgkmcnt(0)\n"
"v_cmp_gt_u32 vcc, 15, v63\n"
"s_nop 1\n"
"s_and_b32 s68, vcc_lo, 15\n"
"s_cbranch_scc1 Lf_70\n"
"s_nop 4\n"
"s_branch Lg_71\n"
"LC_73:\n"
"s_add_u32 s63, s63, 1\n"
"s_cmp_gt_u32 s63, 32768\n"
"s_cbranch_scc1 L_END\n"
"s_add_u32 m0, s58, 0\n"
"s_add_u32 s69, s51, 0xac0000\n"
"buffer_load_dwordx4 v0, s[12:15], s69 offen offset:0 sc1 lds\n"
"buffer_load_dwordx4 v0, s[12:15], s69 offen offset:1024 sc1 lds\n"
"buffer_load_dwordx4 v0, s[12:15], s69 offen offset:2048 sc1 lds\n"
"buffer_load_dwordx4 v0, s[12:15], s69 offen offset:3072 sc1 lds\n"
"s_waitcnt vmcnt(0) lgkmcnt(0)\n"
"ds_read_b128 v[4:7], v60 offset:0\n"
"ds_read_b128 v[8:11], v60 offset:1024\n"
"ds_read_b128 v[12:15], v60 offset:2048\n"
"ds_read_b128 v[16:19], v60 offset:3072\n"
"s_waitcnt vmcnt(0) lgkmcnt(0)\n"
"v_bitop3_b32 v63, 1, v4, v6 bitop3:0x10\n"
"v_bitop3_b32 v63, v63, v8, v10 bitop3:0x10\n"
"v_bitop3_b32 v63, v63, v12, v14 bitop3:0x10\n"
"v_bitop3_b32 v63, v63, v16, v18 bitop3:0x10\n"
"v_cmp_eq_u32 vcc, 0, v63\n"
"s_nop 1\n"
"s_cbranch_vccnz LC_73\n"
"s_nop 7\n"
"v_xor_b32 v61, 64, v61\n"
"ds_read_b64_tr_b16 v[32:33], v61 offset:0\n"
"ds_read_b64_tr_b16 v[34:35], v61 offset:512\n"
"ds_read_b64_tr_b16 v[36:37], v61 offset:4096\n"
"ds_read_b64_tr_b16 v[38:39], v61 offset:4608\n"
"ds_read_b64_tr_b16 v[40:41], v61 offset:8192\n"
"ds_read_b64_tr_b16 v[42:43], v61 offset:8704\n"
"ds_read_b64_tr_b16 v[44:45], v61 offset:12288\n"
"ds_read_b64_tr_b16 v[46:47], v61 offset:12800\n"
"v_xor_b32 v61, 96, v61\n"
"ds_read_b64_tr_b16 v[48:49], v61 offset:0\n"
"ds_read_b64_tr_b16 v[50:51], v61 offset:512\n"
"s_waitcnt vmcnt(0) lgkmcnt(0)\n"
"s_branch LB_72\n"
"Lf_74:\n"
"s_add_u32 s27, s27, 1\n"
"s_cmp_gt_u32 s27, 1048576\n"
"s_cbranch_scc1 L_END\n"
"ds_read_b32 v63, v3 offset:4\n"
"s_waitcnt lgkmcnt(0)\n"
"v_cmp_gt_u32 vcc, 17, v63\n"
"s_nop 1\n"
"s_and_b32 s68, vcc_lo, 15\n"
"s_cbranch_scc1 Lf_74\n"
"s_nop 4\n"
"s_branch Lg_75\n"
"Lf_76:\n"
"s_add_u32 s27, s27, 1\n"
"s_cmp_gt_u32 s27, 1048576\n"
"s_cbranch_scc1 L_END\n"
"ds_read_b32 v63, v3 offset:0\n"
"s_waitcnt lgkmcnt(0)\n"
"v_cmp_gt_u32 vcc, 10, v63\n"
"s_nop 1\n"
"s_and_b32 s68, vcc_lo, 15\n"
"s_cbranch_scc1 Lf_76\n"
"s_nop 4\n"
"s_branch Lg_77\n"
"Lr_78:\n"
"s_add_u32 s63, s63, 1\n"
"s_cmp_gt_u32 s63, 32768\n"
"s_cbranch_scc1 L_END\n"
"s_sleep 4\n"
"s_lshl_b32 s68, s30, 10\n"
"s_add_u32 s68, s68, 0xef0000\n"
"v_bfe_u32 v52, v0, 4, 3\n"
"v_lshlrev_b32 v52, 7, v52\n"
"buffer_load_dwordx3 v[52:54], v52, s[12:15], s68 offen sc1\n"
"s_waitcnt vmcnt(0) lgkmcnt(0)\n"
"v_xor_b32 v52, s64, v52\n"
"v_xor_b32 v53, s65, v53\n"
"v_or_b32 v52, v52, v53\n"
"v_cmp_ne_u32 vcc, 0, v52\n"
"s_nop 1\n"
"s_cbranch_vccnz Lr_78\n"
"s_nop 4\n"
"s_branch Lk_79\n"
"Lr_82:\n"
"s_add_u32 s63, s63, 1\n"
"s_cmp_gt_u32 s63, 32768\n"
"s_cbranch_scc1 L_END\n"
"v_or_b32 v46, s60, v0\n"
"v_lshrrev_b32 v50, 1, v0\n"
"v_or_b32 v50, s60, v50\n"
"s_add_u32 s69, s54, 0x5f0000\n"
"buffer_load_dwordx4 v[46:49], v46, s[12:15], s69 offen sc1\n"
"buffer_load_dwordx2 v[50:51], v50, s[12:15], s69 offen offset:1024 sc1\n"
"s_waitcnt vmcnt(0) lgkmcnt(0)\n"
"v_xor_b32 v4, s64, v49\n"
"v_xor_b32 v5, s64, v51\n"
"v_or_b32 v4, v4, v5\n"
"v_cmp_ne_u32 vcc, 0, v4\n"
"s_cmp_lg_u32 s60, 0\n"
"s_nop 0\n"
"s_cselect_b64 vcc, 0, vcc\n"
"s_cbranch_vccnz Lr_82\n"
"s_nop 4\n"
"s_branch Lk_83\n"
"LC_85:\n"
"s_add_u32 s63, s63, 1\n"
"s_cmp_gt_u32 s63, 32768\n"
"s_cbranch_scc1 L_END\n"
"s_add_u32 m0, s58, 0\n"
"s_add_u32 s69, s51, 0xac0000\n"
"buffer_load_dwordx4 v0, s[12:15], s69 offen offset:0 sc1 lds\n"
"buffer_load_dwordx4 v0, s[12:15], s69 offen offset:1024 sc1 lds\n"
"buffer_load_dwordx4 v0, s[12:15], s69 offen offset:2048 sc1 lds\n"
"buffer_load_dwordx4 v0, s[12:15], s69 offen offset:3072 sc1 lds\n"
"s_waitcnt vmcnt(0) lgkmcnt(0)\n"
"ds_read_b128 v[4:7], v60 offset:0\n"
"ds_read_b128 v[8:11], v60 offset:1024\n"
"ds_read_b128 v[12:15], v60 offset:2048\n"
"ds_read_b128 v[16:19], v60 offset:3072\n"
"s_waitcnt vmcnt(0) lgkmcnt(0)\n"
"v_bitop3_b32 v63, 1, v4, v6 bitop3:0x80\n"
"v_bitop3_b32 v63, v63, v8, v10 bitop3:0x80\n"
"v_bitop3_b32 v63, v63, v12, v14 bitop3:0x80\n"
"v_bitop3_b32 v63, v63, v16, v18 bitop3:0x80\n"
"v_cmp_eq_u32 vcc, 0, v63\n"
"s_nop 1\n"
"s_cbranch_vccnz LC_85\n"
"s_nop 7\n"
"v_xor_b32 v61, 64, v61\n"
"ds_read_b64_tr_b16 v[32:33], v61 offset:0\n"
"ds_read_b64_tr_b16 v[34:35], v61 offset:512\n"
"ds_read_b64_tr_b16 v[36:37], v61 offset:4096\n"
"ds_read_b64_tr_b16 v[38:39], v61 offset:4608\n"
"ds_read_b64_tr_b16 v[40:41], v61 offset:8192\n"
"ds_read_b64_tr_b16 v[42:43], v61 offset:8704\n"
"ds_read_b64_tr_b16 v[44:45], v61 offset:12288\n"
"ds_read_b64_tr_b16 v[46:47], v61 offset:12800\n"
"v_xor_b32 v61, 96, v61\n"
"ds_read_b64_tr_b16 v[48:49], v61 offset:0\n"
"ds_read_b64_tr_b16 v[50:51], v61 offset:512\n"
"s_waitcnt vmcnt(0) lgkmcnt(0)\n"
"s_branch LB_84\n"
"Lr_86:\n"
"s_add_u32 s63, s63, 1\n"
"s_cmp_gt_u32 s63, 32768\n"
"s_cbranch_scc1 L_END\n"
"s_sleep 2\n"
"s_lshl_b32 s68, s31, 9\n"
"s_add_u32 s68, s68, 4096\n"
"v_and_b32 v50, 0x1f0, v0\n"
"buffer_load_dwordx2 v[50:51], v50, s[12:15], s68 offen sc1\n"
"s_waitcnt vmcnt(0) lgkmcnt(0)\n"
"v_xor_b32 v50, s64, v50\n"
"v_xor_b32 v51, s65, v51\n"
"v_or_b32 v50, v50, v51\n"
"v_cmp_ne_u32 vcc, 0, v50\n"
"s_nop 1\n"
"s_cbranch_vccnz Lr_86\n"
"s_nop 4\n"
"s_branch Lk_87\n"
"Lr_88:\n"
"s_add_u32 s63, s63, 1\n"
"s_cmp_gt_u32 s63, 32768\n"
"s_cbranch_scc1 L_END\n"
"s_sleep 4\n"
"s_lshl_b32 s68, s30, 10\n"
"s_add_u32 s68, s68, 0xec0000\n"
"v_bfe_u32 v52, v0, 4, 3\n"
"v_lshlrev_b32 v52, 7, v52\n"
"buffer_load_dwordx3 v[52:54], v52, s[12:15], s68 offen sc1\n"
"s_waitcnt vmcnt(0) lgkmcnt(0)\n"
"v_xor_b32 v52, s64, v52\n"
"v_xor_b32 v53, s65, v53\n"
"v_or_b32 v52, v52, v53\n"
"v_cmp_ne_u32 vcc, 0, v52\n"
"s_nop 1\n"
"s_cbranch_vccnz Lr_88\n"
"s_nop 4\n"
"s_branch Lk_89\n"
"Lr_92:\n"
"s_add_u32 s63, s63, 1\n"
"s_cmp_gt_u32 s63, 32768\n"
"s_cbranch_scc1 L_END\n"
"v_or_b32 v46, s60, v0\n"
"v_lshrrev_b32 v50, 1, v0\n"
"v_or_b32 v50, s60, v50\n"
"s_add_u32 s69, s54, 0xffff0000\n"
"buffer_load_dwordx4 v[46:49], v46, s[12:15], s69 offen sc1\n"
"buffer_load_dwordx2 v[50:51], v50, s[12:15], s69 offen offset:1024 sc1\n"
"s_waitcnt vmcnt(0) lgkmcnt(0)\n"
"v_xor_b32 v4, s64, v49\n"
"v_xor_b32 v5, s64, v51\n"
"v_or_b32 v4, v4, v5\n"
"v_cmp_ne_u32 vcc, 0, v4\n"
"s_cmp_lg_u32 s60, 0\n"
"s_nop 0\n"
"s_cselect_b64 vcc, 0, vcc\n"
"s_cbranch_vccnz Lr_92\n"
"s_nop 4\n"
"s_branch Lk_93\n"
"LQ_94:\n"
"s_add_u32 s63, s63, 1\n"
"s_cmp_gt_u32 s63, 32768\n"
"s_cbranch_scc1 L_END\n"
"s_add_u32 m0, s58, 49152\n"
"s_add_u32 s69, s51, 0xccc000\n"
"buffer_load_dwordx4 v0, s[12:15], s69 offen offset:0 sc1 lds\n"
"buffer_load_dwordx4 v0, s[12:15], s69 offen offset:1024 sc1 lds\n"
"buffer_load_dwordx4 v0, s[12:15], s69 offen offset:2048 sc1 lds\n"
"buffer_load_dwordx4 v0, s[12:15], s69 offen offset:3072 sc1 lds\n"
"s_waitcnt vmcnt(0) lgkmcnt(0)\n"
"ds_read_b128 v[32:35], v60 offset:49152\n"
"ds_read_b128 v[36:39], v60 offset:50176\n"
"ds_read_b128 v[40:43], v60 offset:51200\n"
"ds_read_b128 v[44:47], v60 offset:52224\n"
"s_waitcnt vmcnt(0) lgkmcnt(0)\n"
"v_bitop3_b32 v63, 1, v32, v34 bitop3:0x10\n"
"v_bitop3_b32 v63, v63, v36, v38 bitop3:0x10\n"
"v_bitop3_b32 v63, v63, v40, v42 bitop3:0x10\n"
"v_bitop3_b32 v63, v63, v44, v46 bitop3:0x10\n"
"v_cmp_eq_u32 vcc, 0, v63\n"
"s_nop 1\n"
"s_cbranch_vccnz LQ_94\n"
"v_mfma_f32_16x16x32_bf16 v[48:51], v[56:59], v[32:35], 0\n"
"v_mfma_f32_16x16x32_bf16 v[52:55], v[32:35], v[32:35], 0\n"
"v_mfma_f32_16x16x32_bf16 v[48:51], v[56:59], v[36:39], v[48:51]\n"
"v_mfma_f32_16x16x32_bf16 v[52:55], v[36:39], v[36:39], v[52:55]\n"
"v_mfma_f32_16x16x32_bf16 v[48:51], v[56:59], v[40:43], v[48:51]\n"
"v_mfma_f32_16x16x32_bf16 v[52:55], v[40:43], v[40:43], v[52:55]\n"
"v_mfma_f32_16x16x32_bf16 v[48:51], v[56:59], v[44:47], v[48:51]\n"
"v_mfma_f32_16x16x32_bf16 v[52:55], v[44:47], v[44:47], v[52:55]\n"
"v_mfma_f32_16x16x32_bf16 v[4:7], v[68:71], v[32:35], 0\n"
"v_mfma_f32_16x16x32_bf16 v[8:11], v[32:35], v[72:75], 0\n"
"v_mfma_f32_16x16x32_bf16 v[12:15], v[64:67], v[32:35], 0\n"
"s_nop 7\n"
"s_branch LP_95\n"
"Lf_96:\n"
"s_add_u32 s27, s27, 1\n"
"s_cmp_gt_u32 s27, 1048576\n"
"s_cbranch_scc1 L_END\n"
"ds_read_b32 v17, v3 offset:0\n"
"s_waitcnt lgkmcnt(0)\n"
"v_cmp_gt_u32 vcc, 3, v17\n"
"s_nop 1\n"
"s_and_b32 s68, vcc_lo, 15\n"
"s_cbranch_scc1 Lf_96\n"
"s_nop 4\n"
"s_branch Lg_97\n"
"LQ_98:\n"
"s_add_u32 s63, s63, 1\n"
"s_cmp_gt_u32 s63, 32768\n"
"s_cbranch_scc1 L_END\n"
"ds_read_b128 v[48:51], v3 offset:6656\n"
"ds_read_b128 v[52:55], v3 offset:6912\n"
"s_waitcnt lgkmcnt(0)\n"
"v_bitop3_b32 v63, 1, v48, v50 bitop3:0x80\n"
"v_bitop3_b32 v63, v63, v52, v54 bitop3:0x80\n"
"v_cmp_eq_u32 vcc, 0, v63\n"
"s_nop 1\n"
"s_cbranch_vccnz LQ_98\n"
"s_nop 7\n"
"s_branch LP_99\n"
"Lf_100:\n"
"s_add_u32 s27, s27, 1\n"
"s_cmp_gt_u32 s27, 1048576\n"
"s_cbranch_scc1 L_END\n"
"ds_read_b32 v20, v3 offset:4\n"
"s_waitcnt lgkmcnt(0)\n"
"v_cmp_gt_u32 vcc, 4, v20\n"
"s_nop 1\n"
"s_and_b32 s68, vcc_lo, 15\n"
"s_cbranch_scc1 Lf_100\n"
"s_nop 4\n"
"s_branch Lg_101\n"
"LQ_102:\n"
"s_add_u32 s63, s63, 1\n"
"s_cmp_gt_u32 s63, 32768\n"
"s_cbranch_scc1 L_END\n"
"s_add_u32 m0, s58, 32768\n"
"s_add_u32 s69, s51, 0xcc8000\n"
"buffer_load_dwordx4 v0, s[12:15], s69 offen offset:0 sc1 lds\n"
"buffer_load_dwordx4 v0, s[12:15], s69 offen offset:1024 sc1 lds\n"
"buffer_load_dwordx4 v0, s[12:15], s69 offen offset:2048 sc1 lds\n"
"buffer_load_dwordx4 v0, s[12:15], s69 offen offset:3072 sc1 lds\n"
"s_waitcnt vmcnt(0) lgkmcnt(0)\n"
"ds_read_b128 v[32:35], v60 offset:32768\n"
"ds_read_b128 v[36:39], v60 offset:33792\n"
"ds_read_b128 v[40:43], v60 offset:34816\n"
"ds_read_b128 v[44:47], v60 offset:35840\n"
"s_waitcnt vmcnt(0) lgkmcnt(0)\n"
"v_bitop3_b32 v63, 1, v32, v34 bitop3:0x10\n"
"v_bitop3_b32 v63, v63, v36, v38 bitop3:0x10\n"
"v_bitop3_b32 v63, v63, v40, v42 bitop3:0x10\n"
"v_bitop3_b32 v63, v63, v44, v46 bitop3:0x10\n"
"v_cmp_eq_u32 vcc, 0, v63\n"
"s_nop 1\n"
"s_cbranch_vccnz LQ_102\n"
"v_mfma_f32_16x16x32_bf16 v[48:51], v[56:59], v[32:35], 0\n"
"v_mfma_f32_16x16x32_bf16 v[52:55], v[32:35], v[32:35], 0\n"
"v_mfma_f32_16x16x32_bf16 v[48:51], v[56:59], v[36:39], v[48:51]\n"
"v_mfma_f32_16x16x32_bf16 v[52:55], v[36:39], v[36:39], v[52:55]\n"
"v_mfma_f32_16x16x32_bf16 v[48:51], v[56:59], v[40:43], v[48:51]\n"
"v_mfma_f32_16x16x32_bf16 v[52:55], v[40:43], v[40:43], v[52:55]\n"
"v_mfma_f32_16x16x32_bf16 v[48:51], v[56:59], v[44:47], v[48:51]\n"
"v_mfma_f32_16x16x32_bf16 v[52:55], v[44:47], v[44:47], v[52:55]\n"
"v_mfma_f32_16x16x32_bf16 v[24:27], v[68:71], v[32:35], 0\n"
"v_mfma_f32_16x16x32_bf16 v[20:23], v[32:35], v[72:75], 0\n"
"v_mfma_f32_16x16x32_bf16 v[28:31], v[64:67], v[32:35], 0\n"
"s_nop 7\n"
"s_branch LP_103\n"
"Lf_104:\n"
"s_add_u32 s27, s27, 1\n"
"s_cmp_gt_u32 s27, 1048576\n"
"s_cbranch_scc1 L_END\n"
"ds_read_b32 v17, v3 offset:0\n"
"s_waitcnt lgkmcnt(0)\n"
"v_cmp_gt_u32 vcc, 4, v17\n"
"s_nop 1\n"
"s_and_b32 s68, vcc_lo, 15\n"
"s_cbranch_scc1 Lf_104\n"
"s_nop 4\n"
"s_branch Lg_105\n"
"LQ_106:\n"
"s_add_u32 s63, s63, 1\n"
"s_cmp_gt_u32 s63, 32768\n"
"s_cbranch_scc1 L_END\n"
"ds_read_b128 v[48:51], v3 offset:6144\n"
"ds_read_b128 v[52:55], v3 offset:6400\n"
"s_waitcnt lgkmcnt(0)\n"
"v_bitop3_b32 v63, 1, v48, v50 bitop3:0x80\n"
"v_bitop3_b32 v63, v63, v52, v54 bitop3:0x80\n"
"v_cmp_eq_u32 vcc, 0, v63\n"
"s_nop 1\n"
"s_cbranch_vccnz LQ_106\n"
"s_nop 7\n"
"s_branch LP_107\n"
"LQ_108:\n"
"s_add_u32 s63, s63, 1\n"
"s_cmp_gt_u32 s63, 32768\n"
"s_cbranch_scc1 L_END\n"
"s_add_u32 m0, s58, 16384\n"
"s_add_u32 s69, s51, 0xcc4000\n"
"buffer_load_dwordx4 v0, s[12:15], s69 offen offset:0 sc1 lds\n"
"buffer_load_dwordx4 v0, s[12:15], s69 offen offset:1024 sc1 lds\n"
"buffer_load_dwordx4 v0, s[12:15], s69 offen offset:2048 sc1 lds\n"
"buffer_load_dwordx4 v0, s[12:15], s69 offen offset:3072 sc1 lds\n"
"s_waitcnt vmcnt(0) lgkmcnt(0)\n"
"ds_read_b128 v[32:35], v60 offset:16384\n"
"ds_read_b128 v[36:39], v60 offset:17408\n"
"ds_read_b128 v[40:43], v60 offset:18432\n"
"ds_read_b128 v[44:47], v60 offset:19456\n"
"s_waitcnt vmcnt(0) lgkmcnt(0)\n"
"v_bitop3_b32 v63, 1, v32, v34 bitop3:0x10\n"
"v_bitop3_b32 v63, v63, v36, v38 bitop3:0x10\n"
"v_bitop3_b32 v63, v63, v40, v42 bitop3:0x10\n"
"v_bitop3_b32 v63, v63, v44, v46 bitop3:0x10\n"
"v_cmp_eq_u32 vcc, 0, v63\n"
"s_nop 1\n"
"s_cbranch_vccnz LQ_108\n"
"v_mfma_f32_16x16x32_bf16 v[48:51], v[56:59], v[32:35], 0\n"
"v_mfma_f32_16x16x32_bf16 v[52:55], v[32:35], v[32:35], 0\n"
"v_mfma_f32_16x16x32_bf16 v[48:51], v[56:59], v[36:39], v[48:51]\n"
"v_mfma_f32_16x16x32_bf16 v[52:55], v[36:39], v[36:39], v[52:55]\n"
"v_mfma_f32_16x16x32_bf16 v[48:51], v[56:59], v[40:43], v[48:51]\n"
"v_mfma_f32_16x16x32_bf16 v[52:55], v[40:43], v[40:43], v[52:55]\n"
"v_mfma_f32_16x16x32_bf16 v[48:51], v[56:59], v[44:47], v[48:51]\n"
"v_mfma_f32_16x16x32_bf16 v[52:55], v[44:47], v[44:47], v[52:55]\n"
"v_mfma_f32_16x16x32_bf16 v[4:7], v[68:71], v[32:35], 0\n"
"v_mfma_f32_16x16x32_bf16 v[8:11], v[32:35], v[72:75], 0\n"
"v_mfma_f32_16x16x32_bf16 v[12:15], v[64:67], v[32:35], 0\n"
"s_nop 7\n"
"s_branch LP_109\n"
"LQ_110:\n"
"s_add_u32 s63, s63, 1\n"
"s_cmp_gt_u32 s63, 32768\n"
"s_cbranch_scc1 L_END\n"
"ds_read_b128 v[48:51], v3 offset:5632\n"
"ds_read_b128 v[52:55], v3 offset:5888\n"
"s_waitcnt lgkmcnt(0)\n"
"v_bitop3_b32 v63, 1, v48, v50 bitop3:0x80\n"
"v_bitop3_b32 v63, v63, v52, v54 bitop3:0x80\n"
"v_cmp_eq_u32 vcc, 0, v63\n"
"s_nop 1\n"
"s_cbranch_vccnz LQ_110\n"
"s_nop 7\n"
"s_branch LP_111\n"
"LQ_112:\n"
"s_add_u32 s63, s63, 1\n"
"s_cmp_gt_u32 s63, 32768\n"
"s_cbranch_scc1 L_END\n"
"s_add_u32 m0, s58, 0\n"
"s_add_u32 s69, s51, 0xcc0000\n"
"buffer_load_dwordx4 v0, s[12:15], s69 offen offset:0 sc1 lds\n"
"buffer_load_dwordx4 v0, s[12:15], s69 offen offset:1024 sc1 lds\n"
"buffer_load_dwordx4 v0, s[12:15], s69 offen offset:2048 sc1 lds\n"
"buffer_load_dwordx4 v0, s[12:15], s69 offen offset:3072 sc1 lds\n"
"s_waitcnt vmcnt(0) lgkmcnt(0)\n"
"ds_read_b128 v[32:35], v60 offset:0\n"
"ds_read_b128 v[36:39], v60 offset:1024\n"
"ds_read_b128 v[40:43], v60 offset:2048\n"
"ds_read_b128 v[44:47], v60 offset:3072\n"
"s_waitcnt vmcnt(0) lgkmcnt(0)\n"
"v_bitop3_b32 v63, 1, v32, v34 bitop3:0x10\n"
"v_bitop3_b32 v63, v63, v36, v38 bitop3:0x10\n"
"v_bitop3_b32 v63, v63, v40, v42 bitop3:0x10\n"
"v_bitop3_b32 v63, v63, v44, v46 bitop3:0x10\n"
"v_cmp_eq_u32 vcc, 0, v63\n"
"s_nop 1\n"
"s_cbranch_vccnz LQ_112\n"
"v_mfma_f32_16x16x32_bf16 v[48:51], v[56:59], v[32:35], 0\n"
"v_mfma_f32_16x16x32_bf16 v[52:55], v[32:35], v[32:35], 0\n"
"v_mfma_f32_16x16x32_bf16 v[48:51], v[56:59], v[36:39], v[48:51]\n"
"v_mfma_f32_16x16x32_bf16 v[52:55], v[36:39], v[36:39], v[52:55]\n"
"v_mfma_f32_16x16x32_bf16 v[48:51], v[56:59], v[40:43], v[48:51]\n"
"v_mfma_f32_16x16x32_bf16 v[52:55], v[40:43], v[40:43], v[52:55]\n"
"v_mfma_f32_16x16x32_bf16 v[48:51], v[56:59], v[44:47], v[48:51]\n"
"v_mfma_f32_16x16x32_bf16 v[52:55], v[44:47], v[44:47], v[52:55]\n"
"v_mfma_f32_16x16x32_bf16 v[24:27], v[68:71], v[32:35], 0\n"
"v_mfma_f32_16x16x32_bf16 v[20:23], v[32:35], v[72:75], 0\n"
"v_mfma_f32_16x16x32_bf16 v[28:31], v[64:67], v[32:35], 0\n"
"s_nop 7\n"
"s_branch LP_113\n"
"LQ_114:\n"
"s_add_u32 s63, s63, 1\n"
"s_cmp_gt_u32 s63, 32768\n"
"s_cbranch_scc1 L_END\n"
"ds_read_b128 v[48:51], v3 offset:5120\n"
"ds_read_b128 v[52:55], v3 offset:5376\n"
"s_waitcnt lgkmcnt(0)\n"
"v_bitop3_b32 v63, 1, v48, v50 bitop3:0x80\n"
"v_bitop3_b32 v63, v63, v52, v54 bitop3:0x80\n"
"v_cmp_eq_u32 vcc, 0, v63\n"
"s_nop 1\n"
"s_cbranch_vccnz LQ_114\n"
"s_nop 7\n"
"s_branch LP_115\n"
"LC_117:\n"
"s_add_u32 s63, s63, 1\n"
"s_cmp_gt_u32 s63, 32768\n"
"s_cbranch_scc1 L_END\n"
"s_add_u32 s69, s51, 0xacc000\n"
"v_bitop3_b32 v63, 1, v4, v6 bitop3:0x80\n"
"v_cmp_eq_u32 vcc, 0, v63\n"
"s_nop 1\n"
"s_cbranch_vccz Lz_118\n"
"buffer_load_dwordx4 v[4:7], v0, s[12:15], s69 offen offset:0 sc1\n"
"Lz_118:\n"
"v_bitop3_b32 v63, 1, v8, v10 bitop3:0x80\n"
"v_cmp_eq_u32 vcc, 0, v63\n"
"s_nop 1\n"
"s_cbranch_vccz Lz_119\n"
"buffer_load_dwordx4 v[8:11], v0, s[12:15], s69 offen offset:1024 sc1\n"
"Lz_119:\n"
"v_bitop3_b32 v63, 1, v12, v14 bitop3:0x80\n"
"v_cmp_eq_u32 vcc, 0, v63\n"
"s_nop 1\n"
"s_cbranch_vccz Lz_120\n"
"buffer_load_dwordx4 v[12:15], v0, s[12:15], s69 offen offset:2048 sc1\n"
"Lz_120:\n"
"v_bitop3_b32 v63, 1, v16, v18 bitop3:0x80\n"
"v_cmp_eq_u32 vcc, 0, v63\n"
"s_nop 1\n"
"s_cbranch_vccz Lz_121\n"
"buffer_load_dwordx4 v[16:19], v0, s[12:15], s69 offen offset:3072 sc1\n"
"Lz_121:\n"
"s_waitcnt vmcnt(0) lgkmcnt(0)\n"
"v_bitop3_b32 v63, 1, v4, v6 bitop3:0x80\n"
"v_bitop3_b32 v63, v63, v8, v10 bitop3:0x80\n"
"v_bitop3_b32 v63, v63, v12, v14 bitop3:0x80\n"
"v_bitop3_b32 v63, v63, v16, v18 bitop3:0x80\n"
"v_cmp_eq_u32 vcc, 0, v63\n"
"s_nop 1\n"
"s_cbranch_vccnz LC_117\n"
"s_nop 7\n"
"v_xor_b32 v61, 64, v61\n"
"ds_read_b64_tr_b16 v[32:33], v61 offset:0\n"
"ds_read_b64_tr_b16 v[34:35], v61 offset:512\n"
"ds_read_b64_tr_b16 v[36:37], v61 offset:4096\n"
"ds_read_b64_tr_b16 v[38:39], v61 offset:4608\n"
"ds_read_b64_tr_b16 v[40:41], v61 offset:8192\n"
"ds_read_b64_tr_b16 v[42:43], v61 offset:8704\n"
"ds_read_b64_tr_b16 v[44:45], v61 offset:12288\n"
"ds_read_b64_tr_b16 v[46:47], v61 offset:12800\n"
"v_xor_b32 v61, 96, v61\n"
"ds_read_b64_tr_b16 v[48:49], v61 offset:0\n"
"ds_read_b64_tr_b16 v[50:51], v61 offset:512\n"
"s_waitcnt vmcnt(0) lgkmcnt(0)\n"
"s_branch LB_116\n"
"LC_123:\n"
"s_add_u32 s63, s63, 1\n"
"s_cmp_gt_u32 s63, 32768\n"
"s_cbranch_scc1 L_END\n"
"s_add_u32 m0, s58, 16384\n"
"s_add_u32 s69, s51, 0xac8000\n"
"buffer_load_dwordx4 v0, s[12:15], s69 offen offset:0 sc1 lds\n"
"buffer_load_dwordx4 v0, s[12:15], s69 offen offset:1024 sc1 lds\n"
"buffer_load_dwordx4 v0, s[12:15], s69 offen offset:2048 sc1 lds\n"
"buffer_load_dwordx4 v0, s[12:15], s69 offen offset:3072 sc1 lds\n"
"s_waitcnt vmcnt(0) lgkmcnt(0)\n"
"ds_read_b128 v[4:7], v60 offset:16384\n"
"ds_read_b128 v[8:11], v60 offset:17408\n"
"ds_read_b128 v[12:15], v60 offset:18432\n"
"ds_read_b128 v[16:19], v60 offset:19456\n"
"s_waitcnt vmcnt(0) lgkmcnt(0)\n"
"v_bitop3_b32 v63, 1, v4, v6 bitop3:0x80\n"
"v_bitop3_b32 v63, v63, v8, v10 bitop3:0x80\n"
"v_bitop3_b32 v63, v63, v12, v14 bitop3:0x80\n"
"v_bitop3_b32 v63, v63, v16, v18 bitop3:0x80\n"
"v_cmp_eq_u32 vcc, 0, v63\n"
"s_nop 1\n"
"s_cbranch_vccnz LC_123\n"
"s_nop 7\n"
"v_xor_b32 v61, 64, v61\n"
"ds_read_b64_tr_b16 v[32:33], v61 offset:0\n"
"ds_read_b64_tr_b16 v[34:35], v61 offset:512\n"
"ds_read_b64_tr_b16 v[36:37], v61 offset:4096\n"
"ds_read_b64_tr_b16 v[38:39], v61 offset:4608\n"
"ds_read_b64_tr_b16 v[40:41], v61 offset:8192\n"
"ds_read_b64_tr_b16 v[42:43], v61 offset:8704\n"
"ds_read_b64_tr_b16 v[44:45], v61 offset:12288\n"
"ds_read_b64_tr_b16 v[46:47], v61 offset:12800\n"
"v_xor_b32 v61, 96, v61\n"
"ds_read_b64_tr_b16 v[48:49], v61 offset:0\n"
"ds_read_b64_tr_b16 v[50:51], v61 offset:512\n"
"s_waitcnt vmcnt(0) lgkmcnt(0)\n"
"s_branch LB_122\n"
"LC_125:\n"
"s_add_u32 s63, s63, 1\n"
"s_cmp_gt_u32 s63, 32768\n"
"s_cbranch_scc1 L_END\n"
"s_add_u32 m0, s58, 0\n"
"s_add_u32 s69, s51, 0xac4000\n"
"buffer_load_dwordx4 v0, s[12:15], s69 offen offset:0 sc1 lds\n"
"buffer_load_dwordx4 v0, s[12:15], s69 offen offset:1024 sc1 lds\n"
"buffer_load_dwordx4 v0, s[12:15], s69 offen offset:2048 sc1 lds\n"
"buffer_load_dwordx4 v0, s[12:15], s69 offen offset:3072 sc1 lds\n"
"s_waitcnt vmcnt(0) lgkmcnt(0)\n"
"ds_read_b128 v[4:7], v60 offset:0\n"
"ds_read_b128 v[8:11], v60 offset:1024\n"
"ds_read_b128 v[12:15], v60 offset:2048\n"
"ds_read_b128 v[16:19], v60 offset:3072\n"
"s_waitcnt vmcnt(0) lgkmcnt(0)\n"
"v_bitop3_b32 v63, 1, v4, v6 bitop3:0x80\n"
"v_bitop3_b32 v63, v63, v8, v10 bitop3:0x80\n"
"v_bitop3_b32 v63, v63, v12, v14 bitop3:0x80\n"
"v_bitop3_b32 v63, v63, v16, v18 bitop3:0x80\n"
"v_cmp_eq_u32 vcc, 0, v63\n"
"s_nop 1\n"
"s_cbranch_vccnz LC_125\n"
"s_nop 7\n"
"v_xor_b32 v61, 64, v61\n"
"ds_read_b64_tr_b16 v[32:33], v61 offset:0\n"
"ds_read_b64_tr_b16 v[34:35], v61 offset:512\n"
"ds_read_b64_tr_b16 v[36:37], v61 offset:4096\n"
"ds_read_b64_tr_b16 v[38:39], v61 offset:4608\n"
"ds_read_b64_tr_b16 v[40:41], v61 offset:8192\n"
"ds_read_b64_tr_b16 v[42:43], v61 offset:8704\n"
"ds_read_b64_tr_b16 v[44:45], v61 offset:12288\n"
"ds_read_b64_tr_b16 v[46:47], v61 offset:12800\n"
"v_xor_b32 v61, 96, v61\n"
"ds_read_b64_tr_b16 v[48:49], v61 offset:0\n"
"ds_read_b64_tr_b16 v[50:51], v61 offset:512\n"
"s_waitcnt vmcnt(0) lgkmcnt(0)\n"
"s_branch LB_124\n"
"Lf_126:\n"
"s_add_u32 s27, s27, 1\n"
"s_cmp_gt_u32 s27, 1048576\n"
"s_cbranch_scc1 L_END\n"
"ds_read_b32 v63, v3 offset:4\n"
"s_waitcnt lgkmcnt(0)\n"
"v_cmp_gt_u32 vcc, 9, v63\n"
"s_nop 1\n"
"s_and_b32 s68, vcc_lo, 15\n"
"s_cbranch_scc1 Lf_126\n"
"s_nop 4\n"
"s_branch Lg_127\n"
"LC_129:\n"
"s_add_u32 s63, s63, 1\n"
"s_cmp_gt_u32 s63, 32768\n"
"s_cbranch_scc1 L_END\n"
"s_add_u32 m0, s58, 0\n"
"s_add_u32 s69, s51, 0xac0000\n"
"buffer_load_dwordx4 v0, s[12:15], s69 offen offset:0 sc1 lds\n"
"buffer_load_dwordx4 v0, s[12:15], s69 offen offset:1024 sc1 lds\n"
"buffer_load_dwordx4 v0, s[12:15], s69 offen offset:2048 sc1 lds\n"
"buffer_load_dwordx4 v0, s[12:15], s69 offen offset:3072 sc1 lds\n"
"s_waitcnt vmcnt(0) lgkmcnt(0)\n"
"ds_read_b128 v[4:7], v60 offset:0\n"
"ds_read_b128 v[8:11], v60 offset:1024\n"
"ds_read_b128 v[12:15], v60 offset:2048\n"
"ds_read_b128 v[16:19], v60 offset:3072\n"
"s_waitcnt vmcnt(0) lgkmcnt(0)\n"
"v_bitop3_b32 v63, 1, v4, v6 bitop3:0x80\n"
"v_bitop3_b32 v63, v63, v8, v10 bitop3:0x80\n"
"v_bitop3_b32 v63, v63, v12, v14 bitop3:0x80\n"
"v_bitop3_b32 v63, v63, v16, v18 bitop3:0x80\n"
"v_cmp_eq_u32 vcc, 0, v63\n"
"s_nop 1\n"
"s_cbranch_vccnz LC_129\n"
"s_nop 7\n"
"v_xor_b32 v61, 64, v61\n"
"ds_read_b64_tr_b16 v[32:33], v61 offset:0\n"
"ds_read_b64_tr_b16 v[34:35], v61 offset:512\n"
"ds_read_b64_tr_b16 v[36:37], v61 offset:4096\n"
"ds_read_b64_tr_b16 v[38:39], v61 offset:4608\n"
"ds_read_b64_tr_b16 v[40:41], v61 offset:8192\n"
"ds_read_b64_tr_b16 v[42:43], v61 offset:8704\n"
"ds_read_b64_tr_b16 v[44:45], v61 offset:12288\n"
"ds_read_b64_tr_b16 v[46:47], v61 offset:12800\n"
"v_xor_b32 v61, 96, v61\n"
"ds_read_b64_tr_b16 v[48:49], v61 offset:0\n"
"ds_read_b64_tr_b16 v[50:51], v61 offset:512\n"
"s_waitcnt vmcnt(0) lgkmcnt(0)\n"
"s_branch LB_128\n"
"Lf_130:\n"
"s_add_u32 s27, s27, 1\n"
"s_cmp_gt_u32 s27, 1048576\n"
"s_cbranch_scc1 L_END\n"
"ds_read_b32 v63, v3 offset:4\n"
"s_waitcnt lgkmcnt(0)\n"
"v_cmp_gt_u32 vcc, 11, v63\n"
"s_nop 1\n"
"s_and_b32 s68, vcc_lo, 15\n"
"s_cbranch_scc1 Lf_130\n"
"s_nop 4\n"
"s_branch Lg_131\n"
"Lf_132:\n"
"s_add_u32 s27, s27, 1\n"
"s_cmp_gt_u32 s27, 1048576\n"
"s_cbranch_scc1 L_END\n"
"ds_read_b32 v63, v3 offset:0\n"
"s_waitcnt lgkmcnt(0)\n"
"v_cmp_gt_u32 vcc, 7, v63\n"
"s_nop 1\n"
"s_and_b32 s68, vcc_lo, 15\n"
"s_cbranch_scc1 Lf_132\n"
"s_nop 4\n"
"s_branch Lg_133\n"
"Lr_134:\n"
"s_add_u32 s63, s63, 1\n"
"s_cmp_gt_u32 s63, 32768\n"
"s_cbranch_scc1 L_END\n"
"s_sleep 4\n"
"s_lshl_b32 s68, s30, 10\n"
"s_add_u32 s68, s68, 0xee0000\n"
"v_bfe_u32 v52, v0, 4, 3\n"
"v_lshlrev_b32 v52, 7, v52\n"
"buffer_load_dwordx3 v[52:54], v52, s[12:15], s68 offen sc1\n"
"s_waitcnt vmcnt(0) lgkmcnt(0)\n"
"v_xor_b32 v52, s64, v52\n"
"v_xor_b32 v53, s65, v53\n"
"v_or_b32 v52, v52, v53\n"
"v_cmp_ne_u32 vcc, 0, v52\n"
"s_nop 1\n"
"s_cbranch_vccnz Lr_134\n"
"s_nop 4\n"
"s_branch Lk_135\n"
"Lr_138:\n"
"s_add_u32 s63, s63, 1\n"
"s_cmp_gt_u32 s63, 32768\n"
"s_cbranch_scc1 L_END\n"
"v_or_b32 v46, s60, v0\n"
"v_lshrrev_b32 v50, 1, v0\n"
"v_or_b32 v50, s60, v50\n"
"s_add_u32 s69, s54, 0x3f0000\n"
"buffer_load_dwordx4 v[46:49], v46, s[12:15], s69 offen sc1\n"
"buffer_load_dwordx2 v[50:51], v50, s[12:15], s69 offen offset:1024 sc1\n"
"s_waitcnt vmcnt(0) lgkmcnt(0)\n"
"v_xor_b32 v4, s64, v49\n"
"v_xor_b32 v5, s64, v51\n"
"v_or_b32 v4, v4, v5\n"
"v_cmp_ne_u32 vcc, 0, v4\n"
"s_cmp_lg_u32 s60, 0\n"
"s_nop 0\n"
"s_cselect_b64 vcc, 0, vcc\n"
"s_cbranch_vccnz Lr_138\n"
"s_nop 4\n"
"s_branch Lk_139\n"
"LQ_140:\n"
"s_add_u32 s63, s63, 1\n"
"s_cmp_gt_u32 s63, 32768\n"
"s_cbranch_scc1 L_END\n"
"s_add_u32 m0, s58, 49152\n"
"s_add_u32 s69, s51, 0xccc000\n"
"buffer_load_dwordx4 v0, s[12:15], s69 offen offset:0 sc1 lds\n"
"buffer_load_dwordx4 v0, s[12:15], s69 offen offset:1024 sc1 lds\n"
"buffer_load_dwordx4 v0, s[12:15], s69 offen offset:2048 sc1 lds\n"
"buffer_load_dwordx4 v0, s[12:15], s69 offen offset:3072 sc1 lds\n"
"s_waitcnt vmcnt(0) lgkmcnt(0)\n"
"ds_read_b128 v[32:35], v60 offset:49152\n"
"ds_read_b128 v[36:39], v60 offset:50176\n"
"ds_read_b128 v[40:43], v60 offset:51200\n"
"ds_read_b128 v[44:47], v60 offset:52224\n"
"s_waitcnt vmcnt(0) lgkmcnt(0)\n"
"v_bitop3_b32 v63, 1, v32, v34 bitop3:0x10\n"
"v_bitop3_b32 v63, v63, v36, v38 bitop3:0x10\n"
"v_bitop3_b32 v63, v63, v40, v42 bitop3:0x10\n"
"v_bitop3_b32 v63, v63, v44, v46 bitop3:0x10\n"
"v_cmp_eq_u32 vcc, 0, v63\n"
"s_nop 1\n"
"s_cbranch_vccnz LQ_140\n"
"v_mfma_f32_16x16x32_bf16 v[48:51], v[56:59], v[32:35], 0\n"
"v_mfma_f32_16x16x32_bf16 v[52:55], v[32:35], v[32:35], 0\n"
"v_mfma_f32_16x16x32_bf16 v[48:51], v[56:59], v[36:39], v[48:51]\n"
"v_mfma_f32_16x16x32_bf16 v[52:55], v[36:39], v[36:39], v[52:55]\n"
"v_mfma_f32_16x16x32_bf16 v[48:51], v[56:59], v[40:43], v[48:51]\n"
"v_mfma_f32_16x16x32_bf16 v[52:55], v[40:43], v[40:43], v[52:55]\n"
"v_mfma_f32_16x16x32_bf16 v[48:51], v[56:59], v[44:47], v[48:51]\n"
"v_mfma_f32_16x16x32_bf16 v[52:55], v[44:47], v[44:47], v[52:55]\n"
"v_mfma_f32_16x16x32_bf16 v[4:7], v[68:71], v[32:35], 0\n"
"v_mfma_f32_16x16x32_bf16 v[8:11], v[32:35], v[72:75], 0\n"
"v_mfma_f32_16x16x32_bf16 v[12:15], v[64:67], v[32:35], 0\n"
"s_nop 7\n"
"s_branch LP_141\n"
"Lf_142:\n"
"s_add_u32 s27, s27, 1\n"
"s_cmp_gt_u32 s27, 1048576\n"
"s_cbranch_scc1 L_END\n"
"ds_read_b32 v17, v3 offset:0\n"
"s_waitcnt lgkmcnt(0)\n"
"v_cmp_gt_u32 vcc, 9, v17\n"
"s_nop 1\n"
"s_and_b32 s68, vcc_lo, 15\n"
"s_cbranch_scc1 Lf_142\n"
"s_nop 4\n"
"s_branch Lg_143\n"
"LQ_144:\n"
"s_add_u32 s63, s63, 1\n"
"s_cmp_gt_u32 s63, 32768\n"
"s_cbranch_scc1 L_END\n"
"ds_read_b128 v[48:51], v3 offset:6656\n"
"ds_read_b128 v[52:55], v3 offset:6912\n"
"s_waitcnt lgkmcnt(0)\n"
"v_bitop3_b32 v63, 1, v48, v50 bitop3:0x80\n"
"v_bitop3_b32 v63, v63, v52, v54 bitop3:0x80\n"
"v_cmp_eq_u32 vcc, 0, v63\n"
"s_nop 1\n"
"s_cbranch_vccnz LQ_144\n"
"s_nop 7\n"
"s_branch LP_145\n"
"Lf_146:\n"
"s_add_u32 s27, s27, 1\n"
"s_cmp_gt_u32 s27, 1048576\n"
"s_cbranch_scc1 L_END\n"
"ds_read_b32 v20, v3 offset:4\n"
"s_waitcnt lgkmcnt(0)\n"
"v_cmp_gt_u32 vcc, 16, v20\n"
"s_nop 1\n"
"s_and_b32 s68, vcc_lo, 15\n"
"s_cbranch_scc1 Lf_146\n"
"s_nop 4\n"
"s_branch Lg_147\n"
"LQ_148:\n"
"s_add_u32 s63, s63, 1\n"
"s_cmp_gt_u32 s63, 32768\n"
"s_cbranch_scc1 L_END\n"
"s_add_u32 m0, s58, 32768\n"
"s_add_u32 s69, s51, 0xcc8000\n"
"buffer_load_dwordx4 v0, s[12:15], s69 offen offset:0 sc1 lds\n"
"buffer_load_dwordx4 v0, s[12:15], s69 offen offset:1024 sc1 lds\n"
"buffer_load_dwordx4 v0, s[12:15], s69 offen offset:2048 sc1 lds\n"
"buffer_load_dwordx4 v0, s[12:15], s69 offen offset:3072 sc1 lds\n"
"s_waitcnt vmcnt(0) lgkmcnt(0)\n"
"ds_read_b128 v[32:35], v60 offset:32768\n"
"ds_read_b128 v[36:39], v60 offset:33792\n"
"ds_read_b128 v[40:43], v60 offset:34816\n"
"ds_read_b128 v[44:47], v60 offset:35840\n"
"s_waitcnt vmcnt(0) lgkmcnt(0)\n"
"v_bitop3_b32 v63, 1, v32, v34 bitop3:0x10\n"
"v_bitop3_b32 v63, v63, v36, v38 bitop3:0x10\n"
"v_bitop3_b32 v63, v63, v40, v42 bitop3:0x10\n"
"v_bitop3_b32 v63, v63, v44, v46 bitop3:0x10\n"
"v_cmp_eq_u32 vcc, 0, v63\n"
"s_nop 1\n"
"s_cbranch_vccnz LQ_148\n"
"v_mfma_f32_16x16x32_bf16 v[48:51], v[56:59], v[32:35], 0\n"
"v_mfma_f32_16x16x32_bf16 v[52:55], v[32:35], v[32:35], 0\n"
"v_mfma_f32_16x16x32_bf16 v[48:51], v[56:59], v[36:39], v[48:51]\n"
"v_mfma_f32_16x16x32_bf16 v[52:55], v[36:39], v[36:39], v[52:55]\n"
"v_mfma_f32_16x16x32_bf16 v[48:51], v[56:59], v[40:43], v[48:51]\n"
"v_mfma_f32_16x16x32_bf16 v[52:55], v[40:43], v[40:43], v[52:55]\n"
"v_mfma_f32_16x16x32_bf16 v[48:51], v[56:59], v[44:47], v[48:51]\n"
"v_mfma_f32_16x16x32_bf16 v[52:55], v[44:47], v[44:47], v[52:55]\n"
"v_mfma_f32_16x16x32_bf16 v[24:27], v[68:71], v[32:35], 0\n"
"v_mfma_f32_16x16x32_bf16 v[20:23], v[32:35], v[72:75], 0\n"
"v_mfma_f32_16x16x32_bf16 v[28:31], v[64:67], v[32:35], 0\n"
"s_nop 7\n"
"s_branch LP_149\n"
"Lf_150:\n"
"s_add_u32 s27, s27, 1\n"
"s_cmp_gt_u32 s27, 1048576\n"
"s_cbranch_scc1 L_END\n"
"ds_read_b32 v17, v3 offset:0\n"
"s_waitcnt lgkmcnt(0)\n"
"v_cmp_gt_u32 vcc, 10, v17\n"
"s_nop 1\n"
"s_and_b32 s68, vcc_lo, 15\n"
"s_cbranch_scc1 Lf_150\n"
"s_nop 4\n"
"s_branch Lg_151\n"
"LQ_152:\n"
"s_add_u32 s63, s63, 1\n"
"s_cmp_gt_u32 s63, 32768\n"
"s_cbranch_scc1 L_END\n"
"ds_read_b128 v[48:51], v3 offset:6144\n"
"ds_read_b128 v[52:55], v3 offset:6400\n"
"s_waitcnt lgkmcnt(0)\n"
"v_bitop3_b32 v63, 1, v48, v50 bitop3:0x80\n"
"v_bitop3_b32 v63, v63, v52, v54 bitop3:0x80\n"
"v_cmp_eq_u32 vcc, 0, v63\n"
"s_nop 1\n"
"s_cbranch_vccnz LQ_152\n"
"s_nop 7\n"
"s_branch LP_153\n"
"LQ_154:\n"
"s_add_u32 s63, s63, 1\n"
"s_cmp_gt_u32 s63, 32768\n"
"s_cbranch_scc1 L_END\n"
"s_add_u32 m0, s58, 16384\n"
"s_add_u32 s69, s51, 0xcc4000\n"
"buffer_load_dwordx4 v0, s[12:15], s69 offen offset:0 sc1 lds\n"
"buffer_load_dwordx4 v0, s[12:15], s69 offen offset:1024 sc1 lds\n"
"buffer_load_dwordx4 v0, s[12:15], s69 offen offset:2048 sc1 lds\n"
"buffer_load_dwordx4 v0, s[12:15], s69 offen offset:3072 sc1 lds\n"
"s_waitcnt vmcnt(0) lgkmcnt(0)\n"
"ds_read_b128 v[32:35], v60 offset:16384\n"
"ds_read_b128 v[36:39], v60 offset:17408\n"
"ds_read_b128 v[40:43], v60 offset:18432\n"
"ds_read_b128 v[44:47], v60 offset:19456\n"
"s_waitcnt vmcnt(0) lgkmcnt(0)\n"
"v_bitop3_b32 v63, 1, v32, v34 bitop3:0x10\n"
"v_bitop3_b32 v63, v63, v36, v38 bitop3:0x10\n"
"v_bitop3_b32 v63, v63, v40, v42 bitop3:0x10\n"
"v_bitop3_b32 v63, v63, v44, v46 bitop3:0x10\n"
"v_cmp_eq_u32 vcc, 0, v63\n"
"s_nop 1\n"
"s_cbranch_vccnz LQ_154\n"
"v_mfma_f32_16x16x32_bf16 v[48:51], v[56:59], v[32:35], 0\n"
"v_mfma_f32_16x16x32_bf16 v[52:55], v[32:35], v[32:35], 0\n"
"v_mfma_f32_16x16x32_bf16 v[48:51], v[56:59], v[36:39], v[48:51]\n"
"v_mfma_f32_16x16x32_bf16 v[52:55], v[36:39], v[36:39], v[52:55]\n"
"v_mfma_f32_16x16x32_bf16 v[48:51], v[56:59], v[40:43], v[48:51]\n"
"v_mfma_f32_16x16x32_bf16 v[52:55], v[40:43], v[40:43], v[52:55]\n"
"v_mfma_f32_16x16x32_bf16 v[48:51], v[56:59], v[44:47], v[48:51]\n"
"v_mfma_f32_16x16x32_bf16 v[52:55], v[44:47], v[44:47], v[52:55]\n"
"v_mfma_f32_16x16x32_bf16 v[4:7], v[68:71], v[32:35], 0\n"
"v_mfma_f32_16x16x32_bf16 v[8:11], v[32:35], v[72:75], 0\n"
"v_mfma_f32_16x16x32_bf16 v[12:15], v[64:67], v[32:35], 0\n"
"s_nop 7\n"
"s_branch LP_155\n"
"LQ_156:\n"
"s_add_u32 s63, s63, 1\n"
"s_cmp_gt_u32 s63, 32768\n"
"s_cbranch_scc1 L_END\n"
"ds_read_b128 v[48:51], v3 offset:5632\n"
"ds_read_b128 v[52:55], v3 offset:5888\n"
"s_waitcnt lgkmcnt(0)\n"
"v_bitop3_b32 v63, 1, v48, v50 bitop3:0x80\n"
"v_bitop3_b32 v63, v63, v52, v54 bitop3:0x80\n"
"v_cmp_eq_u32 vcc, 0, v63\n"
"s_nop 1\n"
"s_cbranch_vccnz LQ_156\n"
"s_nop 7\n"
"s_branch LP_157\n"
"LQ_158:\n"
"s_add_u32 s63, s63, 1\n"
"s_cmp_gt_u32 s63, 32768\n"
"s_cbranch_scc1 L_END\n"
"s_add_u32 m0, s58, 0\n"
"s_add_u32 s69, s51, 0xcc0000\n"
"buffer_load_dwordx4 v0, s[12:15], s69 offen offset:0 sc1 lds\n"
"buffer_load_dwordx4 v0, s[12:15], s69 offen offset:1024 sc1 lds\n"
"buffer_load_dwordx4 v0, s[12:15], s69 offen offset:2048 sc1 lds\n"
"buffer_load_dwordx4 v0, s[12:15], s69 offen offset:3072 sc1 lds\n"
"s_waitcnt vmcnt(0) lgkmcnt(0)\n"
"ds_read_b128 v[32:35], v60 offset:0\n"
"ds_read_b128 v[36:39], v60 offset:1024\n"
"ds_read_b128 v[40:43], v60 offset:2048\n"
"ds_read_b128 v[44:47], v60 offset:3072\n"
"s_waitcnt vmcnt(0) lgkmcnt(0)\n"
"v_bitop3_b32 v63, 1, v32, v34 bitop3:0x10\n"
"v_bitop3_b32 v63, v63, v36, v38 bitop3:0x10\n"
"v_bitop3_b32 v63, v63, v40, v42 bitop3:0x10\n"
"v_bitop3_b32 v63, v63, v44, v46 bitop3:0x10\n"
"v_cmp_eq_u32 vcc, 0, v63\n"
"s_nop 1\n"
"s_cbranch_vccnz LQ_158\n"
"v_mfma_f32_16x16x32_bf16 v[48:51], v[56:59], v[32:35], 0\n"
"v_mfma_f32_16x16x32_bf16 v[52:55], v[32:35], v[32:35], 0\n"
"v_mfma_f32_16x16x32_bf16 v[48:51], v[56:59], v[36:39], v[48:51]\n"
"v_mfma_f32_16x16x32_bf16 v[52:55], v[36:39], v[36:39], v[52:55]\n"
"v_mfma_f32_16x16x32_bf16 v[48:51], v[56:59], v[40:43], v[48:51]\n"
"v_mfma_f32_16x16x32_bf16 v[52:55], v[40:43], v[40:43], v[52:55]\n"
"v_mfma_f32_16x16x32_bf16 v[48:51], v[56:59], v[44:47], v[48:51]\n"
"v_mfma_f32_16x16x32_bf16 v[52:55], v[44:47], v[44:47], v[52:55]\n"
"v_mfma_f32_16x16x32_bf16 v[24:27], v[68:71], v[32:35], 0\n"
"v_mfma_f32_16x16x32_bf16 v[20:23], v[32:35], v[72:75], 0\n"
"v_mfma_f32_16x16x32_bf16 v[28:31], v[64:67], v[32:35], 0\n"
"s_nop 7\n"
"s_branch LP_159\n"
"LQ_160:\n"
"s_add_u32 s63, s63, 1\n"
"s_cmp_gt_u32 s63, 32768\n"
"s_cbranch_scc1 L_END\n"
"ds_read_b128 v[48:51], v3 offset:5120\n"
"ds_read_b128 v[52:55], v3 offset:5376\n"
"s_waitcnt lgkmcnt(0)\n"
"v_bitop3_b32 v63, 1, v48, v50 bitop3:0x80\n"
"v_bitop3_b32 v63, v63, v52, v54 bitop3:0x80\n"
"v_cmp_eq_u32 vcc, 0, v63\n"
"s_nop 1\n"
"s_cbranch_vccnz LQ_160\n"
"s_nop 7\n"
"s_branch LP_161\n"
"LC_163:\n"
"s_add_u32 s63, s63, 1\n"
"s_cmp_gt_u32 s63, 32768\n"
"s_cbranch_scc1 L_END\n"
"s_add_u32 s69, s51, 0xacc000\n"
"v_bitop3_b32 v63, 1, v4, v6 bitop3:0x80\n"
"v_cmp_eq_u32 vcc, 0, v63\n"
"s_nop 1\n"
"s_cbranch_vccz Lz_164\n"
"buffer_load_dwordx4 v[4:7], v0, s[12:15], s69 offen offset:0 sc1\n"
"Lz_164:\n"
"v_bitop3_b32 v63, 1, v8, v10 bitop3:0x80\n"
"v_cmp_eq_u32 vcc, 0, v63\n"
"s_nop 1\n"
"s_cbranch_vccz Lz_165\n"
"buffer_load_dwordx4 v[8:11], v0, s[12:15], s69 offen offset:1024 sc1\n"
"Lz_165:\n"
"v_bitop3_b32 v63, 1, v12, v14 bitop3:0x80\n"
"v_cmp_eq_u32 vcc, 0, v63\n"
"s_nop 1\n"
"s_cbranch_vccz Lz_166\n"
"buffer_load_dwordx4 v[12:15], v0, s[12:15], s69 offen offset:2048 sc1\n"
"Lz_166:\n"
"v_bitop3_b32 v63, 1, v16, v18 bitop3:0x80\n"
"v_cmp_eq_u32 vcc, 0, v63\n"
"s_nop 1\n"
"s_cbranch_vccz Lz_167\n"
"buffer_load_dwordx4 v[16:19], v0, s[12:15], s69 offen offset:3072 sc1\n"
"Lz_167:\n"
"s_waitcnt vmcnt(0) lgkmcnt(0)\n"
"v_bitop3_b32 v63, 1, v4, v6 bitop3:0x80\n"
"v_bitop3_b32 v63, v63, v8, v10 bitop3:0x80\n"
"v_bitop3_b32 v63, v63, v12, v14 bitop3:0x80\n"
"v_bitop3_b32 v63, v63, v16, v18 bitop3:0x80\n"
"v_cmp_eq_u32 vcc, 0, v63\n"
"s_nop 1\n"
"s_cbranch_vccnz LC_163\n"
"s_nop 7\n"
"v_xor_b32 v61, 64, v61\n"
"ds_read_b64_tr_b16 v[32:33], v61 offset:0\n"
"ds_read_b64_tr_b16 v[34:35], v61 offset:512\n"
"ds_read_b64_tr_b16 v[36:37], v61 offset:4096\n"
"ds_read_b64_tr_b16 v[38:39], v61 offset:4608\n"
"ds_read_b64_tr_b16 v[40:41], v61 offset:8192\n"
"ds_read_b64_tr_b16 v[42:43], v61 offset:8704\n"
"ds_read_b64_tr_b16 v[44:45], v61 offset:12288\n"
"ds_read_b64_tr_b16 v[46:47], v61 offset:12800\n"
"v_xor_b32 v61, 96, v61\n"
"ds_read_b64_tr_b16 v[48:49], v61 offset:0\n"
"ds_read_b64_tr_b16 v[50:51], v61 offset:512\n"
"s_waitcnt vmcnt(0) lgkmcnt(0)\n"
"s_branch LB_162\n"
"LC_169:\n"
"s_add_u32 s63, s63, 1\n"
"s_cmp_gt_u32 s63, 32768\n"
"s_cbranch_scc1 L_END\n"
"s_add_u32 m0, s58, 16384\n"
"s_add_u32 s69, s51, 0xac8000\n"
"buffer_load_dwordx4 v0, s[12:15], s69 offen offset:0 sc1 lds\n"
"buffer_load_dwordx4 v0, s[12:15], s69 offen offset:1024 sc1 lds\n"
"buffer_load_dwordx4 v0, s[12:15], s69 offen offset:2048 sc1 lds\n"
"buffer_load_dwordx4 v0, s[12:15], s69 offen offset:3072 sc1 lds\n"
"s_waitcnt vmcnt(0) lgkmcnt(0)\n"
"ds_read_b128 v[4:7], v60 offset:16384\n"
"ds_read_b128 v[8:11], v60 offset:17408\n"
"ds_read_b128 v[12:15], v60 offset:18432\n"
"ds_read_b128 v[16:19], v60 offset:19456\n"
"s_waitcnt vmcnt(0) lgkmcnt(0)\n"
"v_bitop3_b32 v63, 1, v4, v6 bitop3:0x80\n"
"v_bitop3_b32 v63, v63, v8, v10 bitop3:0x80\n"
"v_bitop3_b32 v63, v63, v12, v14 bitop3:0x80\n"
"v_bitop3_b32 v63, v63, v16, v18 bitop3:0x80\n"
"v_cmp_eq_u32 vcc, 0, v63\n"
"s_nop 1\n"
"s_cbranch_vccnz LC_169\n"
"s_nop 7\n"
"v_xor_b32 v61, 64, v61\n"
"ds_read_b64_tr_b16 v[32:33], v61 offset:0\n"
"ds_read_b64_tr_b16 v[34:35], v61 offset:512\n"
"ds_read_b64_tr_b16 v[36:37], v61 offset:4096\n"
"ds_read_b64_tr_b16 v[38:39], v61 offset:4608\n"
"ds_read_b64_tr_b16 v[40:41], v61 offset:8192\n"
"ds_read_b64_tr_b16 v[42:43], v61 offset:8704\n"
"ds_read_b64_tr_b16 v[44:45], v61 offset:12288\n"
"ds_read_b64_tr_b16 v[46:47], v61 offset:12800\n"
"v_xor_b32 v61, 96, v61\n"
"ds_read_b64_tr_b16 v[48:49], v61 offset:0\n"
"ds_read_b64_tr_b16 v[50:51], v61 offset:512\n"
"s_waitcnt vmcnt(0) lgkmcnt(0)\n"
"s_branch LB_168\n"
"LC_171:\n"
"s_add_u32 s63, s63, 1\n"
"s_cmp_gt_u32 s63, 32768\n"
"s_cbranch_scc1 L_END\n"
"s_add_u32 m0, s58, 49152\n"
"s_add_u32 s69, s51, 0xac4000\n"
"buffer_load_dwordx4 v0, s[12:15], s69 offen offset:0 sc1 lds\n"
"buffer_load_dwordx4 v0, s[12:15], s69 offen offset:1024 sc1 lds\n"
"buffer_load_dwordx4 v0, s[12:15], s69 offen offset:2048 sc1 lds\n"
"buffer_load_dwordx4 v0, s[12:15], s69 offen offset:3072 sc1 lds\n"
"s_waitcnt vmcnt(0) lgkmcnt(0)\n"
"ds_read_b128 v[4:7], v60 offset:49152\n"
"ds_read_b128 v[8:11], v60 offset:50176\n"
"ds_read_b128 v[12:15], v60 offset:51200\n"
"ds_read_b128 v[16:19], v60 offset:52224\n"
"s_waitcnt vmcnt(0) lgkmcnt(0)\n"
"v_bitop3_b32 v63, 1, v4, v6 bitop3:0x80\n"
"v_bitop3_b32 v63, v63, v8, v10 bitop3:0x80\n"
"v_bitop3_b32 v63, v63, v12, v14 bitop3:0x80\n"
"v_bitop3_b32 v63, v63, v16, v18 bitop3:0x80\n"
"v_cmp_eq_u32 vcc, 0, v63\n"
"s_nop 1\n"
"s_cbranch_vccnz LC_171\n"
"s_nop 7\n"
"v_xor_b32 v61, 64, v61\n"
"ds_read_b64_tr_b16 v[32:33], v61 offset:0\n"
"ds_read_b64_tr_b16 v[34:35], v61 offset:512\n"
"ds_read_b64_tr_b16 v[36:37], v61 offset:4096\n"
"ds_read_b64_tr_b16 v[38:39], v61 offset:4608\n"
"ds_read_b64_tr_b16 v[40:41], v61 offset:8192\n"
"ds_read_b64_tr_b16 v[42:43], v61 offset:8704\n"
"ds_read_b64_tr_b16 v[44:45], v61 offset:12288\n"
"ds_read_b64_tr_b16 v[46:47], v61 offset:12800\n"
"v_xor_b32 v61, 96, v61\n"
"ds_read_b64_tr_b16 v[48:49], v61 offset:0\n"
"ds_read_b64_tr_b16 v[50:51], v61 offset:512\n"
"s_waitcnt vmcnt(0) lgkmcnt(0)\n"
"s_branch LB_170\n"
"L_END:\n"
  :: "s"(__builtin_amdgcn_kernarg_segment_ptr()), "s"(blockIdx.x), "v"(threadIdx.x),
     "s"((unsigned)(size_t)(__attribute__((address_space(3))) char*)smem)
  : "v1", "v2", "v3", "v4", "v5", "v6", "v7", "v8", "v9", "v10", "v11", "v12", "v13", "v14", "v15", "v16",
"v17", "v18", "v19", "v20", "v21", "v22", "v23", "v24", "v25", "v26", "v27", "v28", "v29", "v30", "v31", "v32",
"v33", "v34", "v35", "v36", "v37", "v38", "v39", "v40", "v41", "v42", "v43", "v44", "v45", "v46", "v47", "v48",
"v49", "v50", "v51", "v52", "v53", "v54", "v55", "v56", "v57", "v58", "v59", "v60", "v61", "v62", "v63", "v64",
"v65", "v66", "v67", "v68", "v69", "v70", "v71", "v72", "v73", "v74", "v75", "v76", "v77", "v78", "v79", "v80",
"v81", "v82", "v83", "v84", "v85", "v86", "v87", "v88", "v89", "v90", "v91", "v92", "v93", "v94", "v95", "v96",
"v97", "v98", "v99", "v100", "v101", "v102", "v103", "v104", "v105", "v106", "v107", "v108", "v109", "v110", "v111", "v112",
"v113", "v114", "v115", "v116", "v117", "v118", "v119", "v120", "v121", "v122", "v123", "v124", "v125", "v126", "v127", "v128",
"v129", "v130", "v131", "v132", "v133", "v134", "v135", "v136", "v137", "v138", "v139", "v140", "v141", "v142", "v143", "v144",
"v145", "v146", "v147", "v148", "v149", "v150", "v151", "v152", "v153", "v154", "v155", "v156", "v157", "v158", "v159", "v160",
"v161", "v162", "v163", "v164", "v165", "v166", "v167", "v168", "v169", "v170", "v171", "v172", "v173", "v174", "v175", "v176",
"v177", "v178", "v179", "v180", "v181", "v182", "v183", "v184", "v185", "v186", "v187", "v188", "v189", "v190", "v191", "v192",
"v193", "v194", "v195", "v196", "v197", "v198", "v199", "v200", "v201", "v202", "v203", "v204", "v205", "v206", "v207", "v208",
"v209", "v210", "v211", "v212", "v213", "v214", "v215", "v216", "v217", "v218", "v219", "v220", "v221", "v222", "v223", "v224",
"v225", "v226", "v227", "v228", "v229", "v230", "v231", "v232", "v233", "v234", "v235", "v236", "v237", "v238", "v239", "v240",
"v241", "v242", "v243", "v244", "v245", "v246", "v247", "v248", "v249", "v250", "v251", "v252", "v253", "v254", "v255", "s4",
"s5", "s6", "s7", "s8", "s9", "s10", "s11", "s12", "s13", "s14", "s15", "s16", "s17", "s18", "s19", "s20",
"s21", "s22", "s23", "s24", "s25", "s26", "s27", "s28", "s29", "s30", "s31",
"s34", "s35", "s36", "s37", "s38", "s39", "s40", "s41", "s42", "s43", "s44", "s45", "s46", "s47", "s48", "s49", "s50", "s51", "s52", "s53", "s54", "s55", "s56", "s57", "s58", "s59", "s60", "s61", "s62", "s63", "s64", "s65", "s66", "s67", "s68", "s69", "s70", "s71", "s72", "s73", "s74", "s75", "s76", "s77", "s78", "s79", "s80", "s81", "s82", "s83", "s84", "s85", "s86", "s87", "s88", "s89", "s90", "s91", "s92", "s93", "s94", "s95", "s96", "s97", "s98", "s99", "vcc", "scc", "memory");
}

extern "C" void kernel_launch(void* const* d_in, const int* in_sizes, int n_in, void* d_out, int out_size, void* d_ws, size_t ws_size,
                              hipStream_t stream) {
  swa4_kernel<<<256, 512, 0, stream>>>(d_in[0], d_in[1], d_in[2], d_in[4], d_in[5], d_out, d_ws);
}
```
